```python
import torch

def _softmax(x, axis=-1):
    return torch.softmax(x, dim=axis)
def _gelu(x):
    return torch.nn.functional.gelu(x, approximate="tanh")
def _silu(x):
    return torch.nn.functional.silu(x)
def _sigmoid(x):
    return torch.sigmoid(x)
def _layernorm(x, axis=-1, eps=1e-5):
    m = x.mean(dim=axis, keepdim=True)
    v = x.var(dim=axis, keepdim=True, unbiased=False)
    return (x - m) / torch.sqrt(v + eps)
def _scatter_add(tgt, idx, src, axis=0):
    return tgt.clone().index_add_(axis, idx, src)
def _segment_sum(src, seg, n):
    out = torch.zeros((n,) + src.shape[1:], dtype=src.dtype)
    return out.index_add_(0, seg, src)
def _pad(x, pw):
    flat = tuple(v for pair in reversed(pw) for v in pair)
    return torch.nn.functional.pad(x, flat)

kernel_name = 'sequential_preLN__sliding_local_dilated__glu_gated_glu__prompt'

def setup_inputs():

    _w = (lambda a: (lambda t: t.to(torch.bfloat16) if t.is_floating_point() else t)(torch.as_tensor(a)))
    def _A_setup():
        B, S, D = 1, 2048, 576
        return {
            "x": _w((lambda *s: torch.randn(*s, dtype=torch.bfloat16) * 0.05)(B, S, D)),
            "wqkv": _w((lambda *s: torch.randn(*s, dtype=torch.bfloat16) * 0.05)(D, 3 * D)),
            "wo": _w((lambda *s: torch.randn(*s, dtype=torch.bfloat16) * 0.05)(D, D)),
        }
    _A_in = _A_setup()

    def _B_setup():
        B, S, D, F = 1, 2048, 576, 4608
        return {
            "x": _w((lambda *s: torch.randn(*s, dtype=torch.bfloat16) * 0.05)(B, S, D)),
            "w_gate": _w((lambda *s: torch.randn(*s, dtype=torch.bfloat16) * 0.05)(D, F)),
            "w_up": _w((lambda *s: torch.randn(*s, dtype=torch.bfloat16) * 0.05)(D, F)),
            "w_down": _w((lambda *s: torch.randn(*s, dtype=torch.bfloat16) * 0.05)(F, D)),
        }
    _B_in = _B_setup()

    return {
        "A_x": _A_in["x"],
        "A_wqkv": _A_in["wqkv"],
        "A_wo": _A_in["wo"],
        "B_x": _B_in["x"],
        "B_w_gate": _B_in["w_gate"],
        "B_w_up": _B_in["w_up"],
        "B_w_down": _B_in["w_down"],
    }

def reference(A_x, A_wqkv, A_wo, B_x, B_w_gate, B_w_up, B_w_down):
    def _A_ref(x, wqkv, wo):
        B, S, D = x.shape
        H = 32; Dh = D // H
        W, dil = 8, 2
        qkv = (lambda x, *d: x.permute(*d))((x @ wqkv).reshape(B, S, 3, H, Dh), 2, 0, 3, 1, 4)
        q, k, v = qkv[0], qkv[1], qkv[2]
        scale = Dh ** -0.5
        pos = torch.arange(S)
        off = torch.arange(W) * dil
        idx = pos[:, None] - off[None, :]
        mask = idx >= 0
        idx_c = torch.where(mask, idx, 0).reshape(-1)
        kg = k[:, :, idx_c, :].reshape(B, H, S, W, Dh)
        vg = v[:, :, idx_c, :].reshape(B, H, S, W, Dh)
        logits = torch.einsum("bhsd,bhswd->bhsw", q, kg) * scale
        logits = torch.where(mask[None, None, :, :], logits, -1e9)
        attn = _softmax(logits, axis=-1)
        o = torch.einsum("bhsw,bhswd->bhsd", attn, vg)
        return (lambda x, *d: x.permute(*d))(o, 0, 2, 1, 3).reshape(B, S, D) @ wo

    def _B_ref(x, w_gate, w_up, w_down):
        g = torch.einsum("bsd,df->bsf", x, w_gate)
        u = torch.einsum("bsd,df->bsf", x, w_up)
        h = _sigmoid(g) * u
        return torch.einsum("bsf,fd->bsd", h, w_down)

    x = A_x
    h = x + _A_ref(_layernorm(x, axis=-1), A_wqkv, A_wo)
    h2 = h + _B_ref(_layernorm(h, axis=-1), B_w_gate, B_w_up, B_w_down)
    return h2
```

```cpp
#include <hip/hip_runtime.h>
#include <hip/hip_bf16.h>
typedef unsigned short u16;
__device__ __forceinline__ float bf2f(u16 b) { return __uint_as_float(((unsigned)b) << 16); }
__device__ __forceinline__ u16 f2bf(float f) { unsigned u = __float_as_uint(f); u += 0x7FFFu + ((u >> 16) & 1u); return (u16)(u >> 16); }
constexpr int S = 2048, D = 576, F = 4608, H = 32, DH = 18;

__global__ void n_ln(const u16* x, u16* y) {
  int row = blockIdx.x, t = threadIdx.x;
  __shared__ float red[64];
  float s = 0;
  for (int i = t; i < D; i += 64) s += bf2f(x[row * D + i]);
  for (int o = 32; o; o >>= 1) s += __shfl_xor(s, o);
  float m = s / D;
  float q = 0;
  for (int i = t; i < D; i += 64) { float d = bf2f(x[row * D + i]) - m; q += d * d; }
  for (int o = 32; o; o >>= 1) q += __shfl_xor(q, o);
  float r = rsqrtf(q / D + 1e-5f);
  for (int i = t; i < D; i += 64) y[row * D + i] = f2bf((bf2f(x[row * D + i]) - m) * r);
}
__global__ void n_gemm(const u16* A, const u16* B, u16* C, const u16* res, int M, int N, int K) {
  int n = blockIdx.x * 64 + threadIdx.x, m = blockIdx.y;
  if (n >= N) return;
  float s = 0;
  for (int k = 0; k < K; ++k) s += bf2f(A[(size_t)m * K + k]) * bf2f(B[(size_t)k * N + n]);
  if (res) s = bf2f(f2bf(s)) + bf2f(res[(size_t)m * N + n]);
  C[(size_t)m * N + n] = f2bf(s);
}
__global__ void n_attn(const u16* qkv, u16* o) {
  int s = blockIdx.x, h = threadIdx.x;
  if (h >= H) return;
  float lg[8]; float mx = -1e30f;
  for (int w = 0; w < 8; ++w) {
    int idx = s - 2 * w; bool ok = idx >= 0; if (!ok) idx = 0;
    float a = 0;
    for (int d = 0; d < DH; ++d) a += bf2f(qkv[(size_t)s * 3 * D + h * DH + d]) * bf2f(qkv[(size_t)idx * 3 * D + D + h * DH + d]);
    a *= 0.23570226039551584f;
    lg[w] = ok ? a : -1e9f; mx = fmaxf(mx, lg[w]);
  }
  float sum = 0;
  for (int w = 0; w < 8; ++w) { lg[w] = __expf(lg[w] - mx); sum += lg[w]; }
  for (int d = 0; d < DH; ++d) {
    float a = 0;
    for (int w = 0; w < 8; ++w) { int idx = s - 2 * w; if (idx < 0) idx = 0; a += lg[w] * bf2f(qkv[(size_t)idx * 3 * D + 2 * D + h * DH + d]); }
    o[(size_t)s * D + h * DH + d] = f2bf(a / sum);
  }
}
__global__ void n_act(const u16* g, const u16* u, u16* a, int n) {
  int i = blockIdx.x * 256 + threadIdx.x; if (i >= n) return;
  float gv = bf2f(g[i]); float sg = bf2f(f2bf(1.f / (1.f + __expf(-gv))));
  a[i] = f2bf(sg * bf2f(u[i]));
}
extern "C" void kernel_launch(void* const* d_in, const int* in_sizes, int n_in, void* d_out, int out_size, void* d_ws, size_t ws_size, hipStream_t stream) {
  const u16* x = (const u16*)d_in[0]; const u16* wqkv = (const u16*)d_in[1]; const u16* wo = (const u16*)d_in[2];
  const u16* wg = (const u16*)d_in[4]; const u16* wu = (const u16*)d_in[5]; const u16* wd = (const u16*)d_in[6];
  u16* ws = (u16*)d_ws;
  u16* xn = ws; u16* qkv = xn + S * D; u16* o = qkv + S * 3 * D; u16* h = o + S * D; u16* hn = h + S * D;
  u16* g = hn + S * D; u16* u = g + (size_t)S * F; u16* a = u + (size_t)S * F;
  n_ln<<<S, 64, 0, stream>>>(x, xn);
  n_gemm<<<dim3(3 * D / 64, S), 64, 0, stream>>>(xn, wqkv, qkv, nullptr, S, 3 * D, D);
  n_attn<<<S, 64, 0, stream>>>(qkv, o);
  n_gemm<<<dim3(D / 64, S), 64, 0, stream>>>(o, wo, h, x, S, D, D);
  n_ln<<<S, 64, 0, stream>>>(h, hn);
  n_gemm<<<dim3(F / 64, S), 64, 0, stream>>>(hn, wg, g, nullptr, S, F, D);
  n_gemm<<<dim3(F / 64, S), 64, 0, stream>>>(hn, wu, u, nullptr, S, F, D);
  n_act<<<S * F / 256, 256, 0, stream>>>(g, u, a, S * F);
  n_gemm<<<dim3(D / 64, S), 64, 0, stream>>>(a, wd, (u16*)d_out, h, S, D, F);
}
```

```cpp
#include <hip/hip_runtime.h>
typedef unsigned short u16;
typedef unsigned int u32;
typedef __attribute__((ext_vector_type(8))) __bf16 bf16x8;
typedef __attribute__((ext_vector_type(2))) __bf16 bf16x2;
typedef __attribute__((ext_vector_type(4))) short s16x4;
typedef __attribute__((ext_vector_type(8))) short s16x8;
typedef __attribute__((ext_vector_type(4))) float f32x4;
typedef __attribute__((ext_vector_type(2))) float f32x2;
typedef __attribute__((ext_vector_type(4))) u32 u32x4;
typedef __attribute__((ext_vector_type(2))) u32 u32x2;
typedef __attribute__((ext_vector_type(8))) u32 u32x8;
typedef __attribute__((ext_vector_type(16))) u32 u32x16;
typedef __attribute__((ext_vector_type(16))) float f32x16;
#define LDSP(T, a) ((__attribute__((address_space(3))) T*)(a))
#define DEV __device__ __forceinline__
typedef __amdgpu_buffer_rsrc_t rsrc_t;

namespace cfg {
constexpr int S = 2048, D = 576, F = 4608;
constexpr u32 FST = 4352;
constexpr size_t WS_FLAGS = 122880;
constexpr size_t WS_STATS = 0;
constexpr size_t WS_MEAN = WS_STATS + 65536;
constexpr size_t WS_WBAR = WS_MEAN + 8192;
constexpr size_t WS_HFRAG = 262144;
constexpr size_t WS_OFRAG = WS_HFRAG + 2359296;
constexpr size_t WS_ACT = WS_OFRAG + 2359296;
constexpr size_t WS_SLAB = WS_ACT + 18874368;
constexpr size_t WS_FL2 = WS_SLAB + 7602176;
constexpr size_t WS_DBG = WS_SLAB + 8388608;
constexpr size_t WS_TMP = WS_DBG + 4194304;
constexpr size_t WS_WGT = WS_ACT;
}

DEV rsrc_t mk_rsrc(const void* p, u32 bytes) { return __builtin_amdgcn_make_buffer_rsrc((void*)p, 0, bytes, 0x00020000); }
DEV void dma16(u32 m0v, u32 voff, rsrc_t srd, u32 soff) {
  asm volatile("s_mov_b32 m0, %0\n\ts_nop 0\n\tbuffer_load_dwordx4 %1, %2, %3 offen lds" ::"s"(m0v), "v"(voff), "s"(srd), "s"(soff) : "memory");
}
DEV void dma16h(u32 m0v, u32 voff, rsrc_t srd, u32 soff) {
  asm volatile("s_mov_b32 m0, %0\n\ts_mov_b32 exec_hi, 0\n\tbuffer_load_dwordx4 %1, %2, %3 offen lds\n\ts_mov_b32 exec_hi, -1" ::"s"(m0v), "v"(voff), "s"(srd), "s"(soff) : "memory");
}
#define BLD16(dst, voff, srd, soff, IMM) asm volatile("buffer_load_dwordx4 %0, %1, %2, %3 offen offset:" #IMM : "=v"(dst) : "v"(voff), "s"(srd), "s"(soff) : "memory")
#define BLD16C(dst, voff, srd, soff, IMM) asm volatile("buffer_load_dwordx4 %0, %1, %2, %3 offen offset:" #IMM " sc1" : "=v"(dst) : "v"(voff), "s"(srd), "s"(soff) : "memory")
#define BLD8(dst, voff, srd, soff, IMM) asm volatile("buffer_load_dwordx2 %0, %1, %2, %3 offen offset:" #IMM : "=v"(dst) : "v"(voff), "s"(srd), "s"(soff) : "memory")
#define BLD8C(dst, voff, srd, soff, IMM) asm volatile("buffer_load_dwordx2 %0, %1, %2, %3 offen offset:" #IMM " sc1" : "=v"(dst) : "v"(voff), "s"(srd), "s"(soff) : "memory")
#define BLD4(dst, voff, srd, soff, IMM) asm volatile("buffer_load_dword %0, %1, %2, %3 offen offset:" #IMM : "=v"(dst) : "v"(voff), "s"(srd), "s"(soff) : "memory")
#define BST16(src, voff, srd, soff, IMM, AUX) asm volatile("buffer_store_dwordx4 %0, %1, %2, %3 offen offset:" #IMM " " AUX "\n\ts_nop 0" ::"v"(src), "v"(voff), "s"(srd), "s"(soff) : "memory")
#define BST8(src, voff, srd, soff, IMM, AUX) asm volatile("buffer_store_dwordx2 %0, %1, %2, %3 offen offset:" #IMM " " AUX ::"v"(src), "v"(voff), "s"(srd), "s"(soff) : "memory")
#define VMW(N) asm volatile("s_waitcnt vmcnt(" #N ")" ::: "memory")
#define LGW() __builtin_amdgcn_s_waitcnt(0xc07f)
#define BAR() asm volatile("s_barrier" ::: "memory")
#define SB() __builtin_amdgcn_sched_barrier(0)
#define PIN(a) asm volatile("" : "+v"(a))
#define NOP(N) asm volatile("s_nop " #N)
#define MF0(D, A, B) asm volatile("v_mfma_f32_16x16x32_bf16 %0, %1, %2, 0" : "=&v"(D) : "v"(A), "v"(B))
#define MF(D, A, B) asm volatile("v_mfma_f32_16x16x32_bf16 %0, %1, %2, %0" : "+v"(D) : "v"(A), "v"(B))
#define VADD(r, s) asm volatile("v_add_u32 %0, %1, %0" : "+v"(r) : "s"((u32)(s)))
#define EOP1(op, V, j) { float t_ = V[j]; asm volatile(op " %0, %0" : "+v"(t_)); V[j] = t_; }
#define EMUL(V, j, a) { float t_ = V[j]; asm volatile("v_mul_f32 %0, %1, %0" : "+v"(t_) : "v"(a)); V[j] = t_; }
#define EADD(V, j, a) { float t_ = V[j]; asm volatile("v_add_f32 %0, %1, %0" : "+v"(t_) : "v"(a)); V[j] = t_; }
#define EFMA(V, j, a) { float t_ = V[j]; asm volatile("v_fma_f32 %0, %0, %1, %1" : "+v"(t_) : "v"(a)); V[j] = t_; }
#define ECVT(P, i, a, b) { u32 t_; asm volatile("v_cvt_pk_bf16_f32 %0, %1, %2" : "=v"(t_) : "v"(a), "v"(b)); P[i] = t_; }
#define EMIN(P, i) { u32 t_ = P[i]; asm volatile("v_min_u32 %0, -2, %0" : "+v"(t_)); P[i] = t_; }
#define LDR(dst, a, IMM) asm volatile("ds_read_b128 %0, %1 offset:" #IMM : "=v"(dst) : "v"(a) : "memory")
#define LGN(N) asm volatile("s_waitcnt lgkmcnt(" #N ")" ::: "memory")
DEV bf16x8 trfrag(u32 a0, u32 a1) {
  s16x4 lo = __builtin_amdgcn_ds_read_tr16_b64_v4i16(LDSP(s16x4, a0));
  s16x4 hi = __builtin_amdgcn_ds_read_tr16_b64_v4i16(LDSP(s16x4, a1));
  s16x8 r = __builtin_shufflevector(lo, hi, 0, 1, 2, 3, 4, 5, 6, 7);
  return __builtin_bit_cast(bf16x8, r);
}
DEV u32 pk2(float a, float b) {
  f32x2 v = {a, b};
  bf16x2 r = __builtin_convertvector(v, bf16x2);
  return __builtin_bit_cast(u32, r);
}
DEV float xsum(float v) {
  u32 a = __float_as_uint(v);
  auto r = __builtin_amdgcn_permlane16_swap(a, a, false, false);
  a = __float_as_uint(__uint_as_float(r[0]) + __uint_as_float(r[1]));
  r = __builtin_amdgcn_permlane32_swap(a, a, false, false);
  return __uint_as_float(r[0]) + __uint_as_float(r[1]);
}
DEV float xmax(float v) {
  u32 a = __float_as_uint(v);
  auto r = __builtin_amdgcn_permlane16_swap(a, a, false, false);
  a = __float_as_uint(fmaxf(__uint_as_float(r[0]), __uint_as_float(r[1])));
  r = __builtin_amdgcn_permlane32_swap(a, a, false, false);
  return fmaxf(__uint_as_float(r[0]), __uint_as_float(r[1]));
}
DEV float dot2(u32 a, u32 b, float c) { return __builtin_amdgcn_fdot2_f32_bf16(__builtin_bit_cast(bf16x2, a), __builtin_bit_cast(bf16x2, b), c, false); }
DEV u32 wlane(u32 s, int i, u32 v) { asm("s_nop 1\n\tv_writelane_b32 %0, %1, %2" : "+v"(v) : "s"(__builtin_amdgcn_readfirstlane(s)), "n"(i)); return v; }
DEV float bf_lo(u32 v) { return __uint_as_float(v << 16); }
DEV float bf_hi(u32 v) { return __uint_as_float(v & 0xffff0000u); }
#define STAMP(i, p)
#define STAMPF(p)
#define STAMP0()
#define STAMP_DECL
#define KBK 1

namespace kf {
constexpr u32 LDS_BYTES = 163840, BOX = 29696;
DEV u32 lo32(const void* p) { return (u32)(size_t)p; }
DEV u32 hi32(const void* p) { return (u32)((size_t)p >> 32); }
#define DMA(MB, MI, VO, SRD, SO, SI) { u32 t_; asm volatile("s_add_u32 m0, %1, %2\n\ts_add_u32 %0, %3, %4\n\tbuffer_load_dwordx4 %5, %6, %0 offen lds" : "=&s"(t_) : "s"(MB), "i"(MI), "s"(SO), "i"(SI), "v"(VO), "s"(SRD) : "memory", "scc"); }
#define DMAI(MI, VO, SRD, SO) asm volatile("s_mov_b32 m0, %0\n\ts_nop 0\n\tbuffer_load_dwordx4 %1, %2, %3 offen lds" :: "i"(MI), "v"(VO), "s"(SRD), "s"(SO) : "memory")
#define DMAX(MB, MI, VO, SRD, SO) asm volatile("s_add_u32 m0, %0, %1\n\ts_nop 0\n\tbuffer_load_dwordx4 %3, %4, %2 offen lds" :: "s"(MB), "i"(MI), "s"(SO), "v"(VO), "s"(SRD) : "memory", "scc")
#define KNEXT(KN, SW, K) asm volatile("s_mul_i32 %1, %2, 1728\n\ts_add_u32 %0, %2, 128\n\ts_add_u32 %1, %1, %3\n\ts_cmpk_lg_u32 %0, 1152\n\ts_cselect_b32 %0, %0, 0" : "=&s"(KN), "=&s"(SW) : "s"(K), "s"(hb) : "scc")
#define FD0(D, C, M) asm volatile("v_fmac_f32_dpp %0, %0, %1 row_newbcast:0 row_mask:0xf bank_mask:0xf" : "+v"(D) : "v"(M))
#define FD1(D, C, M) asm volatile("v_fmac_f32_dpp %0, %1, %2 row_newbcast:0 row_mask:0xf bank_mask:0xf" : "+v"(D) : "v"(C), "v"(M))
#define EPI0(FD, A, C, S, AD, OF) { float e_[4]; for (int r = 0; r < 4; ++r) { e_[r] = A[r]; FD(e_[r], C[r], S[1]); } for (int r = 0; r < 4; ++r) asm volatile("v_mul_f32 %0, %1, %0" : "+v"(e_[r]) : "v"(S[0])); \
  *LDSP(u32x2, AD + OF) = u32x2{pk2(e_[0], e_[1]), pk2(e_[2], e_[3])}; }
#define EPI1(FD, A, C, S, AD, OF) { float e_[4]; for (int r = 0; r < 4; ++r) { e_[r] = A[r]; FD(e_[r], C[r], S[1]); } const f32x4 v_ = f32x4{e_[0], e_[1], e_[2], e_[3]} * S[0]; \
  *LDSP(u32x2, AD + OF) = u32x2{pk2(v_[0], v_[1]), pk2(v_[2], v_[3])}; }
#define DMAM(MI, VO, SRD, SO) asm volatile("s_mov_b32 m0, %0\n\ts_mov_b32 exec_lo, 0xffffff00\n\tbuffer_load_dwordx4 %1, %2, %3 offen lds\n\ts_mov_b32 exec_lo, -1" :: "i"(MI), "v"(VO), "s"(SRD), "s"(SO) : "memory")
#define DMAZ(MB, VO, SRD, SO, EX) asm volatile("s_mov_b32 m0, %0\n\ts_mov_b64 exec, %4\n\tbuffer_load_dwordx4 %1, %2, %3 offen lds\n\ts_mov_b64 exec, -1" :: "s"(MB), "v"(VO), "s"(SRD), "s"(SO), "s"(EX) : "memory")
#define DMT(OF, VO, SRD, SO) asm volatile("s_mov_b32 m0, 9216\n\ts_nop 0\n\tbuffer_load_dwordx4 %0, %1, %2 offen offset:" #OF " sc1 lds" :: "v"(VO), "s"(SRD), "s"(SO) : "memory")
#define TCH(K) asm volatile("s_cmp_eq_u32 %1, " #K "\n\ts_cbranch_scc0 1f\n\tbuffer_load_dword v252, v253, %2, 0 offen\n\tbuffer_load_dword v252, %3, %6, %7 offen\n\tbuffer_load_dword v252, %4, %6, 0 offen\n\tbuffer_load_dword v252, %5, %6, 0 offen\n1:" \
  : "+{v[252:253]}"(vt) : "s"(tsl), "s"(srdG), "v"(vp[5]), "v"(vp[6]), "v"(vp[4]), "s"(srdS), "s"(se[2]) : "memory", "scc")
#define TCB(K) asm volatile("s_cmp_eq_u32 %1, " #K "\n\ts_cbranch_scc0 1f\n\ts_mov_b32 exec_hi, 15\n\tbuffer_load_dword v252, v253, %2, 0 offen offset:1024\n\ts_mov_b32 exec_hi, -1\n1:" \
  : "+{v[252:253]}"(vt) : "s"(tsb), "s"(srdG) : "memory", "scc")
#define BLV(voff, srd, soff) asm volatile("buffer_load_dword v252, %1, %2, %3 offen" : "+{v[252:253]}"(vt) : "v"(voff), "s"(srd), "s"(soff) : "memory")
#define BLW() asm volatile("buffer_load_dword v252, v253, %1, 0 offen" : "+{v[252:253]}"(vt) : "s"(srdG) : "memory")
#define KARG __builtin_amdgcn_kernarg_segment_ptr()
#define RFL(a) __builtin_amdgcn_readfirstlane(a)
#define M24(a, b) __umul24(a, b)
#define BLD4C(dst, voff, srd, soff, IMM) asm volatile("buffer_load_dword %0, %1, %2, %3 offen offset:" #IMM " sc1" : "=v"(dst) : "v"(voff), "s"(srd), "s"(soff) : "memory")
#define BST4C(src, voff, srd, IMM) asm volatile("buffer_store_dword %0, %1, %2, 0 offen offset:" #IMM " sc1" ::"v"(src), "v"(voff), "s"(srd) : "memory")
#define BLT(dst, voff, srd, soff, IMM) asm volatile("buffer_load_dword %0, %1, %2, %3 offen offset:" #IMM : "+v"(dst) : "v"(voff), "s"(srd), "s"(soff) : "memory")
#define MFB(A, B, C) __builtin_amdgcn_mfma_f32_16x16x32_bf16(A, B, C, 0, 0, 0)
#define DPPF(v, c) __int_as_float(__builtin_amdgcn_update_dpp(0, __float_as_int(v), c, 0xf, 0xf, false))
#define MA0(D, A, B) asm volatile("v_mfma_f32_16x16x32_bf16 %0, %1, %2, 0" : "=&v"(D) : "v"(A), "a"(B))
#define MA1(D, A, B) asm volatile("v_mfma_f32_16x16x32_bf16 %0, %1, %2, %0" : "+v"(D) : "v"(A), "a"(B))
#define MX0(D, B) asm volatile("v_mfma_f32_16x16x32_bf16 %0, %1, %1, 0" : "=&v"(D) : "a"(B))
#define MX1(D, B) asm volatile("v_mfma_f32_16x16x32_bf16 %0, %1, %1, %0" : "+v"(D) : "a"(B))
#define OOB(c) ((c) ? 0u : 0x80000000u)
#define PINS(a) asm volatile("" : "+s"(a))
DEV u32 wrow(u32 c) { return M24(c, 7282) >> 16; }
DEV u32 msub(u32 c, u32 r, int k) { return (u32)(__mul24((int)r, -k) + (int)c); }
DEV u32 wpiece(u32 c) { const u32 r = wrow(c); return M24((r & 35) + ((r >> 2) & 3) * 8 + ((r >> 4) & 1) * 4, 3456) + msub(c, r, 9) * 16; }
DEV u32 opiece(u32 c) { const u32 r = wrow(c); return M24(r + (r >> 4) * 4 - (r >> 5) * 4, 1152) + msub(c, r, 9) * 16; }
DEV u32 lpiece(u32 c) { const u32 r = wrow(c); return M24(M24(36, (r >> 5) * 8 + ((r >> 2) & 3) * 2 + ((r >> 4) & 1)) + 16 + (r & 3), 1152) + msub(c, r, 9) * 16; }
DEV u32 xpiece(u32 j, u32 l, u32 tt) {
  const int row = (int)(tt * 64 + j * 8 + (l >> 3)) - 16;
  return M24((u32)max(row, 0), 1152) + (((l & 7) ^ ((4 * j + (l >> 4)) & 7)) * 16);
}
DEV u32 hpos(u32 u, u32 n) {
  const u32 cg = M24(u, 57) >> 10, r = msub(u, cg, 18);
  u32 a = (2 * cg + (r >> 3)) * 2048 + (r & 4) * 2, b = (16 + (cg >> 2)) * 2048 + (cg & 1) * 512 + (cg & 2) * 4; PIN(a); PIN(b);
  return (r < 16 ? a : b) + (r & 3) * 256 + n * 16;
}
__global__ __launch_bounds__(256, 1) void k_kf(const u16* __restrict__ x, const u16* __restrict__ wqkv, const u16* __restrict__ wo, const u16* __restrict__ wg, const u16* __restrict__ wu, const u16* __restrict__ wd, char* __restrict__ ws, u16* __restrict__ out) {
  extern __shared__ __attribute__((aligned(16))) char smem[];
  STAMP_DECL
  STAMP(0, 30);
  u32x16 va, vb; u32x8 vp, sc; u32x4 vi0, sa0, sd, se, srdS, srdW, srdG, srdT; u32x2 vi1;
  asm volatile("s_load_dwordx2 s[80:81], %3, 0\n\ts_load_dwordx2 s[40:41], %3, 8\n\ts_load_dwordx2 s[84:85], %3, 16\n\ts_load_dwordx2 s[44:45], %3, 48\n\t"
               "s_mov_b32 s82, 0x240000\n\ts_mov_b32 s83, 0x20000\n\ts_mov_b32 s86, 0xa2000\n\ts_mov_b32 s87, s83\n\ts_mov_b32 s42, 0x1e6000\n\ts_mov_b32 s43, s83\n\ts_mov_b32 s46, %4\n\ts_mov_b32 s47, s83"
               : "={s[80:87]}"(sc), "={s[40:43]}"(srdW), "={s[44:47]}"(srdS) : "s"(KARG), "i"((u32)cfg::WS_TMP));
  SB();
  const u32 bid = blockIdx.x & 255, tid = threadIdx.x & 255;
  const u32 w = RFL(tid >> 6) & 3;
  u32 tpc, tpi, tp1, tp2, ep, tag, toff; u32x2 vt;
  const u32 fl = 0x00020000u;
  const u32 xl = bid & 7, idx = bid >> 3;
  const u32 l = tid & 63, q = l >> 4, n = l & 15, vL = l * 16, vT = l * 128;
  { unsigned long long pc; u32 len; asm volatile("s_getpc_b64 %0\nLKPC:\n\ts_mov_b32 %1, LKEND-LKPC" : "=s"(pc), "=s"(len));
    asm volatile("s_mov_b64 exec, 31\n\tbuffer_load_dword %0, %1, %2, %3 offen\n\ts_mov_b64 exec, -1" : "=v"(tpc) : "v"(vT), "s"(u32x4{(u32)pc, (u32)(pc >> 32), len, fl}), "s"((idx * 4 + w) * 640) : "memory"); }
  SB();
  asm volatile("s_min_u32 %2, %4, 2\n\ts_lshl3_add_u32 %2, %2, 40\n\ts_load_dwordx2 s[48:49], %3, %2\n\ts_and_b32 %2, %4, 1\n\ts_lshl3_add_u32 %2, %2, 24\n\ts_load_dwordx2 s[36:37], %3, %2\n\t"
               "s_mov_b32 s51, 0x20000\n\ts_mov_b32 s39, s51\n\ts_mov_b32 s38, 0x510000\n\ts_cmp_lt_u32 %4, 2\n\ts_cselect_b32 s50, s38, 0x240000\n\ts_cmp_eq_u32 %4, 1\n\ts_cselect_b32 s50, %5, s50"
               : "={s[48:51]}"(srdT), "={s[36:39]}"(srdG), "=&s"(toff) : "s"(KARG), "s"(w), "i"((u32)cfg::WS_DBG) : "scc");
  SB();
  const u32 mt = (xl & 1) * 4 + (idx & 3), kq = xl >> 1, f7 = idx >> 2, tt = (xl >> 1) * 8 + (idx >> 2), hg = (xl & 1) * 4 + (idx & 3);
#define srdX __builtin_shufflevector(sc, sc, 0, 1, 2, 3)
#define srdO __builtin_shufflevector(sc, sc, 4, 5, 6, 7)
  const u32 m0a = w * 1024, m0b = w * 9216, hb = hg * 144;
  const u32 fgrp = (u32)cfg::WS_FLAGS + (tt & 28) * cfg::FST;
  f32x4 acc[4][5]; u32 vw0, vw1, vz, vxa, vxb, vy, vo2, vo3;
  u32 kx[10], sw[9]; kx[0] = (tt + hg) % 9 * 128;
  vw0 = wpiece(w * 64 + l); PIN(vw0);
  SB();
  asm volatile("s_waitcnt lgkmcnt(0)" : "+{s[80:87]}"(sc), "+{s[40:43]}"(srdW), "+{s[44:47]}"(srdS), "+{s[48:51]}"(srdT), "+{s[36:39]}"(srdG));
  const bf16x8 one8 = __builtin_bit_cast(bf16x8, u32x4{0x3f803f80u, 0x3f803f80u, 0x3f803f80u, 0x3f803f80u});
#define WF(b, kp, t) __builtin_bit_cast(bf16x8, __builtin_shufflevector(Wh[b][kp][t][0], Wh[b][kp][t][1], 0, 1, 2, 3, 4, 5, 6, 7))
  asm volatile("s_mov_b64 exec, 63\n\tbuffer_load_dword %0, %1, %2, %3 offen\n\ts_mov_b64 exec, -1" : "=v"(tpi) : "v"(vT), "s"(srdO), "s"((bid * 4 + w) * 768) : "memory");
  SB();
  tp2 = 0;
  SB();
  tp1 = 0;
  SB();

  SB();
  KNEXT(kx[1], sw[0], kx[0]);
  DMAX(m0a, 51200, vw0, srdW, sw[0]);
  SB();
  u32 xu = M24(tt * 64 + w * 8 + (l >> 3), 1152) + (((l & 7) ^ q ^ (~w & 1) * 4) * 16); PIN(xu); vxa = max((int)xu - 9216, 0); PIN(vxa);
  SB();
  DMAX(m0a, 11264, vxa, srdX, kx[0]);
  SB();
  vxb = xu + 27648; PIN(vxb);
  SB();
  DMAX(m0a, 15360, vxb, srdX, kx[0]);
  SB();
  DMA(m0a, 60416, vw0, srdW, sw[0], 1152);
  SB();
  DMA(m0a, 69632, vw0, srdW, sw[0], 2304);
  SB();
  vw1 = wpiece(w * 64 + 256 + l); PIN(vw1);
  SB();
  DMAX(m0a, 55296, vw1, srdW, sw[0]);
  SB();
  DMA(m0a, 64512, vw1, srdW, sw[0], 1152);
  SB();
  DMA(m0a, 73728, vw1, srdW, sw[0], 2304);
  SB();
  u32 e3 = w & (w >> 1); PINS(e3); e3 = 0u - e3; PINS(e3); vz = __builtin_amdgcn_bitop3_b32(max((int)xu - 46080, 0), wpiece(512 + l) + w * 1152, e3, 0xe4); PIN(vz);
  SB();
  const u32x4 srdZ = e3 ? srdX : srdW; const u32x2 ez = {~(e3 & 255u), ~0u}, ey = {e3, e3};
  SB();
  DMAZ(m0b + 59392 + (e3 & 4294900736u), vz, srdZ, e3 ? kx[1] : sw[0], ez);
  SB();
  STAMP(1, 0);
  s16x4 Wh[9][2][4][2]; bf16x8 X[9][2][5]; u32 aw[4], av[4], ae[4], a0, a1;
  const u32 par = w & 1, half = w >> 1;
  for (int t = 0; t < 2; ++t) { const u32 g4 = min(16 * w + 4 * t + (l & 3), 53u), mat = M24(g4, 57) >> 10; aw[t] = 51200 + M24(mat, 9216 - 144) + (l >> 2) * 144 + g4 * 8; av[t] = aw[t] + 55296; PIN(aw[t]); PIN(av[t]); }
  SB();
    for (int t = 2; t < 4; ++t) { const u32 g4 = min(16 * w + 4 * t + (l & 3), 53u), mat = M24(g4, 57) >> 10; aw[t] = 51200 + M24(mat, 9216 - 144) + (l >> 2) * 144 + g4 * 8; av[t] = aw[t] + 55296; PIN(aw[t]); PIN(av[t]); }
  a0 = n * 128 + ((q ^ (n >> 1)) * 16); a1 = a0 ^ 64; PIN(a0); PIN(a1);
  SB();
  u32 o5 = opiece(w * 64 + l), o7 = lpiece(w * 64 + l); PIN(o5); PIN(o7);
  SB();
  u32 o6 = opiece(w * 64 + 256 + l), o8 = lpiece(w * 64 + 256 + l); PIN(o6); PIN(o8);
  SB();
  vo2 = opiece(512 + l) + w * 82944; vo3 = __builtin_amdgcn_bitop3_b32(lpiece(512 + l), vo2 - 82944, w == 0 ? ~0u : 0u, 0xe4); PIN(vo2); PIN(vo3);
  SB();
  { const u32 qm = 0x3feu >> (4 * q); for (int j = 0; j < 5; ++j) vb[j] = __builtin_amdgcn_sbfe(qm, j, 1); }
  vb[5] = o5; vb[6] = o6; vb[7] = o7; vb[8] = o8;
  vb[9] = (l < 32 ? fgrp : (u32)cfg::WS_FLAGS + mt * 4 * cfg::FST) + ((l >> 3) & 3) * cfg::FST + (l & 7) * 4; vb[10] = l * 16 | OOB(q == 0);
  { const u32 r = wrow(l); vb[11] = M24(tt * 64 + w * 21 + r, 1152) + msub(l, r, 9) * 16; }
  u32 mxr = w * 3024; PINS(mxr);
  for (int j = 12; j < 16; ++j) vb[j] = 0x3f803f80u;
  asm volatile("" : "+{v[32:47]}"(vb));
  SB();
  const int d0 = 8 + 8 * (int)half + (int)n - 4 * (int)q;
  u32 m[12];
  { u32 lo = tt + half ? 0u : 32u; PINS(lo); const u32 iv = ~(u32)((0xffull << (d0 + 4)) >> 11) | (l < lo ? 15u : 0u);
    for (int r = 0; r < 8; ++r) { m[r] = (u32)__builtin_amdgcn_sbfe(iv, (r >> 2) * 16 + (r & 3), 1) << 24; PIN(m[r]); } }
  SB();
  { const u32 sh = min(max(d0 - 7, 0), 4) * 8; unsigned long long k = ~0ull << sh; PIN(k); k <<= sh; m[8] = (u32)k; m[9] = (u32)(k >> 32); m[10] = ~m[8]; m[11] = ~m[9]; }
  va = u32x16{m[0], m[1], m[2], m[3], m[4], m[5], m[6], m[7], m[8], m[9], m[10], m[11], 12800 + (2 * (8 + 16 * half + n) + par) * 80 + q * 16, 25600 + (2 * (8 * half + n) + par) * 80 + q * 16,
              38400 + (2 * (8 * half + (l >> 2)) + par) * 80 + (l & 3) * 8, 161792 + 640 + (16 + half * 32 + n * 2 + par) * 4};
  asm volatile("" : "+{v[16:31]}"(va));
  SB();
  u32 p0 = 78848 + (l >> 2) * 144 + (l & 3) * 8, p1 = (n & 1) * 64 + q * 16,
      p4 = (u32)cfg::WS_FL2 + tt * 16384 + hg * 16 + w * 4 + l * 512 | OOB(l < 8); PIN(p0); PIN(p1); PIN(p4);
  SB();
  { u32 b0 = (u32)cfg::WS_STATS + tt * 2048 + hg * 256, b1 = (u32)cfg::WS_MEAN + tt * 256; PINS(b0); PINS(b1);
    vp = u32x8{p0, p1, p0 + 9 * 4608, vo2, p4, (16 + (hg >> 2) - 2 * hg) * 2048 + (hg & 1) * 512 + (hg & 2) * 4 + l * 16 | OOB(q < 2),
               (q == 0 ? b0 : b1) + n * 16 + w * 4 | OOB(q < (hg == 0 ? 2u : 1u)), (half * 32 + n * 2 + par) * 144 + q * 8};
    asm volatile("" : "+{v[240:247]}"(vp)); }
  SB();
  { const u32 st = ((l & 7) >> 1) ^ (q & 3), i1 = (l >> 3) * 9216 + st * 32 + (l & 1) * 16;
    vi0 = u32x4{vL, i1, i1 + (8 * 9216 - 1024), i1 + 2 * (8 * 9216 - 1024)}; vi1 = u32x2{i1 + 3 * (8 * 9216 - 1024), (l >> 1) * 9216 + (l & 1) * 16};
    asm volatile("" : "+{v[0:3]}"(vi0), "+{v[4:5]}"(vi1)); }
  SB();
  { const u32 fj = kq * 8 + f7, fo = ((tt * 2 + half) * 36 + par) * 1024, so = (u32)cfg::WS_OFRAG + fo;
    sa0 = u32x4{w * 4096, 147456 + w * 1024, half * 294912 + kq * 2048 + f7 * 128, half * 294912 + 8192 + fj * 32};
    sd = u32x4{m0a, hb, so + hg * 4096, so + (16 + (hg >> 2)) * 2048 + (hg & 3) * 256};
    se = u32x4{bid, so, (u32)cfg::WS_HFRAG + fo + hg * 4096, (u32)cfg::WS_FL2 + ((mt * 4 + w) * 32 + (fj & (8 - 1))) * 512};
    asm volatile("" : "+{s[88:91]}"(sd), "+{s[32:35]}"(se), "+{s[52:55]}"(sa0)); }
  SB();
  for (int t = 0; t < 4; ++t) { const u32 ge = 16 * w + 4 * t + q, im = M24(ge, 57) >> 9; ae[t] = (t == 1 && ge >= 54 ? 72u : M24(im, 6400 - 72) + ge * 8) + n * 80 + 12800; PIN(ae[t]); }
  SB();
  { const u32 wid = ((xl & 1) * 32 + idx) * 2 + half, ia = wid * 36 + l, ic = wid * 9 + l - 36;
    u32 ta = M24(ia >> 3, 9216) + kq * 2048 + (ia & 7) * 128, tc = M24(ic >> 1, 9216) + 8192 + kq * 256 + (ic & 1) * 128; PIN(ta); PIN(tc);
    vt[1] = (l < 36 ? ta : tc) | OOB(l < 45); vt[0] = 0; asm volatile("" : "+{v[252:253]}"(vt)); }
  u32 tsl = (idx + w * 3 / 2) % 6, tsb = (idx + 3 + w * 3 / 2) % 6; PINS(tsl); PINS(tsb);
  SB();
  STAMP(2, 1);
  DMAZ(10240, vz, srdX, kx[0], ez & ey);
  SB();
  vy = (xu ^ 64) + 36864; PIN(vy);
  SB();
  DMAZ(19456, vy, srdX, kx[0], ey);
  SB();
  DMAX(m0a, 21504, vxa, srdX, kx[1]);
  SB();
  DMAX(m0a, 25600, vxb, srdX, kx[1]);
  SB();
  DMAZ(29696, vy, srdX, kx[1], ey);
  SB();
  BLD4C(ep, l < 32 ? fgrp : (u32)cfg::WS_FLAGS + mt * 4 * cfg::FST, srdS, 0, 2048);
  SB();
  KNEXT(kx[2], sw[1], kx[1]);
  DMAX(m0a, 78848, vw0, srdW, sw[1]);
  SB();
  DMA(m0a, 88064, vw0, srdW, sw[1], 1152);
  SB();
  DMA(m0a, 97280, vw0, srdW, sw[1], 2304);
  SB();
  DMAX(m0a, 31744, vxa, srdX, kx[2]);
  SB();
  DMAX(m0a, 82944, vw1, srdW, sw[1]);
  SB();
  DMA(m0a, 92160, vw1, srdW, sw[1], 1152);
  SB();
  DMA(m0a, 101376, vw1, srdW, sw[1], 2304);
  SB();
  DMAX(m0a, 35840, vxb, srdX, kx[2]);
  SB();
  DMAZ(m0b + 87040 + (e3 & 4294883328u), vz, srdZ, e3 ? kx[2] : sw[1], ez);
  SB();
  DMAZ(39936, vy, srdX, kx[2], ey);
  SB();
  u32x4 m1 = {~0u, ~0u, ~0u, ~0u}; PIN(m1);
  SB();
  if (w == 3) {
  *LDSP(u32x4, min(l >> 3, 4u) * 10240 + (l & 7) * 16) = u32x4{0x3f803f80u, 0x3f803f80u, 0x3f803f80u, 0x3f803f80u}; bf16x8 on8 = one8; PIN(on8); const u32 as = (q == (n >> 2) ? 161792 : 162816) + n * 8;
  VMW(11);
  LGW(); BAR();
  STAMP(3, 2);
  X[0][0][0] = *LDSP(bf16x8, a0 + 10240);
  X[0][0][1] = *LDSP(bf16x8, a0 + 12288);
  X[0][0][2] = *LDSP(bf16x8, a0 + 14336);
  X[0][0][3] = *LDSP(bf16x8, a0 + 16384);
  X[0][0][4] = *LDSP(bf16x8, a0 + 18432);
  SB();
  MA0(acc[2][0], on8, X[0][0][0]); SB();
  X[0][1][0] = *LDSP(bf16x8, a1 + 10240);
  KNEXT(kx[3], sw[2], kx[2]);
  DMAX(m0a, 106496, vw0, srdW, sw[2]);
  SB();
  MX0(acc[3][0], X[0][0][0]); SB();
  X[0][1][1] = *LDSP(bf16x8, a1 + 12288);
  SB();
  MA0(acc[2][1], on8, X[0][0][1]); SB();
  X[0][1][2] = *LDSP(bf16x8, a1 + 14336);
  SB();
  MX0(acc[3][1], X[0][0][1]); SB();
  X[0][1][3] = *LDSP(bf16x8, a1 + 16384);
  SB();
  MA0(acc[2][2], on8, X[0][0][2]); SB();
  X[0][1][4] = *LDSP(bf16x8, a1 + 18432);
  DMA(m0a, 115712, vw0, srdW, sw[2], 1152);
  SB();
  MX0(acc[3][2], X[0][0][2]); SB();
  Wh[0][0][0][0] = __builtin_amdgcn_ds_read_tr16_b64_v4i16(LDSP(s16x4, aw[0] + 0));
  SB();
  MA0(acc[2][3], on8, X[0][0][3]); SB();
  Wh[0][0][0][1] = __builtin_amdgcn_ds_read_tr16_b64_v4i16(LDSP(s16x4, aw[0] + 2304));
  SB();
  MX0(acc[3][3], X[0][0][3]); SB();
  Wh[0][0][1][0] = __builtin_amdgcn_ds_read_tr16_b64_v4i16(LDSP(s16x4, aw[1] + 0));
  SB();
  MA0(acc[2][4], on8, X[0][0][4]); SB();
  Wh[0][0][1][1] = __builtin_amdgcn_ds_read_tr16_b64_v4i16(LDSP(s16x4, aw[1] + 2304));
  DMA(m0a, 124928, vw0, srdW, sw[2], 2304);
  SB();
  MX0(acc[3][4], X[0][0][4]); SB();
  X[1][0][0] = *LDSP(bf16x8, a0 + 20480);
  SB();
  MA1(acc[2][0], on8, X[0][1][0]); SB();
  X[1][0][1] = *LDSP(bf16x8, a0 + 22528);
  SB();
  MX1(acc[3][0], X[0][1][0]); SB();
  X[1][0][2] = *LDSP(bf16x8, a0 + 24576);
  SB();
  MA1(acc[2][1], on8, X[0][1][1]); SB();
  X[1][0][3] = *LDSP(bf16x8, a0 + 26624);
  DMAX(m0a, 41984, vxa, srdX, kx[3]);
  SB();
  MX1(acc[3][1], X[0][1][1]); SB();
  X[1][0][4] = *LDSP(bf16x8, a0 + 28672);
  SB();
  MA1(acc[2][2], on8, X[0][1][2]); SB();
  X[1][1][0] = *LDSP(bf16x8, a1 + 20480);
  SB();
  MX1(acc[3][2], X[0][1][2]); SB();
  X[1][1][1] = *LDSP(bf16x8, a1 + 22528);
  SB();
  MA1(acc[2][3], on8, X[0][1][3]); SB();
  X[1][1][2] = *LDSP(bf16x8, a1 + 24576);
  DMAX(m0a, 110592, vw1, srdW, sw[2]);
  SB();
  MX1(acc[3][3], X[0][1][3]); SB();
  X[1][1][3] = *LDSP(bf16x8, a1 + 26624);
  SB();
  MA1(acc[2][4], on8, X[0][1][4]); SB();
  X[1][1][4] = *LDSP(bf16x8, a1 + 28672);
  SB();
  MX1(acc[3][4], X[0][1][4]); SB();
  Wh[0][1][0][0] = __builtin_amdgcn_ds_read_tr16_b64_v4i16(LDSP(s16x4, aw[0] + 4608));
  SB();
  MA0(acc[0][0], WF(0, 0, 0), X[0][0][0]); SB();
  Wh[0][1][0][1] = __builtin_amdgcn_ds_read_tr16_b64_v4i16(LDSP(s16x4, aw[0] + 6912));
  DMA(m0a, 119808, vw1, srdW, sw[2], 1152);
  SB();
  MA0(acc[0][1], WF(0, 0, 0), X[0][0][1]); SB();
  Wh[0][1][1][0] = __builtin_amdgcn_ds_read_tr16_b64_v4i16(LDSP(s16x4, aw[1] + 4608));
  SB();
  MA0(acc[0][2], WF(0, 0, 0), X[0][0][2]); SB();
  Wh[0][1][1][1] = __builtin_amdgcn_ds_read_tr16_b64_v4i16(LDSP(s16x4, aw[1] + 6912));
  SB();
  MA0(acc[0][3], WF(0, 0, 0), X[0][0][3]); SB();
  SB();
  MA0(acc[0][4], WF(0, 0, 0), X[0][0][4]); SB();
  DMA(m0a, 129024, vw1, srdW, sw[2], 2304);
  SB();
  MA0(acc[1][0], WF(0, 0, 1), X[0][0][0]); SB();
  SB();
  MA0(acc[1][1], WF(0, 0, 1), X[0][0][1]); SB();
  SB();
  MA0(acc[1][2], WF(0, 0, 1), X[0][0][2]); SB();
  SB();
  MA0(acc[1][3], WF(0, 0, 1), X[0][0][3]); SB();
  DMAX(m0a, 46080, vxb, srdX, kx[3]);
  SB();
  MA0(acc[1][4], WF(0, 0, 1), X[0][0][4]); SB();
  SB();
  MA1(acc[2][0], on8, X[1][0][0]); SB();
  SB();
  MX1(acc[3][0], X[1][0][0]); SB();
  SB();
  MA1(acc[2][1], on8, X[1][0][1]); SB();
  DMAM(40960, vz, srdX, kx[3]);
  SB();
  MX1(acc[3][1], X[1][0][1]); SB();
  SB();
  MA1(acc[2][2], on8, X[1][0][2]); SB();
  SB();
  MX1(acc[3][2], X[1][0][2]); SB();
  SB();
  MA1(acc[2][3], on8, X[1][0][3]); SB();
  DMAI(50176, vy, srdX, kx[3]);
  SB();
  MX1(acc[3][3], X[1][0][3]); SB();
  SB();
  MA1(acc[2][4], on8, X[1][0][4]); SB();
  SB();
  MX1(acc[3][4], X[1][0][4]); SB();
  SB();
  MA1(acc[2][0], on8, X[1][1][0]); SB();
  SB();
  MX1(acc[3][0], X[1][1][0]); SB();
  SB();
  MA1(acc[2][1], on8, X[1][1][1]); SB();
  SB();
  MX1(acc[3][1], X[1][1][1]); SB();
  SB();
  MA1(acc[2][2], on8, X[1][1][2]); SB();
  SB();
  MX1(acc[3][2], X[1][1][2]); SB();
  SB();
  MA1(acc[2][3], on8, X[1][1][3]); SB();
  SB();
  MX1(acc[3][3], X[1][1][3]); SB();
  SB();
  MA1(acc[2][4], on8, X[1][1][4]); SB();
  SB();
  MX1(acc[3][4], X[1][1][4]); SB();
  SB();
  VMW(10);
  LGW(); BAR();
  STAMP(4, 3);
  MA1(acc[0][0], WF(0, 1, 0), X[0][1][0]); SB();
  X[2][0][0] = *LDSP(bf16x8, a0 + 30720);
  KNEXT(kx[4], sw[3], kx[3]);
  DMAX(m0a, 134144, vw0, srdW, sw[3]);
  SB();
  MA1(acc[0][1], WF(0, 1, 0), X[0][1][1]); SB();
  X[2][0][1] = *LDSP(bf16x8, a0 + 32768);
  SB();
  MA1(acc[0][2], WF(0, 1, 0), X[0][1][2]); SB();
  X[2][0][2] = *LDSP(bf16x8, a0 + 34816);
  SB();
  MA1(acc[0][3], WF(0, 1, 0), X[0][1][3]); SB();
  X[2][0][3] = *LDSP(bf16x8, a0 + 36864);
  SB();
  MA1(acc[0][4], WF(0, 1, 0), X[0][1][4]); SB();
  X[2][0][4] = *LDSP(bf16x8, a0 + 38912);
  DMA(m0a, 143360, vw0, srdW, sw[3], 1152);
  SB();
  MA1(acc[1][0], WF(0, 1, 1), X[0][1][0]); SB();
  X[2][1][0] = *LDSP(bf16x8, a1 + 30720);
  SB();
  MA1(acc[1][1], WF(0, 1, 1), X[0][1][1]); SB();
  X[2][1][1] = *LDSP(bf16x8, a1 + 32768);
  SB();
  MA1(acc[1][2], WF(0, 1, 1), X[0][1][2]); SB();
  X[2][1][2] = *LDSP(bf16x8, a1 + 34816);
  DMA(m0a, 152576, vw0, srdW, sw[3], 2304);
  SB();
  MA1(acc[1][3], WF(0, 1, 1), X[0][1][3]); SB();
  X[2][1][3] = *LDSP(bf16x8, a1 + 36864);
  SB();
  MA1(acc[1][4], WF(0, 1, 1), X[0][1][4]); SB();
  X[2][1][4] = *LDSP(bf16x8, a1 + 38912);
  SB();
  MA1(acc[2][0], on8, X[2][0][0]); SB();
  Wh[1][0][0][0] = __builtin_amdgcn_ds_read_tr16_b64_v4i16(LDSP(s16x4, aw[0] + 27648));
  SB();
  MX1(acc[3][0], X[2][0][0]); SB();
  Wh[1][0][0][1] = __builtin_amdgcn_ds_read_tr16_b64_v4i16(LDSP(s16x4, aw[0] + 29952));
  SB();
  MA1(acc[2][1], on8, X[2][0][1]); SB();
  Wh[1][0][1][0] = __builtin_amdgcn_ds_read_tr16_b64_v4i16(LDSP(s16x4, aw[1] + 27648));
  DMAX(m0a, 1024, vxa, srdX, kx[4]);
  SB();
  MX1(acc[3][1], X[2][0][1]); SB();
  Wh[1][0][1][1] = __builtin_amdgcn_ds_read_tr16_b64_v4i16(LDSP(s16x4, aw[1] + 29952));
  SB();
  MA1(acc[2][2], on8, X[2][0][2]); SB();
  Wh[1][1][0][0] = __builtin_amdgcn_ds_read_tr16_b64_v4i16(LDSP(s16x4, aw[0] + 32256));
  SB();
  MX1(acc[3][2], X[2][0][2]); SB();
  Wh[1][1][0][1] = __builtin_amdgcn_ds_read_tr16_b64_v4i16(LDSP(s16x4, aw[0] + 34560));
  DMAX(m0a, 138240, vw1, srdW, sw[3]);
  SB();
  MA1(acc[2][3], on8, X[2][0][3]); SB();
  Wh[1][1][1][0] = __builtin_amdgcn_ds_read_tr16_b64_v4i16(LDSP(s16x4, aw[1] + 32256));
  SB();
  MX1(acc[3][3], X[2][0][3]); SB();
  Wh[1][1][1][1] = __builtin_amdgcn_ds_read_tr16_b64_v4i16(LDSP(s16x4, aw[1] + 34560));
  SB();
  MA1(acc[2][4], on8, X[2][0][4]); SB();
  SB();
  MX1(acc[3][4], X[2][0][4]); SB();
  SB();
  MA1(acc[2][0], on8, X[2][1][0]); SB();
  DMA(m0a, 147456, vw1, srdW, sw[3], 1152);
  { u32x4 m2 = {~0u, ~0u, ~0u, ~0u}; PIN(m2); BST16(m2, vL, srdS, sd[2], 0, "sc1"); }
  SB();
  MX1(acc[3][0], X[2][1][0]); SB();
  SB();
  MA1(acc[2][1], on8, X[2][1][1]); SB();
  SB();
  MX1(acc[3][1], X[2][1][1]); SB();
  SB();
  MA1(acc[2][2], on8, X[2][1][2]); SB();
  DMA(m0a, 156672, vw1, srdW, sw[3], 2304);
  SB();
  MX1(acc[3][2], X[2][1][2]); SB();
  { u32x4 m2 = {~0u, ~0u, ~0u, ~0u}; PIN(m2); BST16(m2, vL, srdS, sd[2], 2048, "sc1"); }
  SB();
  MA1(acc[2][3], on8, X[2][1][3]); SB();
  SB();
  MX1(acc[3][3], X[2][1][3]); SB();
  SB();
  MA1(acc[2][4], on8, X[2][1][4]); SB();
  DMAX(m0a, 5120, vxb, srdX, kx[4]);
  SB();
  MX1(acc[3][4], X[2][1][4]); SB();
  SB();
  MA1(acc[0][0], WF(1, 0, 0), X[1][0][0]); SB();
  SB();
  MA1(acc[0][1], WF(1, 0, 0), X[1][0][1]); SB();
  SB();
  MA1(acc[0][2], WF(1, 0, 0), X[1][0][2]); SB();
  DMAM(0, vz, srdX, kx[4]);
  { u32x4 m2 = {~0u, ~0u, ~0u, ~0u}; PIN(m2); BST16(m2, n * 16 + min(q, 1u) * 32768 + (q >> 1) * 2359296 | OOB(q < 3), srdS, sd[3] - (2359296 + 32768), 0, "sc1"); }
  SB();
  MA1(acc[0][3], WF(1, 0, 0), X[1][0][3]); SB();
  SB();
  MA1(acc[0][4], WF(1, 0, 0), X[1][0][4]); SB();
  SB();
  MA1(acc[1][0], WF(1, 0, 1), X[1][0][0]); SB();
  SB();
  MA1(acc[1][1], WF(1, 0, 1), X[1][0][1]); SB();
  DMAI(9216, vy, srdX, kx[4]);
  SB();
  MA1(acc[1][2], WF(1, 0, 1), X[1][0][2]); SB();
  SB();
  MA1(acc[1][3], WF(1, 0, 1), X[1][0][3]); SB();
  SB();
  MA1(acc[1][4], WF(1, 0, 1), X[1][0][4]); SB();
  SB();
  VMW(13);
  LGW(); BAR();
  STAMP(5, 4);
  MA1(acc[0][0], WF(1, 1, 0), X[1][1][0]); SB();
  X[3][0][0] = *LDSP(bf16x8, a0 + 40960);
  KNEXT(kx[5], sw[4], kx[4]);
  DMAX(m0a, 51200, vw0, srdW, sw[4]);
  SB();
  MA1(acc[0][1], WF(1, 1, 0), X[1][1][1]); SB();
  X[3][0][1] = *LDSP(bf16x8, a0 + 43008);
  SB();
  MA1(acc[0][2], WF(1, 1, 0), X[1][1][2]); SB();
  X[3][0][2] = *LDSP(bf16x8, a0 + 45056);
  SB();
  MA1(acc[0][3], WF(1, 1, 0), X[1][1][3]); SB();
  X[3][0][3] = *LDSP(bf16x8, a0 + 47104);
  SB();
  MA1(acc[0][4], WF(1, 1, 0), X[1][1][4]); SB();
  X[3][0][4] = *LDSP(bf16x8, a0 + 49152);
  DMA(m0a, 60416, vw0, srdW, sw[4], 1152);
  SB();
  MA1(acc[1][0], WF(1, 1, 1), X[1][1][0]); SB();
  X[3][1][0] = *LDSP(bf16x8, a1 + 40960);
  SB();
  MA1(acc[1][1], WF(1, 1, 1), X[1][1][1]); SB();
  X[3][1][1] = *LDSP(bf16x8, a1 + 43008);
  SB();
  MA1(acc[1][2], WF(1, 1, 1), X[1][1][2]); SB();
  X[3][1][2] = *LDSP(bf16x8, a1 + 45056);
  SB();
  MA1(acc[1][3], WF(1, 1, 1), X[1][1][3]); SB();
  X[3][1][3] = *LDSP(bf16x8, a1 + 47104);
  DMA(m0a, 69632, vw0, srdW, sw[4], 2304);
  SB();
  MA1(acc[1][4], WF(1, 1, 1), X[1][1][4]); SB();
  X[3][1][4] = *LDSP(bf16x8, a1 + 49152);
  SB();
  MA1(acc[2][0], on8, X[3][0][0]); SB();
  Wh[2][0][0][0] = __builtin_amdgcn_ds_read_tr16_b64_v4i16(LDSP(s16x4, av[0] + 0));
  SB();
  MX1(acc[3][0], X[3][0][0]); SB();
  Wh[2][0][0][1] = __builtin_amdgcn_ds_read_tr16_b64_v4i16(LDSP(s16x4, av[0] + 2304));
  DMAX(m0a, 11264, vxa, srdX, kx[5]);
  SB();
  MA1(acc[2][1], on8, X[3][0][1]); SB();
  Wh[2][0][1][0] = __builtin_amdgcn_ds_read_tr16_b64_v4i16(LDSP(s16x4, av[1] + 0));
  SB();
  MX1(acc[3][1], X[3][0][1]); SB();
  Wh[2][0][1][1] = __builtin_amdgcn_ds_read_tr16_b64_v4i16(LDSP(s16x4, av[1] + 2304));
  SB();
  MA1(acc[2][2], on8, X[3][0][2]); SB();
  Wh[2][1][0][0] = __builtin_amdgcn_ds_read_tr16_b64_v4i16(LDSP(s16x4, av[0] + 4608));
  SB();
  MX1(acc[3][2], X[3][0][2]); SB();
  Wh[2][1][0][1] = __builtin_amdgcn_ds_read_tr16_b64_v4i16(LDSP(s16x4, av[0] + 6912));
  DMAX(m0a, 55296, vw1, srdW, sw[4]);
  SB();
  MA1(acc[2][3], on8, X[3][0][3]); SB();
  Wh[2][1][1][0] = __builtin_amdgcn_ds_read_tr16_b64_v4i16(LDSP(s16x4, av[1] + 4608));
  SB();
  MX1(acc[3][3], X[3][0][3]); SB();
  Wh[2][1][1][1] = __builtin_amdgcn_ds_read_tr16_b64_v4i16(LDSP(s16x4, av[1] + 6912));
  SB();
  MA1(acc[2][4], on8, X[3][0][4]); SB();
  SB();
  MX1(acc[3][4], X[3][0][4]); SB();
  SB();
  MA1(acc[2][0], on8, X[3][1][0]); SB();
  DMA(m0a, 64512, vw1, srdW, sw[4], 1152);
  SB();
  MX1(acc[3][0], X[3][1][0]); SB();
  SB();
  MA1(acc[2][1], on8, X[3][1][1]); SB();
  SB();
  MX1(acc[3][1], X[3][1][1]); SB();
  SB();
  MA1(acc[2][2], on8, X[3][1][2]); SB();
  DMA(m0a, 73728, vw1, srdW, sw[4], 2304);
  SB();
  MX1(acc[3][2], X[3][1][2]); SB();
  SB();
  MA1(acc[2][3], on8, X[3][1][3]); SB();
  SB();
  MX1(acc[3][3], X[3][1][3]); SB();
  SB();
  MA1(acc[2][4], on8, X[3][1][4]); SB();
  DMAX(m0a, 15360, vxb, srdX, kx[5]);
  SB();
  MX1(acc[3][4], X[3][1][4]); SB();
  SB();
  MA1(acc[0][0], WF(2, 0, 0), X[2][0][0]); SB();
  SB();
  MA1(acc[0][1], WF(2, 0, 0), X[2][0][1]); SB();
  DMAM(10240, vz, srdX, kx[5]);
  SB();
  MA1(acc[0][2], WF(2, 0, 0), X[2][0][2]); SB();
  SB();
  MA1(acc[0][3], WF(2, 0, 0), X[2][0][3]); SB();
  SB();
  MA1(acc[0][4], WF(2, 0, 0), X[2][0][4]); SB();
  SB();
  MA1(acc[1][0], WF(2, 0, 1), X[2][0][0]); SB();
  SB();
  MA1(acc[1][1], WF(2, 0, 1), X[2][0][1]); SB();
  DMAI(19456, vy, srdX, kx[5]);
  SB();
  MA1(acc[1][2], WF(2, 0, 1), X[2][0][2]); SB();
  SB();
  MA1(acc[1][3], WF(2, 0, 1), X[2][0][3]); SB();
  SB();
  MA1(acc[1][4], WF(2, 0, 1), X[2][0][4]); SB();
  SB();
  VMW(10);
  LGW(); BAR();
  STAMP(6, 5);
  MA1(acc[0][0], WF(2, 1, 0), X[2][1][0]); SB();
  X[4][0][0] = *LDSP(bf16x8, a0 + 0);
  KNEXT(kx[6], sw[5], kx[5]);
  DMAX(m0a, 78848, vw0, srdW, sw[5]);
  SB();
  MA1(acc[0][1], WF(2, 1, 0), X[2][1][1]); SB();
  X[4][0][1] = *LDSP(bf16x8, a0 + 2048);
  SB();
  MA1(acc[0][2], WF(2, 1, 0), X[2][1][2]); SB();
  X[4][0][2] = *LDSP(bf16x8, a0 + 4096);
  SB();
  MA1(acc[0][3], WF(2, 1, 0), X[2][1][3]); SB();
  X[4][0][3] = *LDSP(bf16x8, a0 + 6144);
  SB();
  MA1(acc[0][4], WF(2, 1, 0), X[2][1][4]); SB();
  X[4][0][4] = *LDSP(bf16x8, a0 + 8192);
  DMA(m0a, 88064, vw0, srdW, sw[5], 1152);
  SB();
  MA1(acc[1][0], WF(2, 1, 1), X[2][1][0]); SB();
  X[4][1][0] = *LDSP(bf16x8, a1 + 0);
  SB();
  MA1(acc[1][1], WF(2, 1, 1), X[2][1][1]); SB();
  X[4][1][1] = *LDSP(bf16x8, a1 + 2048);
  SB();
  MA1(acc[1][2], WF(2, 1, 1), X[2][1][2]); SB();
  X[4][1][2] = *LDSP(bf16x8, a1 + 4096);
  SB();
  MA1(acc[1][3], WF(2, 1, 1), X[2][1][3]); SB();
  X[4][1][3] = *LDSP(bf16x8, a1 + 6144);
  DMA(m0a, 97280, vw0, srdW, sw[5], 2304);
  SB();
  MA1(acc[1][4], WF(2, 1, 1), X[2][1][4]); SB();
  X[4][1][4] = *LDSP(bf16x8, a1 + 8192);
  SB();
  MA1(acc[2][0], on8, X[4][0][0]); SB();
  Wh[3][0][0][0] = __builtin_amdgcn_ds_read_tr16_b64_v4i16(LDSP(s16x4, av[0] + 27648));
  SB();
  MX1(acc[3][0], X[4][0][0]); SB();
  Wh[3][0][0][1] = __builtin_amdgcn_ds_read_tr16_b64_v4i16(LDSP(s16x4, av[0] + 29952));
  DMAX(m0a, 21504, vxa, srdX, kx[6]);
  SB();
  MA1(acc[2][1], on8, X[4][0][1]); SB();
  Wh[3][0][1][0] = __builtin_amdgcn_ds_read_tr16_b64_v4i16(LDSP(s16x4, av[1] + 27648));
  SB();
  MX1(acc[3][1], X[4][0][1]); SB();
  Wh[3][0][1][1] = __builtin_amdgcn_ds_read_tr16_b64_v4i16(LDSP(s16x4, av[1] + 29952));
  SB();
  MA1(acc[2][2], on8, X[4][0][2]); SB();
  Wh[3][1][0][0] = __builtin_amdgcn_ds_read_tr16_b64_v4i16(LDSP(s16x4, av[0] + 32256));
  SB();
  MX1(acc[3][2], X[4][0][2]); SB();
  Wh[3][1][0][1] = __builtin_amdgcn_ds_read_tr16_b64_v4i16(LDSP(s16x4, av[0] + 34560));
  DMAX(m0a, 82944, vw1, srdW, sw[5]);
  SB();
  MA1(acc[2][3], on8, X[4][0][3]); SB();
  Wh[3][1][1][0] = __builtin_amdgcn_ds_read_tr16_b64_v4i16(LDSP(s16x4, av[1] + 32256));
  SB();
  MX1(acc[3][3], X[4][0][3]); SB();
  Wh[3][1][1][1] = __builtin_amdgcn_ds_read_tr16_b64_v4i16(LDSP(s16x4, av[1] + 34560));
  SB();
  MA1(acc[2][4], on8, X[4][0][4]); SB();
  SB();
  MX1(acc[3][4], X[4][0][4]); SB();
  SB();
  MA1(acc[2][0], on8, X[4][1][0]); SB();
  DMA(m0a, 92160, vw1, srdW, sw[5], 1152);
  SB();
  MX1(acc[3][0], X[4][1][0]); SB();
  SB();
  MA1(acc[2][1], on8, X[4][1][1]); SB();
  SB();
  MX1(acc[3][1], X[4][1][1]); SB();
  SB();
  MA1(acc[2][2], on8, X[4][1][2]); SB();
  DMA(m0a, 101376, vw1, srdW, sw[5], 2304);
  SB();
  MX1(acc[3][2], X[4][1][2]); SB();
  SB();
  MA1(acc[2][3], on8, X[4][1][3]); SB();
  SB();
  MX1(acc[3][3], X[4][1][3]); SB();
  SB();
  MA1(acc[2][4], on8, X[4][1][4]); SB();
  DMAX(m0a, 25600, vxb, srdX, kx[6]);
  SB();
  MX1(acc[3][4], X[4][1][4]); SB();
  SB();
  MA1(acc[0][0], WF(3, 0, 0), X[3][0][0]); SB();
  SB();
  MA1(acc[0][1], WF(3, 0, 0), X[3][0][1]); SB();
  DMAM(20480, vz, srdX, kx[6]);
  SB();
  MA1(acc[0][2], WF(3, 0, 0), X[3][0][2]); SB();
  SB();
  MA1(acc[0][3], WF(3, 0, 0), X[3][0][3]); SB();
  SB();
  MA1(acc[0][4], WF(3, 0, 0), X[3][0][4]); SB();
  SB();
  MA1(acc[1][0], WF(3, 0, 1), X[3][0][0]); SB();
  SB();
  MA1(acc[1][1], WF(3, 0, 1), X[3][0][1]); SB();
  DMAI(29696, vy, srdX, kx[6]);
  SB();
  MA1(acc[1][2], WF(3, 0, 1), X[3][0][2]); SB();
  SB();
  MA1(acc[1][3], WF(3, 0, 1), X[3][0][3]); SB();
  SB();
  MA1(acc[1][4], WF(3, 0, 1), X[3][0][4]); SB();
  SB();
  VMW(10);
  LGW(); BAR();
  STAMP(7, 6);
  PIN(ep); tag = ep * 0x9E3779B1u + 0x7F4A7C15u; PIN(tag);
  MA1(acc[0][0], WF(3, 1, 0), X[3][1][0]); SB();
  X[5][0][0] = *LDSP(bf16x8, a0 + 10240);
  KNEXT(kx[7], sw[6], kx[6]);
  DMAX(m0a, 106496, vw0, srdW, sw[6]);
  SB();
  MA1(acc[0][1], WF(3, 1, 0), X[3][1][1]); SB();
  X[5][0][1] = *LDSP(bf16x8, a0 + 12288);
  SB();
  MA1(acc[0][2], WF(3, 1, 0), X[3][1][2]); SB();
  X[5][0][2] = *LDSP(bf16x8, a0 + 14336);
  SB();
  MA1(acc[0][3], WF(3, 1, 0), X[3][1][3]); SB();
  X[5][0][3] = *LDSP(bf16x8, a0 + 16384);
  DMA(m0a, 115712, vw0, srdW, sw[6], 1152);
  SB();
  MA1(acc[0][4], WF(3, 1, 0), X[3][1][4]); SB();
  X[5][0][4] = *LDSP(bf16x8, a0 + 18432);
  SB();
  MA1(acc[1][0], WF(3, 1, 1), X[3][1][0]); SB();
  X[5][1][0] = *LDSP(bf16x8, a1 + 10240);
  SB();
  MA1(acc[1][1], WF(3, 1, 1), X[3][1][1]); SB();
  X[5][1][1] = *LDSP(bf16x8, a1 + 12288);
  SB();
  MA1(acc[1][2], WF(3, 1, 1), X[3][1][2]); SB();
  X[5][1][2] = *LDSP(bf16x8, a1 + 14336);
  SB();
  MA1(acc[1][3], WF(3, 1, 1), X[3][1][3]); SB();
  X[5][1][3] = *LDSP(bf16x8, a1 + 16384);
  DMA(m0a, 124928, vw0, srdW, sw[6], 2304);
  SB();
  MA1(acc[1][4], WF(3, 1, 1), X[3][1][4]); SB();
  X[5][1][4] = *LDSP(bf16x8, a1 + 18432);
  SB();
  MA1(acc[2][0], on8, X[5][0][0]); SB();
  Wh[4][0][0][0] = __builtin_amdgcn_ds_read_tr16_b64_v4i16(LDSP(s16x4, aw[0] + 0));
  SB();
  MX1(acc[3][0], X[5][0][0]); SB();
  Wh[4][0][0][1] = __builtin_amdgcn_ds_read_tr16_b64_v4i16(LDSP(s16x4, aw[0] + 2304));
  DMAX(m0a, 31744, vxa, srdX, kx[7]);
  SB();
  MA1(acc[2][1], on8, X[5][0][1]); SB();
  Wh[4][0][1][0] = __builtin_amdgcn_ds_read_tr16_b64_v4i16(LDSP(s16x4, aw[1] + 0));
  SB();
  MX1(acc[3][1], X[5][0][1]); SB();
  Wh[4][0][1][1] = __builtin_amdgcn_ds_read_tr16_b64_v4i16(LDSP(s16x4, aw[1] + 2304));
  SB();
  MA1(acc[2][2], on8, X[5][0][2]); SB();
  Wh[4][1][0][0] = __builtin_amdgcn_ds_read_tr16_b64_v4i16(LDSP(s16x4, aw[0] + 4608));
  SB();
  MX1(acc[3][2], X[5][0][2]); SB();
  Wh[4][1][0][1] = __builtin_amdgcn_ds_read_tr16_b64_v4i16(LDSP(s16x4, aw[0] + 6912));
  SB();
  MA1(acc[2][3], on8, X[5][0][3]); SB();
  Wh[4][1][1][0] = __builtin_amdgcn_ds_read_tr16_b64_v4i16(LDSP(s16x4, aw[1] + 4608));
  DMAX(m0a, 110592, vw1, srdW, sw[6]);
  SB();
  MX1(acc[3][3], X[5][0][3]); SB();
  Wh[4][1][1][1] = __builtin_amdgcn_ds_read_tr16_b64_v4i16(LDSP(s16x4, aw[1] + 6912));
  SB();
  MA1(acc[2][4], on8, X[5][0][4]); SB();
  SB();
  MX1(acc[3][4], X[5][0][4]); SB();
  SB();
  MA1(acc[2][0], on8, X[5][1][0]); SB();
  DMA(m0a, 119808, vw1, srdW, sw[6], 1152);
  SB();
  MX1(acc[3][0], X[5][1][0]); SB();
  SB();
  MA1(acc[2][1], on8, X[5][1][1]); SB();
  SB();
  MX1(acc[3][1], X[5][1][1]); SB();
  DMA(m0a, 129024, vw1, srdW, sw[6], 2304);
  SB();
  MA1(acc[2][2], on8, X[5][1][2]); SB();
  SB();
  MX1(acc[3][2], X[5][1][2]); SB();
  SB();
  MA1(acc[2][3], on8, X[5][1][3]); SB();
  SB();
  MX1(acc[3][3], X[5][1][3]); SB();
  SB();
  MA1(acc[2][4], on8, X[5][1][4]); SB();
  DMAX(m0a, 35840, vxb, srdX, kx[7]);
  SB();
  MX1(acc[3][4], X[5][1][4]); SB();
  SB();
  MA1(acc[0][0], WF(4, 0, 0), X[4][0][0]); SB();
  SB();
  MA1(acc[0][1], WF(4, 0, 0), X[4][0][1]); SB();
  DMAM(30720, vz, srdX, kx[7]);
  SB();
  MA1(acc[0][2], WF(4, 0, 0), X[4][0][2]); SB();
  SB();
  MA1(acc[0][3], WF(4, 0, 0), X[4][0][3]); SB();
  SB();
  MA1(acc[0][4], WF(4, 0, 0), X[4][0][4]); SB();
  SB();
  MA1(acc[1][0], WF(4, 0, 1), X[4][0][0]); SB();
  SB();
  MA1(acc[1][1], WF(4, 0, 1), X[4][0][1]); SB();
  DMAI(39936, vy, srdX, kx[7]);
  SB();
  MA1(acc[1][2], WF(4, 0, 1), X[4][0][2]); SB();
  SB();
  MA1(acc[1][3], WF(4, 0, 1), X[4][0][3]); SB();
  SB();
  MA1(acc[1][4], WF(4, 0, 1), X[4][0][4]); SB();
  SB();
  VMW(10);
  LGW(); BAR();
  STAMP(8, 7);
  MA1(acc[0][0], WF(4, 1, 0), X[4][1][0]); SB();
  X[6][0][0] = *LDSP(bf16x8, a0 + 20480);
  KNEXT(kx[8], sw[7], kx[7]);
  DMAX(m0a, 134144, vw0, srdW, sw[7]);
  SB();
  MA1(acc[0][1], WF(4, 1, 0), X[4][1][1]); SB();
  X[6][0][1] = *LDSP(bf16x8, a0 + 22528);
  SB();
  MA1(acc[0][2], WF(4, 1, 0), X[4][1][2]); SB();
  X[6][0][2] = *LDSP(bf16x8, a0 + 24576);
  SB();
  MA1(acc[0][3], WF(4, 1, 0), X[4][1][3]); SB();
  X[6][0][3] = *LDSP(bf16x8, a0 + 26624);
  DMA(m0a, 143360, vw0, srdW, sw[7], 1152);
  SB();
  MA1(acc[0][4], WF(4, 1, 0), X[4][1][4]); SB();
  X[6][0][4] = *LDSP(bf16x8, a0 + 28672);
  SB();
  MA1(acc[1][0], WF(4, 1, 1), X[4][1][0]); SB();
  X[6][1][0] = *LDSP(bf16x8, a1 + 20480);
  SB();
  MA1(acc[1][1], WF(4, 1, 1), X[4][1][1]); SB();
  X[6][1][1] = *LDSP(bf16x8, a1 + 22528);
  SB();
  MA1(acc[1][2], WF(4, 1, 1), X[4][1][2]); SB();
  X[6][1][2] = *LDSP(bf16x8, a1 + 24576);
  SB();
  MA1(acc[1][3], WF(4, 1, 1), X[4][1][3]); SB();
  X[6][1][3] = *LDSP(bf16x8, a1 + 26624);
  DMA(m0a, 152576, vw0, srdW, sw[7], 2304);
  SB();
  MA1(acc[1][4], WF(4, 1, 1), X[4][1][4]); SB();
  X[6][1][4] = *LDSP(bf16x8, a1 + 28672);
  SB();
  MA1(acc[2][0], on8, X[6][0][0]); SB();
  Wh[5][0][0][0] = __builtin_amdgcn_ds_read_tr16_b64_v4i16(LDSP(s16x4, aw[0] + 27648));
  SB();
  MX1(acc[3][0], X[6][0][0]); SB();
  Wh[5][0][0][1] = __builtin_amdgcn_ds_read_tr16_b64_v4i16(LDSP(s16x4, aw[0] + 29952));
  DMAX(m0a, 41984, vxa, srdX, kx[8]);
  SB();
  MA1(acc[2][1], on8, X[6][0][1]); SB();
  Wh[5][0][1][0] = __builtin_amdgcn_ds_read_tr16_b64_v4i16(LDSP(s16x4, aw[1] + 27648));
  SB();
  MX1(acc[3][1], X[6][0][1]); SB();
  Wh[5][0][1][1] = __builtin_amdgcn_ds_read_tr16_b64_v4i16(LDSP(s16x4, aw[1] + 29952));
  SB();
  MA1(acc[2][2], on8, X[6][0][2]); SB();
  Wh[5][1][0][0] = __builtin_amdgcn_ds_read_tr16_b64_v4i16(LDSP(s16x4, aw[0] + 32256));
  SB();
  MX1(acc[3][2], X[6][0][2]); SB();
  Wh[5][1][0][1] = __builtin_amdgcn_ds_read_tr16_b64_v4i16(LDSP(s16x4, aw[0] + 34560));
  SB();
  MA1(acc[2][3], on8, X[6][0][3]); SB();
  Wh[5][1][1][0] = __builtin_amdgcn_ds_read_tr16_b64_v4i16(LDSP(s16x4, aw[1] + 32256));
  DMAX(m0a, 138240, vw1, srdW, sw[7]);
  SB();
  MX1(acc[3][3], X[6][0][3]); SB();
  Wh[5][1][1][1] = __builtin_amdgcn_ds_read_tr16_b64_v4i16(LDSP(s16x4, aw[1] + 34560));
  SB();
  MA1(acc[2][4], on8, X[6][0][4]); SB();
  SB();
  MX1(acc[3][4], X[6][0][4]); SB();
  SB();
  MA1(acc[2][0], on8, X[6][1][0]); SB();
  DMA(m0a, 147456, vw1, srdW, sw[7], 1152);
  SB();
  MX1(acc[3][0], X[6][1][0]); SB();
  SB();
  MA1(acc[2][1], on8, X[6][1][1]); SB();
  SB();
  MX1(acc[3][1], X[6][1][1]); SB();
  DMA(m0a, 156672, vw1, srdW, sw[7], 2304);
  SB();
  MA1(acc[2][2], on8, X[6][1][2]); SB();
  SB();
  MX1(acc[3][2], X[6][1][2]); SB();
  SB();
  MA1(acc[2][3], on8, X[6][1][3]); SB();
  SB();
  MX1(acc[3][3], X[6][1][3]); SB();
  SB();
  MA1(acc[2][4], on8, X[6][1][4]); SB();
  DMAX(m0a, 46080, vxb, srdX, kx[8]);
  SB();
  MX1(acc[3][4], X[6][1][4]); SB();
  SB();
  MA1(acc[0][0], WF(5, 0, 0), X[5][0][0]); SB();
  SB();
  MA1(acc[0][1], WF(5, 0, 0), X[5][0][1]); SB();
  DMAM(40960, vz, srdX, kx[8]);
  SB();
  MA1(acc[0][2], WF(5, 0, 0), X[5][0][2]); SB();
  SB();
  MA1(acc[0][3], WF(5, 0, 0), X[5][0][3]); SB();
  SB();
  MA1(acc[0][4], WF(5, 0, 0), X[5][0][4]); SB();
  SB();
  MA1(acc[1][0], WF(5, 0, 1), X[5][0][0]); SB();
  SB();
  MA1(acc[1][1], WF(5, 0, 1), X[5][0][1]); SB();
  DMAI(50176, vy, srdX, kx[8]);
  SB();
  MA1(acc[1][2], WF(5, 0, 1), X[5][0][2]); SB();
  SB();
  MA1(acc[1][3], WF(5, 0, 1), X[5][0][3]); SB();
  SB();
  MA1(acc[1][4], WF(5, 0, 1), X[5][0][4]); SB();
  SB();
  VMW(10);
  LGW(); BAR();
  STAMP(9, 8);
  MA1(acc[0][0], WF(5, 1, 0), X[5][1][0]); SB();
  X[7][0][0] = *LDSP(bf16x8, a0 + 30720);
  KNEXT(kx[9], sw[8], kx[8]);
  DMAX(m0a, 51200, vw0, srdW, sw[8]);
  SB();
  MA1(acc[0][1], WF(5, 1, 0), X[5][1][1]); SB();
  X[7][0][1] = *LDSP(bf16x8, a0 + 32768);
  SB();
  MA1(acc[0][2], WF(5, 1, 0), X[5][1][2]); SB();
  X[7][0][2] = *LDSP(bf16x8, a0 + 34816);
  SB();
  MA1(acc[0][3], WF(5, 1, 0), X[5][1][3]); SB();
  X[7][0][3] = *LDSP(bf16x8, a0 + 36864);
  SB();
  MA1(acc[0][4], WF(5, 1, 0), X[5][1][4]); SB();
  X[7][0][4] = *LDSP(bf16x8, a0 + 38912);
  SB();
  MA1(acc[1][0], WF(5, 1, 1), X[5][1][0]); SB();
  X[7][1][0] = *LDSP(bf16x8, a1 + 30720);
  SB();
  MA1(acc[1][1], WF(5, 1, 1), X[5][1][1]); SB();
  X[7][1][1] = *LDSP(bf16x8, a1 + 32768);
  DMA(m0a, 60416, vw0, srdW, sw[8], 1152);
  SB();
  MA1(acc[1][2], WF(5, 1, 1), X[5][1][2]); SB();
  X[7][1][2] = *LDSP(bf16x8, a1 + 34816);
  SB();
  MA1(acc[1][3], WF(5, 1, 1), X[5][1][3]); SB();
  X[7][1][3] = *LDSP(bf16x8, a1 + 36864);
  SB();
  MA1(acc[1][4], WF(5, 1, 1), X[5][1][4]); SB();
  X[7][1][4] = *LDSP(bf16x8, a1 + 38912);
  SB();
  MA1(acc[2][0], on8, X[7][0][0]); SB();
  Wh[6][0][0][0] = __builtin_amdgcn_ds_read_tr16_b64_v4i16(LDSP(s16x4, av[0] + 0));
  SB();
  MX1(acc[3][0], X[7][0][0]); SB();
  Wh[6][0][0][1] = __builtin_amdgcn_ds_read_tr16_b64_v4i16(LDSP(s16x4, av[0] + 2304));
  u32 p7; BLD4C(p7, vb[9], srdS, 0, 1792);
  SB();
  MA1(acc[2][1], on8, X[7][0][1]); SB();
  Wh[6][0][1][0] = __builtin_amdgcn_ds_read_tr16_b64_v4i16(LDSP(s16x4, av[1] + 0));
  SB();
  MX1(acc[3][1], X[7][0][1]); SB();
  Wh[6][0][1][1] = __builtin_amdgcn_ds_read_tr16_b64_v4i16(LDSP(s16x4, av[1] + 2304));
  DMA(m0a, 69632, vw0, srdW, sw[8], 2304);
  SB();
  MA1(acc[2][2], on8, X[7][0][2]); SB();
  Wh[6][1][0][0] = __builtin_amdgcn_ds_read_tr16_b64_v4i16(LDSP(s16x4, av[0] + 4608));
  SB();
  MX1(acc[3][2], X[7][0][2]); SB();
  Wh[6][1][0][1] = __builtin_amdgcn_ds_read_tr16_b64_v4i16(LDSP(s16x4, av[0] + 6912));
  SB();
  MA1(acc[2][3], on8, X[7][0][3]); SB();
  Wh[6][1][1][0] = __builtin_amdgcn_ds_read_tr16_b64_v4i16(LDSP(s16x4, av[1] + 4608));
  SB();
  MX1(acc[3][3], X[7][0][3]); SB();
  Wh[6][1][1][1] = __builtin_amdgcn_ds_read_tr16_b64_v4i16(LDSP(s16x4, av[1] + 6912));
  SB();
  MA1(acc[2][4], on8, X[7][0][4]); SB();
  SB();
  MX1(acc[3][4], X[7][0][4]); SB();
  SB();
  MA1(acc[2][0], on8, X[7][1][0]); SB();
  DMAX(m0a, 55296, vw1, srdW, sw[8]);
  SB();
  MX1(acc[3][0], X[7][1][0]); SB();
  SB();
  MA1(acc[2][1], on8, X[7][1][1]); SB();
  SB();
  MX1(acc[3][1], X[7][1][1]); SB();
  SB();
  MA1(acc[2][2], on8, X[7][1][2]); SB();
  SB();
  MX1(acc[3][2], X[7][1][2]); SB();
  SB();
  MA1(acc[2][3], on8, X[7][1][3]); SB();
  DMA(m0a, 64512, vw1, srdW, sw[8], 1152);
  SB();
  MX1(acc[3][3], X[7][1][3]); SB();
  SB();
  MA1(acc[2][4], on8, X[7][1][4]); SB();
  SB();
  MX1(acc[3][4], X[7][1][4]); SB();
  SB();
  MA1(acc[0][0], WF(6, 0, 0), X[6][0][0]); SB();
  SB();
  MA1(acc[0][1], WF(6, 0, 0), X[6][0][1]); SB();
  SB();
  MA1(acc[0][2], WF(6, 0, 0), X[6][0][2]); SB();
  SB();
  MA1(acc[0][3], WF(6, 0, 0), X[6][0][3]); SB();
  DMA(m0a, 73728, vw1, srdW, sw[8], 2304);
  SB();
  MA1(acc[0][4], WF(6, 0, 0), X[6][0][4]); SB();
  SB();
  MA1(acc[1][0], WF(6, 0, 1), X[6][0][0]); SB();
  SB();
  MA1(acc[1][1], WF(6, 0, 1), X[6][0][1]); SB();
  SB();
  MA1(acc[1][2], WF(6, 0, 1), X[6][0][2]); SB();
  SB();
  MA1(acc[1][3], WF(6, 0, 1), X[6][0][3]); SB();
  SB();
  MA1(acc[1][4], WF(6, 0, 1), X[6][0][4]); SB();
  SB();
  VMW(7);
  LGW(); BAR();
  STAMP(10, 9);
  MA1(acc[0][0], WF(6, 1, 0), X[6][1][0]); SB();
  X[8][0][0] = *LDSP(bf16x8, a0 + 40960);
  SB();
  MA1(acc[0][1], WF(6, 1, 0), X[6][1][1]); SB();
  X[8][0][1] = *LDSP(bf16x8, a0 + 43008);
  SB();
  MA1(acc[0][2], WF(6, 1, 0), X[6][1][2]); SB();
  X[8][0][2] = *LDSP(bf16x8, a0 + 45056);
  SB();
  MA1(acc[0][3], WF(6, 1, 0), X[6][1][3]); SB();
  X[8][0][3] = *LDSP(bf16x8, a0 + 47104);
  SB();
  MA1(acc[0][4], WF(6, 1, 0), X[6][1][4]); SB();
  X[8][0][4] = *LDSP(bf16x8, a0 + 49152);
  DMA(m0a, 78848, vb[7], srdO, hb, 0);
  SB();
  MA1(acc[1][0], WF(6, 1, 1), X[6][1][0]); SB();
  X[8][1][0] = *LDSP(bf16x8, a1 + 40960);
  SB();
  MA1(acc[1][1], WF(6, 1, 1), X[6][1][1]); SB();
  X[8][1][1] = *LDSP(bf16x8, a1 + 43008);
  SB();
  MA1(acc[1][2], WF(6, 1, 1), X[6][1][2]); SB();
  X[8][1][2] = *LDSP(bf16x8, a1 + 45056);
  SB();
  MA1(acc[1][3], WF(6, 1, 1), X[6][1][3]); SB();
  X[8][1][3] = *LDSP(bf16x8, a1 + 47104);
  SB();
  MA1(acc[1][4], WF(6, 1, 1), X[6][1][4]); SB();
  X[8][1][4] = *LDSP(bf16x8, a1 + 49152);
  DMA(m0a, 82944, vb[8], srdO, hb, 0);
  SB();
  MA1(acc[2][0], on8, X[8][0][0]); SB();
  Wh[7][0][0][0] = __builtin_amdgcn_ds_read_tr16_b64_v4i16(LDSP(s16x4, av[0] + 27648));
  SB();
  MX1(acc[3][0], X[8][0][0]); SB();
  Wh[7][0][0][1] = __builtin_amdgcn_ds_read_tr16_b64_v4i16(LDSP(s16x4, av[0] + 29952));
  SB();
  MA1(acc[2][1], on8, X[8][0][1]); SB();
  Wh[7][0][1][0] = __builtin_amdgcn_ds_read_tr16_b64_v4i16(LDSP(s16x4, av[1] + 27648));
  SB();
  MX1(acc[3][1], X[8][0][1]); SB();
  Wh[7][0][1][1] = __builtin_amdgcn_ds_read_tr16_b64_v4i16(LDSP(s16x4, av[1] + 29952));
  SB();
  MA1(acc[2][2], on8, X[8][0][2]); SB();
  Wh[7][1][0][0] = __builtin_amdgcn_ds_read_tr16_b64_v4i16(LDSP(s16x4, av[0] + 32256));
  DMA(m0a, 88064, vb[5], srdO, hb, 0);
  VMW(7);
  { u32 n_; asm volatile("s_mov_b32 %1, 200000\n1:\n\tv_cmp_ne_u32 vcc, %2, %0\n\ts_cbranch_vccz 2f\n\tbuffer_load_dword %0, %3, %4, 0 offen offset:1792 sc1\n\ts_waitcnt vmcnt(0)\n\ts_sub_u32 %1, %1, 1\n\ts_cbranch_scc0 1b\n2:"
      : "+v"(p7), "=&s"(n_) : "v"(tag), "v"(vb[9]), "s"(srdS) : "vcc", "scc", "memory"); }
  DMT(0, vL, srdS, (u32)cfg::WS_WBAR + (tt >> 2) * 4096);
  DMT(1024, vL, srdS, (u32)cfg::WS_WBAR + (tt >> 2) * 4096);
  DMT(2048, vL, srdS, (u32)cfg::WS_WBAR + (tt >> 2) * 4096);
  SB();
  MX1(acc[3][2], X[8][0][2]); SB();
  Wh[7][1][0][1] = __builtin_amdgcn_ds_read_tr16_b64_v4i16(LDSP(s16x4, av[0] + 34560));
  SB();
  MA1(acc[2][3], on8, X[8][0][3]); SB();
  Wh[7][1][1][0] = __builtin_amdgcn_ds_read_tr16_b64_v4i16(LDSP(s16x4, av[1] + 32256));
  SB();
  MX1(acc[3][3], X[8][0][3]); SB();
  Wh[7][1][1][1] = __builtin_amdgcn_ds_read_tr16_b64_v4i16(LDSP(s16x4, av[1] + 34560));
  SB();
  MA1(acc[2][4], on8, X[8][0][4]); SB();
  SB();
  MX1(acc[3][4], X[8][0][4]); SB();
  DMA(m0a, 92160, vb[6], srdO, hb, 0);
  SB();
  MA1(acc[2][0], on8, X[8][1][0]); SB();
  SB();
  MX1(acc[3][0], X[8][1][0]); SB();
  SB();
  MA1(acc[2][1], on8, X[8][1][1]); SB();
  SB();
  MX1(acc[3][1], X[8][1][1]); SB();
  SB();
  MA1(acc[2][2], on8, X[8][1][2]); SB();
  DMA(m0a, 97280, vb[5], srdO, hb, 82944);
  SB();
  MX1(acc[3][2], X[8][1][2]); SB();
  SB();
  MA1(acc[2][3], on8, X[8][1][3]); SB();
  SB();
  MX1(acc[3][3], X[8][1][3]); SB();
  SB();
  MA1(acc[2][4], on8, X[8][1][4]); SB();
  SB();
  MX1(acc[3][4], X[8][1][4]); SB();
  DMA(m0a, 101376, vb[6], srdO, hb, 82944);
  SB();
  MA1(acc[0][0], WF(7, 0, 0), X[7][0][0]); SB();
  SB();
  MA1(acc[0][1], WF(7, 0, 0), X[7][0][1]); SB();
  { const float mean = acc[2][0][0] * (1.f / 576.f);
      const float ss = (n & 2) ? ((n & 1) ? acc[3][0][3] : acc[3][0][2]) : ((n & 1) ? acc[3][0][1] : acc[3][0][0]);
      const float var = fmaxf(ss * (1.f / 576.f) - mean * mean, 0.f), r = __builtin_amdgcn_rsqf(var + 1e-5f);
      *LDSP(f32x2, as + 0) = f32x2{r, -mean}; *LDSP(float, 162432 + n * 4) = mean; }
  SB();
  MA1(acc[0][2], WF(7, 0, 0), X[7][0][2]); SB();
  DMAZ(mxr, vb[11], srdX, hb, (u32x2{511u, 0u}));
  SB();
  MA1(acc[0][3], WF(7, 0, 0), X[7][0][3]); SB();
  { const float mean = acc[2][1][0] * (1.f / 576.f);
      const float ss = (n & 2) ? ((n & 1) ? acc[3][1][3] : acc[3][1][2]) : ((n & 1) ? acc[3][1][1] : acc[3][1][0]);
      const float var = fmaxf(ss * (1.f / 576.f) - mean * mean, 0.f), r = __builtin_amdgcn_rsqf(var + 1e-5f);
      *LDSP(f32x2, as + 128) = f32x2{r, -mean}; *LDSP(float, 162496 + n * 4) = mean; }
  SB();
  MA1(acc[0][4], WF(7, 0, 0), X[7][0][4]); SB();
  SB();
  MA1(acc[1][0], WF(7, 0, 1), X[7][0][0]); SB();
  { const float mean = acc[2][2][0] * (1.f / 576.f);
      const float ss = (n & 2) ? ((n & 1) ? acc[3][2][3] : acc[3][2][2]) : ((n & 1) ? acc[3][2][1] : acc[3][2][0]);
      const float var = fmaxf(ss * (1.f / 576.f) - mean * mean, 0.f), r = __builtin_amdgcn_rsqf(var + 1e-5f);
      *LDSP(f32x2, as + 256) = f32x2{r, -mean}; *LDSP(float, 162560 + n * 4) = mean; }
  SB();
  MA1(acc[1][1], WF(7, 0, 1), X[7][0][1]); SB();
  SB();
  MA1(acc[1][2], WF(7, 0, 1), X[7][0][2]); SB();
  { const float mean = acc[2][3][0] * (1.f / 576.f);
      const float ss = (n & 2) ? ((n & 1) ? acc[3][3][3] : acc[3][3][2]) : ((n & 1) ? acc[3][3][1] : acc[3][3][0]);
      const float var = fmaxf(ss * (1.f / 576.f) - mean * mean, 0.f), r = __builtin_amdgcn_rsqf(var + 1e-5f);
      *LDSP(f32x2, as + 384) = f32x2{r, -mean}; *LDSP(float, 162624 + n * 4) = mean; }
  SB();
  MA1(acc[1][3], WF(7, 0, 1), X[7][0][3]); SB();
  SB();
  MA1(acc[1][4], WF(7, 0, 1), X[7][0][4]); SB();
  { const float mean = acc[2][4][0] * (1.f / 576.f);
      const float ss = (n & 2) ? ((n & 1) ? acc[3][4][3] : acc[3][4][2]) : ((n & 1) ? acc[3][4][1] : acc[3][4][0]);
      const float var = fmaxf(ss * (1.f / 576.f) - mean * mean, 0.f), r = __builtin_amdgcn_rsqf(var + 1e-5f);
      *LDSP(f32x2, as + 512) = f32x2{r, -mean}; *LDSP(float, 162688 + n * 4) = mean; }
  SB();
  MA1(acc[0][0], WF(7, 1, 0), X[7][1][0]); SB();
  SB();
  MA1(acc[0][1], WF(7, 1, 0), X[7][1][1]); SB();
  SB();
  MA1(acc[0][2], WF(7, 1, 0), X[7][1][2]); SB();
  SB();
  MA1(acc[0][3], WF(7, 1, 0), X[7][1][3]); SB();
  SB();
  MA1(acc[0][4], WF(7, 1, 0), X[7][1][4]); SB();
  SB();
  MA1(acc[1][0], WF(7, 1, 1), X[7][1][0]); SB();
  SB();
  MA1(acc[1][1], WF(7, 1, 1), X[7][1][1]); SB();
  SB();
  MA1(acc[1][2], WF(7, 1, 1), X[7][1][2]); SB();
  SB();
  MA1(acc[1][3], WF(7, 1, 1), X[7][1][3]); SB();
  SB();
  MA1(acc[1][4], WF(7, 1, 1), X[7][1][4]); SB();
  SB();
  VMW(10);
  LGW(); BAR();
  STAMP(11, 10);
  PIN(tpc); PIN(tpi); PIN(tp1); PIN(tp2); f32x2 st[5];
  Wh[8][0][0][0] = __builtin_amdgcn_ds_read_tr16_b64_v4i16(LDSP(s16x4, aw[0] + 0));
  Wh[8][0][0][1] = __builtin_amdgcn_ds_read_tr16_b64_v4i16(LDSP(s16x4, aw[0] + 2304));
  Wh[8][1][0][0] = __builtin_amdgcn_ds_read_tr16_b64_v4i16(LDSP(s16x4, aw[0] + 4608));
  Wh[8][1][0][1] = __builtin_amdgcn_ds_read_tr16_b64_v4i16(LDSP(s16x4, aw[0] + 6912));
  SB();
  MA1(acc[0][0], WF(8, 0, 0), X[8][0][0]); SB();
  Wh[8][0][1][0] = __builtin_amdgcn_ds_read_tr16_b64_v4i16(LDSP(s16x4, aw[1] + 0));
  BLV(vp[6], srdS, 0);
  BLV(vp[4], srdS, 0);
  BLV(vp[4], srdS, 256);
  BLV(vL * 16 | OOB(l < 9), srdS, (u32)cfg::WS_FLAGS + tt * cfg::FST);
  BLW();
  SB();
  MA1(acc[0][1], WF(8, 0, 0), X[8][0][1]); SB();
  Wh[8][0][1][1] = __builtin_amdgcn_ds_read_tr16_b64_v4i16(LDSP(s16x4, aw[1] + 2304));
  SB();
  MA1(acc[0][2], WF(8, 0, 0), X[8][0][2]); SB();
  Wh[8][1][1][0] = __builtin_amdgcn_ds_read_tr16_b64_v4i16(LDSP(s16x4, aw[1] + 4608));
  SB();
  MA1(acc[0][3], WF(8, 0, 0), X[8][0][3]); SB();
  Wh[8][1][1][1] = __builtin_amdgcn_ds_read_tr16_b64_v4i16(LDSP(s16x4, aw[1] + 6912));
  SB();
  MA1(acc[0][4], WF(8, 0, 0), X[8][0][4]); SB();
  for (int k = 0; k < 5; ++k) st[k] = *LDSP(f32x2, 161792 + (k * 16 + n) * 8);
  SB();
  MA1(acc[0][0], WF(8, 1, 0), X[8][1][0]); SB();
  SB();
  MA1(acc[0][1], WF(8, 1, 0), X[8][1][1]); SB();
  SB();
  MA1(acc[0][2], WF(8, 1, 0), X[8][1][2]); SB();
  SB();
  MA1(acc[0][3], WF(8, 1, 0), X[8][1][3]); SB();
  SB();
  MA1(acc[0][4], WF(8, 1, 0), X[8][1][4]); SB();
  SB();
  MA1(acc[1][0], WF(8, 0, 1), X[8][0][0]); SB();
  SB();
  MA1(acc[1][1], WF(8, 0, 1), X[8][0][1]); SB();
  EPI0(FD1, acc[0][1], acc[0][0], st[1], ae[0], 1280);
  SB();
  MA1(acc[1][2], WF(8, 0, 1), X[8][0][2]); SB();
  SB();
  MA1(acc[1][3], WF(8, 0, 1), X[8][0][3]); SB();
  EPI0(FD1, acc[0][2], acc[0][0], st[2], ae[0], 2560);
  SB();
  MA1(acc[1][4], WF(8, 0, 1), X[8][0][4]); SB();
  SB();
  MA1(acc[1][0], WF(8, 1, 1), X[8][1][0]); SB();
  EPI0(FD1, acc[0][3], acc[0][0], st[3], ae[0], 3840);
  SB();
  MA1(acc[1][1], WF(8, 1, 1), X[8][1][1]); SB();
  SB();
  MA1(acc[1][2], WF(8, 1, 1), X[8][1][2]); SB();
  EPI0(FD1, acc[0][4], acc[0][0], st[4], ae[0], 5120);
  SB();
  MA1(acc[1][3], WF(8, 1, 1), X[8][1][3]); SB();
  SB();
  MA1(acc[1][4], WF(8, 1, 1), X[8][1][4]); SB();
  EPI0(FD0, acc[0][0], acc[0][0], st[0], ae[0], 0);
  SB();
  NOP(4); SB();
  EPI1(FD1, acc[1][1], acc[1][0], st[1], ae[1], 1280); SB();
  EPI1(FD1, acc[1][2], acc[1][0], st[2], ae[1], 2560); SB();
  EPI1(FD1, acc[1][3], acc[1][0], st[3], ae[1], 3840); SB();
  EPI1(FD1, acc[1][4], acc[1][0], st[4], ae[1], 5120); SB();
  EPI1(FD0, acc[1][0], acc[1][0], st[0], ae[1], 0); SB();
  STAMPF(11);
  } else {
  VMW(10);
  LGW(); BAR();
  STAMP(3, 2);
  Wh[0][0][0][0] = __builtin_amdgcn_ds_read_tr16_b64_v4i16(LDSP(s16x4, aw[0] + 0));
  Wh[0][0][0][1] = __builtin_amdgcn_ds_read_tr16_b64_v4i16(LDSP(s16x4, aw[0] + 2304));
  Wh[0][0][1][0] = __builtin_amdgcn_ds_read_tr16_b64_v4i16(LDSP(s16x4, aw[1] + 0));
  Wh[0][0][1][1] = __builtin_amdgcn_ds_read_tr16_b64_v4i16(LDSP(s16x4, aw[1] + 2304));
  Wh[0][0][2][0] = __builtin_amdgcn_ds_read_tr16_b64_v4i16(LDSP(s16x4, aw[2] + 0));
  Wh[0][0][2][1] = __builtin_amdgcn_ds_read_tr16_b64_v4i16(LDSP(s16x4, aw[2] + 2304));
  Wh[0][0][3][0] = __builtin_amdgcn_ds_read_tr16_b64_v4i16(LDSP(s16x4, aw[3] + 0));
  Wh[0][0][3][1] = __builtin_amdgcn_ds_read_tr16_b64_v4i16(LDSP(s16x4, aw[3] + 2304));
  X[0][0][0] = *LDSP(bf16x8, a0 + 10240);
  X[0][0][1] = *LDSP(bf16x8, a0 + 12288);
  X[0][0][2] = *LDSP(bf16x8, a0 + 14336);
  X[0][0][3] = *LDSP(bf16x8, a0 + 16384);
  X[0][0][4] = *LDSP(bf16x8, a0 + 18432);
  SB();
  MA0(acc[0][0], WF(0, 0, 0), X[0][0][0]); SB();
  Wh[0][1][0][0] = __builtin_amdgcn_ds_read_tr16_b64_v4i16(LDSP(s16x4, aw[0] + 4608));
  Wh[0][1][0][1] = __builtin_amdgcn_ds_read_tr16_b64_v4i16(LDSP(s16x4, aw[0] + 6912));
  KNEXT(kx[3], sw[2], kx[2]);
  DMAX(m0a, 106496, vw0, srdW, sw[2]);
  const u32 rb = ((tt & 3) * 8 + hg) * 18 + w * 6 + (l >> 3), kc = rb >> 5, qc = (rb >> 3) & 3, jc = rb & 7;
    u32 ca = 36 * kc + 20 * (jc >> 2) + 4 * qc, cb = 36 * (8 * (kc - 16) + 2 * qc + (jc >> 2)) + 16; PIN(ca); PIN(cb);
    const u32 vC = ((kc < 16 ? ca : cb) + (jc & 3)) * 1152 + (l & 7) * 16 | OOB(l < 48);
    u32x4 rc[9]; float ra = 0.f, rb2 = 0.f;
  SB();
  MA0(acc[0][1], WF(0, 0, 0), X[0][0][1]); SB();
  Wh[0][1][1][0] = __builtin_amdgcn_ds_read_tr16_b64_v4i16(LDSP(s16x4, aw[1] + 4608));
  Wh[0][1][1][1] = __builtin_amdgcn_ds_read_tr16_b64_v4i16(LDSP(s16x4, aw[1] + 6912));
  BLD16(rc[0], vC, srdO, 0, 0);
  SB();
  MA0(acc[0][2], WF(0, 0, 0), X[0][0][2]); SB();
  Wh[0][1][2][0] = __builtin_amdgcn_ds_read_tr16_b64_v4i16(LDSP(s16x4, aw[2] + 4608));
  Wh[0][1][2][1] = __builtin_amdgcn_ds_read_tr16_b64_v4i16(LDSP(s16x4, aw[2] + 6912));
  DMA(m0a, 115712, vw0, srdW, sw[2], 1152);
  SB();
  MA0(acc[0][3], WF(0, 0, 0), X[0][0][3]); SB();
  Wh[0][1][3][0] = __builtin_amdgcn_ds_read_tr16_b64_v4i16(LDSP(s16x4, aw[3] + 4608));
  Wh[0][1][3][1] = __builtin_amdgcn_ds_read_tr16_b64_v4i16(LDSP(s16x4, aw[3] + 6912));
  BLD16(rc[1], vC, srdO, 0, 128);
  SB();
  MA0(acc[0][4], WF(0, 0, 0), X[0][0][4]); SB();
  X[0][1][0] = *LDSP(bf16x8, a1 + 10240);
  X[0][1][1] = *LDSP(bf16x8, a1 + 12288);
  DMA(m0a, 124928, vw0, srdW, sw[2], 2304);
  SB();
  MA0(acc[1][0], WF(0, 0, 1), X[0][0][0]); SB();
  X[0][1][2] = *LDSP(bf16x8, a1 + 14336);
  X[0][1][3] = *LDSP(bf16x8, a1 + 16384);
  BLD16(rc[2], vC, srdO, 0, 256);
  SB();
  MA0(acc[1][1], WF(0, 0, 1), X[0][0][1]); SB();
  X[0][1][4] = *LDSP(bf16x8, a1 + 18432);
  X[1][0][0] = *LDSP(bf16x8, a0 + 20480);
  DMAX(m0a, 41984, vxa, srdX, kx[3]);
  SB();
  MA0(acc[1][2], WF(0, 0, 1), X[0][0][2]); SB();
  X[1][0][1] = *LDSP(bf16x8, a0 + 22528);
  X[1][0][2] = *LDSP(bf16x8, a0 + 24576);
  BLD16(rc[3], vC, srdO, 0, 384);
  SB();
  MA0(acc[1][3], WF(0, 0, 1), X[0][0][3]); SB();
  X[1][0][3] = *LDSP(bf16x8, a0 + 26624);
  X[1][0][4] = *LDSP(bf16x8, a0 + 28672);
  DMAX(m0a, 110592, vw1, srdW, sw[2]);
  SB();
  MA0(acc[1][4], WF(0, 0, 1), X[0][0][4]); SB();
  X[1][1][0] = *LDSP(bf16x8, a1 + 20480);
  X[1][1][1] = *LDSP(bf16x8, a1 + 22528);
  SB();
  MA0(acc[2][0], WF(0, 0, 2), X[0][0][0]); SB();
  X[1][1][2] = *LDSP(bf16x8, a1 + 24576);
  X[1][1][3] = *LDSP(bf16x8, a1 + 26624);
  BLD16(rc[4], vC, srdO, 0, 512);
  SB();
  MA0(acc[2][1], WF(0, 0, 2), X[0][0][1]); SB();
  X[1][1][4] = *LDSP(bf16x8, a1 + 28672);
  DMA(m0a, 119808, vw1, srdW, sw[2], 1152);
  SB();
  MA0(acc[2][2], WF(0, 0, 2), X[0][0][2]); SB();
  BLD16(rc[5], vC, srdO, 0, 640);
  SB();
  MA0(acc[2][3], WF(0, 0, 2), X[0][0][3]); SB();
  DMA(m0a, 129024, vw1, srdW, sw[2], 2304);
  SB();
  MA0(acc[2][4], WF(0, 0, 2), X[0][0][4]); SB();
  BLD16(rc[6], vC, srdO, 0, 768);
  SB();
  MA0(acc[3][0], WF(0, 0, 3), X[0][0][0]); SB();
  DMAX(m0a, 46080, vxb, srdX, kx[3]);
  SB();
  MA0(acc[3][1], WF(0, 0, 3), X[0][0][1]); SB();
  BLD16(rc[7], vC, srdO, 0, 896);
  SB();
  MA0(acc[3][2], WF(0, 0, 3), X[0][0][2]); SB();
  DMAX(m0b, 114688, vz, srdW, sw[2]);
  SB();
  MA0(acc[3][3], WF(0, 0, 3), X[0][0][3]); SB();
  BLD16(rc[8], vC, srdO, 0, 1024);
  SB();
  MA0(acc[3][4], WF(0, 0, 3), X[0][0][4]); SB();
  SB();
  VMW(18);
  LGW(); BAR();
  STAMP(4, 3);
  MA1(acc[0][0], WF(0, 1, 0), X[0][1][0]); SB();
  Wh[1][0][0][0] = __builtin_amdgcn_ds_read_tr16_b64_v4i16(LDSP(s16x4, aw[0] + 27648));
  KNEXT(kx[4], sw[3], kx[3]);
  DMAX(m0a, 134144, vw0, srdW, sw[3]);
  SB();
  MA1(acc[0][1], WF(0, 1, 0), X[0][1][1]); SB();
  Wh[1][0][0][1] = __builtin_amdgcn_ds_read_tr16_b64_v4i16(LDSP(s16x4, aw[0] + 29952));
  SB();
  MA1(acc[0][2], WF(0, 1, 0), X[0][1][2]); SB();
  Wh[1][0][1][0] = __builtin_amdgcn_ds_read_tr16_b64_v4i16(LDSP(s16x4, aw[1] + 27648));
  SB();
  MA1(acc[0][3], WF(0, 1, 0), X[0][1][3]); SB();
  Wh[1][0][1][1] = __builtin_amdgcn_ds_read_tr16_b64_v4i16(LDSP(s16x4, aw[1] + 29952));
  SB();
  MA1(acc[0][4], WF(0, 1, 0), X[0][1][4]); SB();
  Wh[1][0][2][0] = __builtin_amdgcn_ds_read_tr16_b64_v4i16(LDSP(s16x4, aw[2] + 27648));
  DMA(m0a, 143360, vw0, srdW, sw[3], 1152);
  SB();
  MA1(acc[1][0], WF(0, 1, 1), X[0][1][0]); SB();
  Wh[1][0][2][1] = __builtin_amdgcn_ds_read_tr16_b64_v4i16(LDSP(s16x4, aw[2] + 29952));
  SB();
  MA1(acc[1][1], WF(0, 1, 1), X[0][1][1]); SB();
  Wh[1][0][3][0] = __builtin_amdgcn_ds_read_tr16_b64_v4i16(LDSP(s16x4, aw[3] + 27648));
  SB();
  MA1(acc[1][2], WF(0, 1, 1), X[0][1][2]); SB();
  Wh[1][0][3][1] = __builtin_amdgcn_ds_read_tr16_b64_v4i16(LDSP(s16x4, aw[3] + 29952));
  SB();
  MA1(acc[1][3], WF(0, 1, 1), X[0][1][3]); SB();
  X[2][0][0] = *LDSP(bf16x8, a0 + 30720);
  DMA(m0a, 152576, vw0, srdW, sw[3], 2304);
  SB();
  MA1(acc[1][4], WF(0, 1, 1), X[0][1][4]); SB();
  X[2][0][1] = *LDSP(bf16x8, a0 + 32768);
  SB();
  MA1(acc[2][0], WF(0, 1, 2), X[0][1][0]); SB();
  X[2][0][2] = *LDSP(bf16x8, a0 + 34816);
  SB();
  MA1(acc[2][1], WF(0, 1, 2), X[0][1][1]); SB();
  X[2][0][3] = *LDSP(bf16x8, a0 + 36864);
  SB();
  MA1(acc[2][2], WF(0, 1, 2), X[0][1][2]); SB();
  X[2][0][4] = *LDSP(bf16x8, a0 + 38912);
  SB();
  MA1(acc[2][3], WF(0, 1, 2), X[0][1][3]); SB();
  DMAX(m0a, 1024, vxa, srdX, kx[4]);
  SB();
  MA1(acc[2][4], WF(0, 1, 2), X[0][1][4]); SB();
  SB();
  MA1(acc[3][0], WF(0, 1, 3), X[0][1][0]); SB();
  SB();
  MA1(acc[3][1], WF(0, 1, 3), X[0][1][1]); SB();
  SB();
  MA1(acc[3][2], WF(0, 1, 3), X[0][1][2]); SB();
  DMAX(m0a, 138240, vw1, srdW, sw[3]);
  SB();
  MA1(acc[3][3], WF(0, 1, 3), X[0][1][3]); SB();
  SB();
  MA1(acc[3][4], WF(0, 1, 3), X[0][1][4]); SB();
  SB();
  MA1(acc[0][0], WF(1, 0, 0), X[1][0][0]); SB();
  Wh[1][1][0][0] = __builtin_amdgcn_ds_read_tr16_b64_v4i16(LDSP(s16x4, aw[0] + 32256));
  { u32x4 m2 = {~0u, ~0u, ~0u, ~0u}; PIN(m2); BST16(m2, vL, srdS, sd[2], 0, "sc1"); }
  SB();
  MA1(acc[0][1], WF(1, 0, 0), X[1][0][1]); SB();
  Wh[1][1][0][1] = __builtin_amdgcn_ds_read_tr16_b64_v4i16(LDSP(s16x4, aw[0] + 34560));
  SB();
  MA1(acc[0][2], WF(1, 0, 0), X[1][0][2]); SB();
  Wh[1][1][1][0] = __builtin_amdgcn_ds_read_tr16_b64_v4i16(LDSP(s16x4, aw[1] + 32256));
  DMA(m0a, 147456, vw1, srdW, sw[3], 1152);
  SB();
  MA1(acc[0][3], WF(1, 0, 0), X[1][0][3]); SB();
  Wh[1][1][1][1] = __builtin_amdgcn_ds_read_tr16_b64_v4i16(LDSP(s16x4, aw[1] + 34560));
  SB();
  MA1(acc[0][4], WF(1, 0, 0), X[1][0][4]); SB();
  Wh[1][1][2][0] = __builtin_amdgcn_ds_read_tr16_b64_v4i16(LDSP(s16x4, aw[2] + 32256));
  SB();
  MA1(acc[1][0], WF(1, 0, 1), X[1][0][0]); SB();
  Wh[1][1][2][1] = __builtin_amdgcn_ds_read_tr16_b64_v4i16(LDSP(s16x4, aw[2] + 34560));
  { u32x4 m2 = {~0u, ~0u, ~0u, ~0u}; PIN(m2); BST16(m2, vL, srdS, sd[2], 2048, "sc1"); }
  SB();
  MA1(acc[1][1], WF(1, 0, 1), X[1][0][1]); SB();
  Wh[1][1][3][0] = __builtin_amdgcn_ds_read_tr16_b64_v4i16(LDSP(s16x4, aw[3] + 32256));
  DMA(m0a, 156672, vw1, srdW, sw[3], 2304);
  SB();
  MA1(acc[1][2], WF(1, 0, 1), X[1][0][2]); SB();
  Wh[1][1][3][1] = __builtin_amdgcn_ds_read_tr16_b64_v4i16(LDSP(s16x4, aw[3] + 34560));
  SB();
  MA1(acc[1][3], WF(1, 0, 1), X[1][0][3]); SB();
  X[2][1][0] = *LDSP(bf16x8, a1 + 30720);
  SB();
  MA1(acc[1][4], WF(1, 0, 1), X[1][0][4]); SB();
  X[2][1][1] = *LDSP(bf16x8, a1 + 32768);
  SB();
  MA1(acc[2][0], WF(1, 0, 2), X[1][0][0]); SB();
  X[2][1][2] = *LDSP(bf16x8, a1 + 34816);
  SB();
  MA1(acc[2][1], WF(1, 0, 2), X[1][0][1]); SB();
  X[2][1][3] = *LDSP(bf16x8, a1 + 36864);
  DMAX(m0a, 5120, vxb, srdX, kx[4]);
  SB();
  MA1(acc[2][2], WF(1, 0, 2), X[1][0][2]); SB();
  X[2][1][4] = *LDSP(bf16x8, a1 + 38912);
  { u32x4 m2 = {~0u, ~0u, ~0u, ~0u}; PIN(m2); BST16(m2, n * 16 + min(q, 1u) * 32768 + (q >> 1) * 2359296 | OOB(q < 3), srdS, sd[3] - (2359296 + 32768), 0, "sc1"); }
  SB();
  MA1(acc[2][3], WF(1, 0, 2), X[1][0][3]); SB();
  SB();
  MA1(acc[2][4], WF(1, 0, 2), X[1][0][4]); SB();
  SB();
  MA1(acc[3][0], WF(1, 0, 3), X[1][0][0]); SB();
  DMAX(m0b, 142336, vz, srdW, sw[3]);
  SB();
  MA1(acc[3][1], WF(1, 0, 3), X[1][0][1]); SB();
  SB();
  MA1(acc[3][2], WF(1, 0, 3), X[1][0][2]); SB();
  SB();
  MA1(acc[3][3], WF(1, 0, 3), X[1][0][3]); SB();
  SB();
  MA1(acc[3][4], WF(1, 0, 3), X[1][0][4]); SB();
  SB();
  VMW(13);
  LGW(); BAR();
  STAMP(5, 4);
  MA1(acc[0][0], WF(1, 1, 0), X[1][1][0]); SB();
  Wh[2][0][0][0] = __builtin_amdgcn_ds_read_tr16_b64_v4i16(LDSP(s16x4, av[0] + 0));
  KNEXT(kx[5], sw[4], kx[4]);
  DMAX(m0a, 51200, vw0, srdW, sw[4]);
  VMW(13);
  PIN(rc[0]); for (int d = 0; d < 4; d += 2) { ra = dot2(rc[0][d], 0x3f803f80u, ra); rb2 = dot2(rc[0][d + 1], 0x3f803f80u, rb2); }
  SB();
  MA1(acc[0][1], WF(1, 1, 0), X[1][1][1]); SB();
  Wh[2][0][0][1] = __builtin_amdgcn_ds_read_tr16_b64_v4i16(LDSP(s16x4, av[0] + 2304));
  PIN(rc[1]); for (int d = 0; d < 4; d += 2) { ra = dot2(rc[1][d], 0x3f803f80u, ra); rb2 = dot2(rc[1][d + 1], 0x3f803f80u, rb2); }
  SB();
  MA1(acc[0][2], WF(1, 1, 0), X[1][1][2]); SB();
  Wh[2][0][1][0] = __builtin_amdgcn_ds_read_tr16_b64_v4i16(LDSP(s16x4, av[1] + 0));
  SB();
  MA1(acc[0][3], WF(1, 1, 0), X[1][1][3]); SB();
  Wh[2][0][1][1] = __builtin_amdgcn_ds_read_tr16_b64_v4i16(LDSP(s16x4, av[1] + 2304));
  SB();
  MA1(acc[0][4], WF(1, 1, 0), X[1][1][4]); SB();
  Wh[2][0][2][0] = __builtin_amdgcn_ds_read_tr16_b64_v4i16(LDSP(s16x4, av[2] + 0));
  DMA(m0a, 60416, vw0, srdW, sw[4], 1152);
  PIN(rc[2]); for (int d = 0; d < 4; d += 2) { ra = dot2(rc[2][d], 0x3f803f80u, ra); rb2 = dot2(rc[2][d + 1], 0x3f803f80u, rb2); }
  SB();
  MA1(acc[1][0], WF(1, 1, 1), X[1][1][0]); SB();
  Wh[2][0][2][1] = __builtin_amdgcn_ds_read_tr16_b64_v4i16(LDSP(s16x4, av[2] + 2304));
  PIN(rc[3]); for (int d = 0; d < 4; d += 2) { ra = dot2(rc[3][d], 0x3f803f80u, ra); rb2 = dot2(rc[3][d + 1], 0x3f803f80u, rb2); }
  SB();
  MA1(acc[1][1], WF(1, 1, 1), X[1][1][1]); SB();
  Wh[2][0][3][0] = __builtin_amdgcn_ds_read_tr16_b64_v4i16(LDSP(s16x4, av[3] + 0));
  SB();
  MA1(acc[1][2], WF(1, 1, 1), X[1][1][2]); SB();
  Wh[2][0][3][1] = __builtin_amdgcn_ds_read_tr16_b64_v4i16(LDSP(s16x4, av[3] + 2304));
  SB();
  MA1(acc[1][3], WF(1, 1, 1), X[1][1][3]); SB();
  X[3][0][0] = *LDSP(bf16x8, a0 + 40960);
  DMA(m0a, 69632, vw0, srdW, sw[4], 2304);
  PIN(rc[4]); for (int d = 0; d < 4; d += 2) { ra = dot2(rc[4][d], 0x3f803f80u, ra); rb2 = dot2(rc[4][d + 1], 0x3f803f80u, rb2); }
  SB();
  MA1(acc[1][4], WF(1, 1, 1), X[1][1][4]); SB();
  X[3][0][1] = *LDSP(bf16x8, a0 + 43008);
  SB();
  MA1(acc[2][0], WF(1, 1, 2), X[1][1][0]); SB();
  X[3][0][2] = *LDSP(bf16x8, a0 + 45056);
  PIN(rc[5]); for (int d = 0; d < 4; d += 2) { ra = dot2(rc[5][d], 0x3f803f80u, ra); rb2 = dot2(rc[5][d + 1], 0x3f803f80u, rb2); }
  SB();
  MA1(acc[2][1], WF(1, 1, 2), X[1][1][1]); SB();
  X[3][0][3] = *LDSP(bf16x8, a0 + 47104);
  PIN(rc[6]); for (int d = 0; d < 4; d += 2) { ra = dot2(rc[6][d], 0x3f803f80u, ra); rb2 = dot2(rc[6][d + 1], 0x3f803f80u, rb2); }
  SB();
  MA1(acc[2][2], WF(1, 1, 2), X[1][1][2]); SB();
  X[3][0][4] = *LDSP(bf16x8, a0 + 49152);
  SB();
  MA1(acc[2][3], WF(1, 1, 2), X[1][1][3]); SB();
  DMAX(m0a, 11264, vxa, srdX, kx[5]);
  SB();
  MA1(acc[2][4], WF(1, 1, 2), X[1][1][4]); SB();
  PIN(rc[7]); for (int d = 0; d < 4; d += 2) { ra = dot2(rc[7][d], 0x3f803f80u, ra); rb2 = dot2(rc[7][d + 1], 0x3f803f80u, rb2); }
  SB();
  MA1(acc[3][0], WF(1, 1, 3), X[1][1][0]); SB();
  PIN(rc[8]); for (int d = 0; d < 4; d += 2) { ra = dot2(rc[8][d], 0x3f803f80u, ra); rb2 = dot2(rc[8][d + 1], 0x3f803f80u, rb2); }
  SB();
  MA1(acc[3][1], WF(1, 1, 3), X[1][1][1]); SB();
  SB();
  MA1(acc[3][2], WF(1, 1, 3), X[1][1][2]); SB();
  DMAX(m0a, 55296, vw1, srdW, sw[4]);
  SB();
  MA1(acc[3][3], WF(1, 1, 3), X[1][1][3]); SB();
  SB();
  MA1(acc[3][4], WF(1, 1, 3), X[1][1][4]); SB();
  SB();
  MA1(acc[0][0], WF(2, 0, 0), X[2][0][0]); SB();
  Wh[2][1][0][0] = __builtin_amdgcn_ds_read_tr16_b64_v4i16(LDSP(s16x4, av[0] + 4608));
  ra += rb2; ra += DPPF(ra, 0xb1); ra += DPPF(ra, 0x4e); ra += DPPF(ra, 0x141); ra *= (1.f / 576.f);
  SB();
  MA1(acc[0][1], WF(2, 0, 0), X[2][0][1]); SB();
  Wh[2][1][0][1] = __builtin_amdgcn_ds_read_tr16_b64_v4i16(LDSP(s16x4, av[0] + 6912));
  { const u32 hi = pk2(ra, 0.f), lo = pk2(ra - bf_lo(hi), 0.f), val = (l & 1) ? lo : hi, vo = (l & 1) * 64 + kc * 128 + qc * 16 + jc * 2 | OOB(!(l & 6) && l < 48);
      asm volatile("buffer_store_short %0, %1, %2, %3 offen sc1" ::"v"(val), "v"(vo), "s"(srdS), "s"((u32)cfg::WS_WBAR + (tt >> 2) * 4096) : "memory"); }
  SB();
  MA1(acc[0][2], WF(2, 0, 0), X[2][0][2]); SB();
  Wh[2][1][1][0] = __builtin_amdgcn_ds_read_tr16_b64_v4i16(LDSP(s16x4, av[1] + 4608));
  DMA(m0a, 64512, vw1, srdW, sw[4], 1152);
  SB();
  MA1(acc[0][3], WF(2, 0, 0), X[2][0][3]); SB();
  Wh[2][1][1][1] = __builtin_amdgcn_ds_read_tr16_b64_v4i16(LDSP(s16x4, av[1] + 6912));
  SB();
  MA1(acc[0][4], WF(2, 0, 0), X[2][0][4]); SB();
  Wh[2][1][2][0] = __builtin_amdgcn_ds_read_tr16_b64_v4i16(LDSP(s16x4, av[2] + 4608));
  SB();
  MA1(acc[1][0], WF(2, 0, 1), X[2][0][0]); SB();
  Wh[2][1][2][1] = __builtin_amdgcn_ds_read_tr16_b64_v4i16(LDSP(s16x4, av[2] + 6912));
  SB();
  MA1(acc[1][1], WF(2, 0, 1), X[2][0][1]); SB();
  Wh[2][1][3][0] = __builtin_amdgcn_ds_read_tr16_b64_v4i16(LDSP(s16x4, av[3] + 4608));
  DMA(m0a, 73728, vw1, srdW, sw[4], 2304);
  SB();
  MA1(acc[1][2], WF(2, 0, 1), X[2][0][2]); SB();
  Wh[2][1][3][1] = __builtin_amdgcn_ds_read_tr16_b64_v4i16(LDSP(s16x4, av[3] + 6912));
  SB();
  MA1(acc[1][3], WF(2, 0, 1), X[2][0][3]); SB();
  X[3][1][0] = *LDSP(bf16x8, a1 + 40960);
  SB();
  MA1(acc[1][4], WF(2, 0, 1), X[2][0][4]); SB();
  X[3][1][1] = *LDSP(bf16x8, a1 + 43008);
  SB();
  MA1(acc[2][0], WF(2, 0, 2), X[2][0][0]); SB();
  X[3][1][2] = *LDSP(bf16x8, a1 + 45056);
  SB();
  MA1(acc[2][1], WF(2, 0, 2), X[2][0][1]); SB();
  X[3][1][3] = *LDSP(bf16x8, a1 + 47104);
  DMAX(m0a, 15360, vxb, srdX, kx[5]);
  SB();
  MA1(acc[2][2], WF(2, 0, 2), X[2][0][2]); SB();
  X[3][1][4] = *LDSP(bf16x8, a1 + 49152);
  SB();
  MA1(acc[2][3], WF(2, 0, 2), X[2][0][3]); SB();
  SB();
  MA1(acc[2][4], WF(2, 0, 2), X[2][0][4]); SB();
  SB();
  MA1(acc[3][0], WF(2, 0, 3), X[2][0][0]); SB();
  DMAX(m0b, 59392, vz, srdW, sw[4]);
  SB();
  MA1(acc[3][1], WF(2, 0, 3), X[2][0][1]); SB();
  SB();
  MA1(acc[3][2], WF(2, 0, 3), X[2][0][2]); SB();
  SB();
  MA1(acc[3][3], WF(2, 0, 3), X[2][0][3]); SB();
  SB();
  MA1(acc[3][4], WF(2, 0, 3), X[2][0][4]); SB();
  SB();
  VMW(10);
  LGW(); BAR();
  STAMP(6, 5);
  MA1(acc[0][0], WF(2, 1, 0), X[2][1][0]); SB();
  Wh[3][0][0][0] = __builtin_amdgcn_ds_read_tr16_b64_v4i16(LDSP(s16x4, av[0] + 27648));
  KNEXT(kx[6], sw[5], kx[5]);
  DMAX(m0a, 78848, vw0, srdW, sw[5]);
  SB();
  MA1(acc[0][1], WF(2, 1, 0), X[2][1][1]); SB();
  Wh[3][0][0][1] = __builtin_amdgcn_ds_read_tr16_b64_v4i16(LDSP(s16x4, av[0] + 29952));
  SB();
  MA1(acc[0][2], WF(2, 1, 0), X[2][1][2]); SB();
  Wh[3][0][1][0] = __builtin_amdgcn_ds_read_tr16_b64_v4i16(LDSP(s16x4, av[1] + 27648));
  SB();
  MA1(acc[0][3], WF(2, 1, 0), X[2][1][3]); SB();
  Wh[3][0][1][1] = __builtin_amdgcn_ds_read_tr16_b64_v4i16(LDSP(s16x4, av[1] + 29952));
  SB();
  MA1(acc[0][4], WF(2, 1, 0), X[2][1][4]); SB();
  Wh[3][0][2][0] = __builtin_amdgcn_ds_read_tr16_b64_v4i16(LDSP(s16x4, av[2] + 27648));
  DMA(m0a, 88064, vw0, srdW, sw[5], 1152);
  SB();
  MA1(acc[1][0], WF(2, 1, 1), X[2][1][0]); SB();
  Wh[3][0][2][1] = __builtin_amdgcn_ds_read_tr16_b64_v4i16(LDSP(s16x4, av[2] + 29952));
  SB();
  MA1(acc[1][1], WF(2, 1, 1), X[2][1][1]); SB();
  Wh[3][0][3][0] = __builtin_amdgcn_ds_read_tr16_b64_v4i16(LDSP(s16x4, av[3] + 27648));
  SB();
  MA1(acc[1][2], WF(2, 1, 1), X[2][1][2]); SB();
  Wh[3][0][3][1] = __builtin_amdgcn_ds_read_tr16_b64_v4i16(LDSP(s16x4, av[3] + 29952));
  SB();
  MA1(acc[1][3], WF(2, 1, 1), X[2][1][3]); SB();
  X[4][0][0] = *LDSP(bf16x8, a0 + 0);
  DMA(m0a, 97280, vw0, srdW, sw[5], 2304);
  SB();
  MA1(acc[1][4], WF(2, 1, 1), X[2][1][4]); SB();
  X[4][0][1] = *LDSP(bf16x8, a0 + 2048);
  SB();
  MA1(acc[2][0], WF(2, 1, 2), X[2][1][0]); SB();
  X[4][0][2] = *LDSP(bf16x8, a0 + 4096);
  SB();
  MA1(acc[2][1], WF(2, 1, 2), X[2][1][1]); SB();
  X[4][0][3] = *LDSP(bf16x8, a0 + 6144);
  SB();
  MA1(acc[2][2], WF(2, 1, 2), X[2][1][2]); SB();
  X[4][0][4] = *LDSP(bf16x8, a0 + 8192);
  SB();
  MA1(acc[2][3], WF(2, 1, 2), X[2][1][3]); SB();
  DMAX(m0a, 21504, vxa, srdX, kx[6]);
  SB();
  MA1(acc[2][4], WF(2, 1, 2), X[2][1][4]); SB();
  SB();
  MA1(acc[3][0], WF(2, 1, 3), X[2][1][0]); SB();
  SB();
  MA1(acc[3][1], WF(2, 1, 3), X[2][1][1]); SB();
  SB();
  MA1(acc[3][2], WF(2, 1, 3), X[2][1][2]); SB();
  DMAX(m0a, 82944, vw1, srdW, sw[5]);
  SB();
  MA1(acc[3][3], WF(2, 1, 3), X[2][1][3]); SB();
  SB();
  MA1(acc[3][4], WF(2, 1, 3), X[2][1][4]); SB();
  SB();
  MA1(acc[0][0], WF(3, 0, 0), X[3][0][0]); SB();
  Wh[3][1][0][0] = __builtin_amdgcn_ds_read_tr16_b64_v4i16(LDSP(s16x4, av[0] + 32256));
  SB();
  MA1(acc[0][1], WF(3, 0, 0), X[3][0][1]); SB();
  Wh[3][1][0][1] = __builtin_amdgcn_ds_read_tr16_b64_v4i16(LDSP(s16x4, av[0] + 34560));
  SB();
  MA1(acc[0][2], WF(3, 0, 0), X[3][0][2]); SB();
  Wh[3][1][1][0] = __builtin_amdgcn_ds_read_tr16_b64_v4i16(LDSP(s16x4, av[1] + 32256));
  DMA(m0a, 92160, vw1, srdW, sw[5], 1152);
  SB();
  MA1(acc[0][3], WF(3, 0, 0), X[3][0][3]); SB();
  Wh[3][1][1][1] = __builtin_amdgcn_ds_read_tr16_b64_v4i16(LDSP(s16x4, av[1] + 34560));
  SB();
  MA1(acc[0][4], WF(3, 0, 0), X[3][0][4]); SB();
  Wh[3][1][2][0] = __builtin_amdgcn_ds_read_tr16_b64_v4i16(LDSP(s16x4, av[2] + 32256));
  SB();
  MA1(acc[1][0], WF(3, 0, 1), X[3][0][0]); SB();
  Wh[3][1][2][1] = __builtin_amdgcn_ds_read_tr16_b64_v4i16(LDSP(s16x4, av[2] + 34560));
  SB();
  MA1(acc[1][1], WF(3, 0, 1), X[3][0][1]); SB();
  Wh[3][1][3][0] = __builtin_amdgcn_ds_read_tr16_b64_v4i16(LDSP(s16x4, av[3] + 32256));
  DMA(m0a, 101376, vw1, srdW, sw[5], 2304);
  SB();
  MA1(acc[1][2], WF(3, 0, 1), X[3][0][2]); SB();
  Wh[3][1][3][1] = __builtin_amdgcn_ds_read_tr16_b64_v4i16(LDSP(s16x4, av[3] + 34560));
  SB();
  MA1(acc[1][3], WF(3, 0, 1), X[3][0][3]); SB();
  X[4][1][0] = *LDSP(bf16x8, a1 + 0);
  SB();
  MA1(acc[1][4], WF(3, 0, 1), X[3][0][4]); SB();
  X[4][1][1] = *LDSP(bf16x8, a1 + 2048);
  SB();
  MA1(acc[2][0], WF(3, 0, 2), X[3][0][0]); SB();
  X[4][1][2] = *LDSP(bf16x8, a1 + 4096);
  SB();
  MA1(acc[2][1], WF(3, 0, 2), X[3][0][1]); SB();
  X[4][1][3] = *LDSP(bf16x8, a1 + 6144);
  DMAX(m0a, 25600, vxb, srdX, kx[6]);
  SB();
  MA1(acc[2][2], WF(3, 0, 2), X[3][0][2]); SB();
  X[4][1][4] = *LDSP(bf16x8, a1 + 8192);
  SB();
  MA1(acc[2][3], WF(3, 0, 2), X[3][0][3]); SB();
  SB();
  MA1(acc[2][4], WF(3, 0, 2), X[3][0][4]); SB();
  SB();
  MA1(acc[3][0], WF(3, 0, 3), X[3][0][0]); SB();
  DMAX(m0b, 87040, vz, srdW, sw[5]);
  SB();
  MA1(acc[3][1], WF(3, 0, 3), X[3][0][1]); SB();
  SB();
  MA1(acc[3][2], WF(3, 0, 3), X[3][0][2]); SB();
  SB();
  MA1(acc[3][3], WF(3, 0, 3), X[3][0][3]); SB();
  SB();
  MA1(acc[3][4], WF(3, 0, 3), X[3][0][4]); SB();
  SB();
  VMW(9);
  LGW(); BAR();
  STAMP(7, 6);
  PIN(ep); tag = ep * 0x9E3779B1u + 0x7F4A7C15u; PIN(tag);
  BST4C(tag, fgrp + (tt & 3) * cfg::FST + hg * 4 | OOB(tid == 0), srdS, 1792);
  MA1(acc[0][0], WF(3, 1, 0), X[3][1][0]); SB();
  Wh[4][0][0][0] = __builtin_amdgcn_ds_read_tr16_b64_v4i16(LDSP(s16x4, aw[0] + 0));
  KNEXT(kx[7], sw[6], kx[6]);
  DMAX(m0a, 106496, vw0, srdW, sw[6]);
  SB();
  MA1(acc[0][1], WF(3, 1, 0), X[3][1][1]); SB();
  Wh[4][0][0][1] = __builtin_amdgcn_ds_read_tr16_b64_v4i16(LDSP(s16x4, aw[0] + 2304));
  SB();
  MA1(acc[0][2], WF(3, 1, 0), X[3][1][2]); SB();
  Wh[4][0][1][0] = __builtin_amdgcn_ds_read_tr16_b64_v4i16(LDSP(s16x4, aw[1] + 0));
  SB();
  MA1(acc[0][3], WF(3, 1, 0), X[3][1][3]); SB();
  Wh[4][0][1][1] = __builtin_amdgcn_ds_read_tr16_b64_v4i16(LDSP(s16x4, aw[1] + 2304));
  SB();
  MA1(acc[0][4], WF(3, 1, 0), X[3][1][4]); SB();
  Wh[4][0][2][0] = __builtin_amdgcn_ds_read_tr16_b64_v4i16(LDSP(s16x4, aw[2] + 0));
  DMA(m0a, 115712, vw0, srdW, sw[6], 1152);
  SB();
  MA1(acc[1][0], WF(3, 1, 1), X[3][1][0]); SB();
  Wh[4][0][2][1] = __builtin_amdgcn_ds_read_tr16_b64_v4i16(LDSP(s16x4, aw[2] + 2304));
  SB();
  MA1(acc[1][1], WF(3, 1, 1), X[3][1][1]); SB();
  Wh[4][0][3][0] = __builtin_amdgcn_ds_read_tr16_b64_v4i16(LDSP(s16x4, aw[3] + 0));
  SB();
  MA1(acc[1][2], WF(3, 1, 1), X[3][1][2]); SB();
  Wh[4][0][3][1] = __builtin_amdgcn_ds_read_tr16_b64_v4i16(LDSP(s16x4, aw[3] + 2304));
  SB();
  MA1(acc[1][3], WF(3, 1, 1), X[3][1][3]); SB();
  X[5][0][0] = *LDSP(bf16x8, a0 + 10240);
  DMA(m0a, 124928, vw0, srdW, sw[6], 2304);
  SB();
  MA1(acc[1][4], WF(3, 1, 1), X[3][1][4]); SB();
  X[5][0][1] = *LDSP(bf16x8, a0 + 12288);
  SB();
  MA1(acc[2][0], WF(3, 1, 2), X[3][1][0]); SB();
  X[5][0][2] = *LDSP(bf16x8, a0 + 14336);
  SB();
  MA1(acc[2][1], WF(3, 1, 2), X[3][1][1]); SB();
  X[5][0][3] = *LDSP(bf16x8, a0 + 16384);
  SB();
  MA1(acc[2][2], WF(3, 1, 2), X[3][1][2]); SB();
  X[5][0][4] = *LDSP(bf16x8, a0 + 18432);
  SB();
  MA1(acc[2][3], WF(3, 1, 2), X[3][1][3]); SB();
  DMAX(m0a, 31744, vxa, srdX, kx[7]);
  SB();
  MA1(acc[2][4], WF(3, 1, 2), X[3][1][4]); SB();
  SB();
  MA1(acc[3][0], WF(3, 1, 3), X[3][1][0]); SB();
  SB();
  MA1(acc[3][1], WF(3, 1, 3), X[3][1][1]); SB();
  SB();
  MA1(acc[3][2], WF(3, 1, 3), X[3][1][2]); SB();
  DMAX(m0a, 110592, vw1, srdW, sw[6]);
  SB();
  MA1(acc[3][3], WF(3, 1, 3), X[3][1][3]); SB();
  SB();
  MA1(acc[3][4], WF(3, 1, 3), X[3][1][4]); SB();
  SB();
  MA1(acc[0][0], WF(4, 0, 0), X[4][0][0]); SB();
  Wh[4][1][0][0] = __builtin_amdgcn_ds_read_tr16_b64_v4i16(LDSP(s16x4, aw[0] + 4608));
  SB();
  MA1(acc[0][1], WF(4, 0, 0), X[4][0][1]); SB();
  Wh[4][1][0][1] = __builtin_amdgcn_ds_read_tr16_b64_v4i16(LDSP(s16x4, aw[0] + 6912));
  SB();
  MA1(acc[0][2], WF(4, 0, 0), X[4][0][2]); SB();
  Wh[4][1][1][0] = __builtin_amdgcn_ds_read_tr16_b64_v4i16(LDSP(s16x4, aw[1] + 4608));
  DMA(m0a, 119808, vw1, srdW, sw[6], 1152);
  SB();
  MA1(acc[0][3], WF(4, 0, 0), X[4][0][3]); SB();
  Wh[4][1][1][1] = __builtin_amdgcn_ds_read_tr16_b64_v4i16(LDSP(s16x4, aw[1] + 6912));
  SB();
  MA1(acc[0][4], WF(4, 0, 0), X[4][0][4]); SB();
  Wh[4][1][2][0] = __builtin_amdgcn_ds_read_tr16_b64_v4i16(LDSP(s16x4, aw[2] + 4608));
  SB();
  MA1(acc[1][0], WF(4, 0, 1), X[4][0][0]); SB();
  Wh[4][1][2][1] = __builtin_amdgcn_ds_read_tr16_b64_v4i16(LDSP(s16x4, aw[2] + 6912));
  SB();
  MA1(acc[1][1], WF(4, 0, 1), X[4][0][1]); SB();
  Wh[4][1][3][0] = __builtin_amdgcn_ds_read_tr16_b64_v4i16(LDSP(s16x4, aw[3] + 4608));
  DMA(m0a, 129024, vw1, srdW, sw[6], 2304);
  SB();
  MA1(acc[1][2], WF(4, 0, 1), X[4][0][2]); SB();
  Wh[4][1][3][1] = __builtin_amdgcn_ds_read_tr16_b64_v4i16(LDSP(s16x4, aw[3] + 6912));
  SB();
  MA1(acc[1][3], WF(4, 0, 1), X[4][0][3]); SB();
  X[5][1][0] = *LDSP(bf16x8, a1 + 10240);
  SB();
  MA1(acc[1][4], WF(4, 0, 1), X[4][0][4]); SB();
  X[5][1][1] = *LDSP(bf16x8, a1 + 12288);
  SB();
  MA1(acc[2][0], WF(4, 0, 2), X[4][0][0]); SB();
  X[5][1][2] = *LDSP(bf16x8, a1 + 14336);
  SB();
  MA1(acc[2][1], WF(4, 0, 2), X[4][0][1]); SB();
  X[5][1][3] = *LDSP(bf16x8, a1 + 16384);
  DMAX(m0a, 35840, vxb, srdX, kx[7]);
  SB();
  MA1(acc[2][2], WF(4, 0, 2), X[4][0][2]); SB();
  X[5][1][4] = *LDSP(bf16x8, a1 + 18432);
  SB();
  MA1(acc[2][3], WF(4, 0, 2), X[4][0][3]); SB();
  SB();
  MA1(acc[2][4], WF(4, 0, 2), X[4][0][4]); SB();
  SB();
  MA1(acc[3][0], WF(4, 0, 3), X[4][0][0]); SB();
  DMAX(m0b, 114688, vz, srdW, sw[6]);
  SB();
  MA1(acc[3][1], WF(4, 0, 3), X[4][0][1]); SB();
  SB();
  MA1(acc[3][2], WF(4, 0, 3), X[4][0][2]); SB();
  SB();
  MA1(acc[3][3], WF(4, 0, 3), X[4][0][3]); SB();
  SB();
  MA1(acc[3][4], WF(4, 0, 3), X[4][0][4]); SB();
  SB();
  VMW(10);
  LGW(); BAR();
  STAMP(8, 7);
  MA1(acc[0][0], WF(4, 1, 0), X[4][1][0]); SB();
  Wh[5][0][0][0] = __builtin_amdgcn_ds_read_tr16_b64_v4i16(LDSP(s16x4, aw[0] + 27648));
  KNEXT(kx[8], sw[7], kx[7]);
  DMAX(m0a, 134144, vw0, srdW, sw[7]);
  SB();
  MA1(acc[0][1], WF(4, 1, 0), X[4][1][1]); SB();
  Wh[5][0][0][1] = __builtin_amdgcn_ds_read_tr16_b64_v4i16(LDSP(s16x4, aw[0] + 29952));
  SB();
  MA1(acc[0][2], WF(4, 1, 0), X[4][1][2]); SB();
  Wh[5][0][1][0] = __builtin_amdgcn_ds_read_tr16_b64_v4i16(LDSP(s16x4, aw[1] + 27648));
  SB();
  MA1(acc[0][3], WF(4, 1, 0), X[4][1][3]); SB();
  Wh[5][0][1][1] = __builtin_amdgcn_ds_read_tr16_b64_v4i16(LDSP(s16x4, aw[1] + 29952));
  SB();
  MA1(acc[0][4], WF(4, 1, 0), X[4][1][4]); SB();
  Wh[5][0][2][0] = __builtin_amdgcn_ds_read_tr16_b64_v4i16(LDSP(s16x4, aw[2] + 27648));
  DMA(m0a, 143360, vw0, srdW, sw[7], 1152);
  SB();
  MA1(acc[1][0], WF(4, 1, 1), X[4][1][0]); SB();
  Wh[5][0][2][1] = __builtin_amdgcn_ds_read_tr16_b64_v4i16(LDSP(s16x4, aw[2] + 29952));
  SB();
  MA1(acc[1][1], WF(4, 1, 1), X[4][1][1]); SB();
  Wh[5][0][3][0] = __builtin_amdgcn_ds_read_tr16_b64_v4i16(LDSP(s16x4, aw[3] + 27648));
  SB();
  MA1(acc[1][2], WF(4, 1, 1), X[4][1][2]); SB();
  Wh[5][0][3][1] = __builtin_amdgcn_ds_read_tr16_b64_v4i16(LDSP(s16x4, aw[3] + 29952));
  SB();
  MA1(acc[1][3], WF(4, 1, 1), X[4][1][3]); SB();
  X[6][0][0] = *LDSP(bf16x8, a0 + 20480);
  DMA(m0a, 152576, vw0, srdW, sw[7], 2304);
  SB();
  MA1(acc[1][4], WF(4, 1, 1), X[4][1][4]); SB();
  X[6][0][1] = *LDSP(bf16x8, a0 + 22528);
  SB();
  MA1(acc[2][0], WF(4, 1, 2), X[4][1][0]); SB();
  X[6][0][2] = *LDSP(bf16x8, a0 + 24576);
  SB();
  MA1(acc[2][1], WF(4, 1, 2), X[4][1][1]); SB();
  X[6][0][3] = *LDSP(bf16x8, a0 + 26624);
  SB();
  MA1(acc[2][2], WF(4, 1, 2), X[4][1][2]); SB();
  X[6][0][4] = *LDSP(bf16x8, a0 + 28672);
  SB();
  MA1(acc[2][3], WF(4, 1, 2), X[4][1][3]); SB();
  DMAX(m0a, 41984, vxa, srdX, kx[8]);
  SB();
  MA1(acc[2][4], WF(4, 1, 2), X[4][1][4]); SB();
  SB();
  MA1(acc[3][0], WF(4, 1, 3), X[4][1][0]); SB();
  SB();
  MA1(acc[3][1], WF(4, 1, 3), X[4][1][1]); SB();
  SB();
  MA1(acc[3][2], WF(4, 1, 3), X[4][1][2]); SB();
  DMAX(m0a, 138240, vw1, srdW, sw[7]);
  SB();
  MA1(acc[3][3], WF(4, 1, 3), X[4][1][3]); SB();
  SB();
  MA1(acc[3][4], WF(4, 1, 3), X[4][1][4]); SB();
  SB();
  MA1(acc[0][0], WF(5, 0, 0), X[5][0][0]); SB();
  Wh[5][1][0][0] = __builtin_amdgcn_ds_read_tr16_b64_v4i16(LDSP(s16x4, aw[0] + 32256));
  SB();
  MA1(acc[0][1], WF(5, 0, 0), X[5][0][1]); SB();
  Wh[5][1][0][1] = __builtin_amdgcn_ds_read_tr16_b64_v4i16(LDSP(s16x4, aw[0] + 34560));
  SB();
  MA1(acc[0][2], WF(5, 0, 0), X[5][0][2]); SB();
  Wh[5][1][1][0] = __builtin_amdgcn_ds_read_tr16_b64_v4i16(LDSP(s16x4, aw[1] + 32256));
  DMA(m0a, 147456, vw1, srdW, sw[7], 1152);
  SB();
  MA1(acc[0][3], WF(5, 0, 0), X[5][0][3]); SB();
  Wh[5][1][1][1] = __builtin_amdgcn_ds_read_tr16_b64_v4i16(LDSP(s16x4, aw[1] + 34560));
  SB();
  MA1(acc[0][4], WF(5, 0, 0), X[5][0][4]); SB();
  Wh[5][1][2][0] = __builtin_amdgcn_ds_read_tr16_b64_v4i16(LDSP(s16x4, aw[2] + 32256));
  SB();
  MA1(acc[1][0], WF(5, 0, 1), X[5][0][0]); SB();
  Wh[5][1][2][1] = __builtin_amdgcn_ds_read_tr16_b64_v4i16(LDSP(s16x4, aw[2] + 34560));
  SB();
  MA1(acc[1][1], WF(5, 0, 1), X[5][0][1]); SB();
  Wh[5][1][3][0] = __builtin_amdgcn_ds_read_tr16_b64_v4i16(LDSP(s16x4, aw[3] + 32256));
  DMA(m0a, 156672, vw1, srdW, sw[7], 2304);
  SB();
  MA1(acc[1][2], WF(5, 0, 1), X[5][0][2]); SB();
  Wh[5][1][3][1] = __builtin_amdgcn_ds_read_tr16_b64_v4i16(LDSP(s16x4, aw[3] + 34560));
  SB();
  MA1(acc[1][3], WF(5, 0, 1), X[5][0][3]); SB();
  X[6][1][0] = *LDSP(bf16x8, a1 + 20480);
  SB();
  MA1(acc[1][4], WF(5, 0, 1), X[5][0][4]); SB();
  X[6][1][1] = *LDSP(bf16x8, a1 + 22528);
  SB();
  MA1(acc[2][0], WF(5, 0, 2), X[5][0][0]); SB();
  X[6][1][2] = *LDSP(bf16x8, a1 + 24576);
  SB();
  MA1(acc[2][1], WF(5, 0, 2), X[5][0][1]); SB();
  X[6][1][3] = *LDSP(bf16x8, a1 + 26624);
  DMAX(m0a, 46080, vxb, srdX, kx[8]);
  SB();
  MA1(acc[2][2], WF(5, 0, 2), X[5][0][2]); SB();
  X[6][1][4] = *LDSP(bf16x8, a1 + 28672);
  SB();
  MA1(acc[2][3], WF(5, 0, 2), X[5][0][3]); SB();
  SB();
  MA1(acc[2][4], WF(5, 0, 2), X[5][0][4]); SB();
  SB();
  MA1(acc[3][0], WF(5, 0, 3), X[5][0][0]); SB();
  DMAX(m0b, 142336, vz, srdW, sw[7]);
  SB();
  MA1(acc[3][1], WF(5, 0, 3), X[5][0][1]); SB();
  SB();
  MA1(acc[3][2], WF(5, 0, 3), X[5][0][2]); SB();
  SB();
  MA1(acc[3][3], WF(5, 0, 3), X[5][0][3]); SB();
  SB();
  MA1(acc[3][4], WF(5, 0, 3), X[5][0][4]); SB();
  SB();
  VMW(9);
  LGW(); BAR();
  STAMP(9, 8);
  MA1(acc[0][0], WF(5, 1, 0), X[5][1][0]); SB();
  Wh[6][0][0][0] = __builtin_amdgcn_ds_read_tr16_b64_v4i16(LDSP(s16x4, av[0] + 0));
  KNEXT(kx[9], sw[8], kx[8]);
  DMAX(m0a, 51200, vw0, srdW, sw[8]);
  SB();
  MA1(acc[0][1], WF(5, 1, 0), X[5][1][1]); SB();
  Wh[6][0][0][1] = __builtin_amdgcn_ds_read_tr16_b64_v4i16(LDSP(s16x4, av[0] + 2304));
  SB();
  MA1(acc[0][2], WF(5, 1, 0), X[5][1][2]); SB();
  Wh[6][0][1][0] = __builtin_amdgcn_ds_read_tr16_b64_v4i16(LDSP(s16x4, av[1] + 0));
  SB();
  MA1(acc[0][3], WF(5, 1, 0), X[5][1][3]); SB();
  Wh[6][0][1][1] = __builtin_amdgcn_ds_read_tr16_b64_v4i16(LDSP(s16x4, av[1] + 2304));
  SB();
  MA1(acc[0][4], WF(5, 1, 0), X[5][1][4]); SB();
  Wh[6][0][2][0] = __builtin_amdgcn_ds_read_tr16_b64_v4i16(LDSP(s16x4, av[2] + 0));
  SB();
  MA1(acc[1][0], WF(5, 1, 1), X[5][1][0]); SB();
  Wh[6][0][2][1] = __builtin_amdgcn_ds_read_tr16_b64_v4i16(LDSP(s16x4, av[2] + 2304));
  DMA(m0a, 60416, vw0, srdW, sw[8], 1152);
  SB();
  MA1(acc[1][1], WF(5, 1, 1), X[5][1][1]); SB();
  Wh[6][0][3][0] = __builtin_amdgcn_ds_read_tr16_b64_v4i16(LDSP(s16x4, av[3] + 0));
  SB();
  MA1(acc[1][2], WF(5, 1, 1), X[5][1][2]); SB();
  Wh[6][0][3][1] = __builtin_amdgcn_ds_read_tr16_b64_v4i16(LDSP(s16x4, av[3] + 2304));
  SB();
  MA1(acc[1][3], WF(5, 1, 1), X[5][1][3]); SB();
  X[7][0][0] = *LDSP(bf16x8, a0 + 30720);
  SB();
  MA1(acc[1][4], WF(5, 1, 1), X[5][1][4]); SB();
  X[7][0][1] = *LDSP(bf16x8, a0 + 32768);
  SB();
  MA1(acc[2][0], WF(5, 1, 2), X[5][1][0]); SB();
  X[7][0][2] = *LDSP(bf16x8, a0 + 34816);
  SB();
  MA1(acc[2][1], WF(5, 1, 2), X[5][1][1]); SB();
  X[7][0][3] = *LDSP(bf16x8, a0 + 36864);
  DMA(m0a, 69632, vw0, srdW, sw[8], 2304);
  SB();
  MA1(acc[2][2], WF(5, 1, 2), X[5][1][2]); SB();
  X[7][0][4] = *LDSP(bf16x8, a0 + 38912);
  SB();
  MA1(acc[2][3], WF(5, 1, 2), X[5][1][3]); SB();
  SB();
  MA1(acc[2][4], WF(5, 1, 2), X[5][1][4]); SB();
  SB();
  MA1(acc[3][0], WF(5, 1, 3), X[5][1][0]); SB();
  SB();
  MA1(acc[3][1], WF(5, 1, 3), X[5][1][1]); SB();
  SB();
  MA1(acc[3][2], WF(5, 1, 3), X[5][1][2]); SB();
  DMAX(m0a, 55296, vw1, srdW, sw[8]);
  SB();
  MA1(acc[3][3], WF(5, 1, 3), X[5][1][3]); SB();
  SB();
  MA1(acc[3][4], WF(5, 1, 3), X[5][1][4]); SB();
  SB();
  MA1(acc[0][0], WF(6, 0, 0), X[6][0][0]); SB();
  Wh[6][1][0][0] = __builtin_amdgcn_ds_read_tr16_b64_v4i16(LDSP(s16x4, av[0] + 4608));
  SB();
  MA1(acc[0][1], WF(6, 0, 0), X[6][0][1]); SB();
  Wh[6][1][0][1] = __builtin_amdgcn_ds_read_tr16_b64_v4i16(LDSP(s16x4, av[0] + 6912));
  SB();
  MA1(acc[0][2], WF(6, 0, 0), X[6][0][2]); SB();
  Wh[6][1][1][0] = __builtin_amdgcn_ds_read_tr16_b64_v4i16(LDSP(s16x4, av[1] + 4608));
  DMA(m0a, 64512, vw1, srdW, sw[8], 1152);
  SB();
  MA1(acc[0][3], WF(6, 0, 0), X[6][0][3]); SB();
  Wh[6][1][1][1] = __builtin_amdgcn_ds_read_tr16_b64_v4i16(LDSP(s16x4, av[1] + 6912));
  SB();
  MA1(acc[0][4], WF(6, 0, 0), X[6][0][4]); SB();
  Wh[6][1][2][0] = __builtin_amdgcn_ds_read_tr16_b64_v4i16(LDSP(s16x4, av[2] + 4608));
  SB();
  MA1(acc[1][0], WF(6, 0, 1), X[6][0][0]); SB();
  Wh[6][1][2][1] = __builtin_amdgcn_ds_read_tr16_b64_v4i16(LDSP(s16x4, av[2] + 6912));
  SB();
  MA1(acc[1][1], WF(6, 0, 1), X[6][0][1]); SB();
  Wh[6][1][3][0] = __builtin_amdgcn_ds_read_tr16_b64_v4i16(LDSP(s16x4, av[3] + 4608));
  SB();
  MA1(acc[1][2], WF(6, 0, 1), X[6][0][2]); SB();
  Wh[6][1][3][1] = __builtin_amdgcn_ds_read_tr16_b64_v4i16(LDSP(s16x4, av[3] + 6912));
  SB();
  MA1(acc[1][3], WF(6, 0, 1), X[6][0][3]); SB();
  X[7][1][0] = *LDSP(bf16x8, a1 + 30720);
  DMA(m0a, 73728, vw1, srdW, sw[8], 2304);
  SB();
  MA1(acc[1][4], WF(6, 0, 1), X[6][0][4]); SB();
  X[7][1][1] = *LDSP(bf16x8, a1 + 32768);
  SB();
  MA1(acc[2][0], WF(6, 0, 2), X[6][0][0]); SB();
  X[7][1][2] = *LDSP(bf16x8, a1 + 34816);
  SB();
  MA1(acc[2][1], WF(6, 0, 2), X[6][0][1]); SB();
  X[7][1][3] = *LDSP(bf16x8, a1 + 36864);
  SB();
  MA1(acc[2][2], WF(6, 0, 2), X[6][0][2]); SB();
  X[7][1][4] = *LDSP(bf16x8, a1 + 38912);
  SB();
  MA1(acc[2][3], WF(6, 0, 2), X[6][0][3]); SB();
  SB();
  MA1(acc[2][4], WF(6, 0, 2), X[6][0][4]); SB();
  DMAX(m0b, 59392, vz, srdW, sw[8]);
  SB();
  MA1(acc[3][0], WF(6, 0, 3), X[6][0][0]); SB();
  SB();
  MA1(acc[3][1], WF(6, 0, 3), X[6][0][1]); SB();
  SB();
  MA1(acc[3][2], WF(6, 0, 3), X[6][0][2]); SB();
  SB();
  MA1(acc[3][3], WF(6, 0, 3), X[6][0][3]); SB();
  SB();
  MA1(acc[3][4], WF(6, 0, 3), X[6][0][4]); SB();
  SB();
  VMW(7);
  LGW(); BAR();
  STAMP(10, 9);
  MA1(acc[0][0], WF(6, 1, 0), X[6][1][0]); SB();
  Wh[7][0][0][0] = __builtin_amdgcn_ds_read_tr16_b64_v4i16(LDSP(s16x4, av[0] + 27648));
  DMA(m0b, 87040, vo3, srdO, hb, 0);
  SB();
  MA1(acc[0][1], WF(6, 1, 0), X[6][1][1]); SB();
  Wh[7][0][0][1] = __builtin_amdgcn_ds_read_tr16_b64_v4i16(LDSP(s16x4, av[0] + 29952));
  SB();
  MA1(acc[0][2], WF(6, 1, 0), X[6][1][2]); SB();
  Wh[7][0][1][0] = __builtin_amdgcn_ds_read_tr16_b64_v4i16(LDSP(s16x4, av[1] + 27648));
  SB();
  MA1(acc[0][3], WF(6, 1, 0), X[6][1][3]); SB();
  Wh[7][0][1][1] = __builtin_amdgcn_ds_read_tr16_b64_v4i16(LDSP(s16x4, av[1] + 29952));
  SB();
  MA1(acc[0][4], WF(6, 1, 0), X[6][1][4]); SB();
  Wh[7][0][2][0] = __builtin_amdgcn_ds_read_tr16_b64_v4i16(LDSP(s16x4, av[2] + 27648));
  SB();
  MA1(acc[1][0], WF(6, 1, 1), X[6][1][0]); SB();
  Wh[7][0][2][1] = __builtin_amdgcn_ds_read_tr16_b64_v4i16(LDSP(s16x4, av[2] + 29952));
  DMA(m0a, 78848, vb[7], srdO, hb, 0);
  SB();
  MA1(acc[1][1], WF(6, 1, 1), X[6][1][1]); SB();
  Wh[7][0][3][0] = __builtin_amdgcn_ds_read_tr16_b64_v4i16(LDSP(s16x4, av[3] + 27648));
  SB();
  MA1(acc[1][2], WF(6, 1, 1), X[6][1][2]); SB();
  Wh[7][0][3][1] = __builtin_amdgcn_ds_read_tr16_b64_v4i16(LDSP(s16x4, av[3] + 29952));
  SB();
  MA1(acc[1][3], WF(6, 1, 1), X[6][1][3]); SB();
  X[8][0][0] = *LDSP(bf16x8, a0 + 40960);
  SB();
  MA1(acc[1][4], WF(6, 1, 1), X[6][1][4]); SB();
  X[8][0][1] = *LDSP(bf16x8, a0 + 43008);
  SB();
  MA1(acc[2][0], WF(6, 1, 2), X[6][1][0]); SB();
  X[8][0][2] = *LDSP(bf16x8, a0 + 45056);
  SB();
  MA1(acc[2][1], WF(6, 1, 2), X[6][1][1]); SB();
  X[8][0][3] = *LDSP(bf16x8, a0 + 47104);
  DMA(m0a, 82944, vb[8], srdO, hb, 0);
  SB();
  MA1(acc[2][2], WF(6, 1, 2), X[6][1][2]); SB();
  X[8][0][4] = *LDSP(bf16x8, a0 + 49152);
  SB();
  MA1(acc[2][3], WF(6, 1, 2), X[6][1][3]); SB();
  SB();
  MA1(acc[2][4], WF(6, 1, 2), X[6][1][4]); SB();
  SB();
  MA1(acc[3][0], WF(6, 1, 3), X[6][1][0]); SB();
  SB();
  MA1(acc[3][1], WF(6, 1, 3), X[6][1][1]); SB();
  SB();
  MA1(acc[3][2], WF(6, 1, 3), X[6][1][2]); SB();
  DMA(m0a, 88064, vb[5], srdO, hb, 0);
  SB();
  MA1(acc[3][3], WF(6, 1, 3), X[6][1][3]); SB();
  SB();
  MA1(acc[3][4], WF(6, 1, 3), X[6][1][4]); SB();
  SB();
  MA1(acc[0][0], WF(7, 0, 0), X[7][0][0]); SB();
  Wh[7][1][0][0] = __builtin_amdgcn_ds_read_tr16_b64_v4i16(LDSP(s16x4, av[0] + 32256));
  SB();
  MA1(acc[0][1], WF(7, 0, 0), X[7][0][1]); SB();
  Wh[7][1][0][1] = __builtin_amdgcn_ds_read_tr16_b64_v4i16(LDSP(s16x4, av[0] + 34560));
  SB();
  MA1(acc[0][2], WF(7, 0, 0), X[7][0][2]); SB();
  Wh[7][1][1][0] = __builtin_amdgcn_ds_read_tr16_b64_v4i16(LDSP(s16x4, av[1] + 32256));
  SB();
  MA1(acc[0][3], WF(7, 0, 0), X[7][0][3]); SB();
  Wh[7][1][1][1] = __builtin_amdgcn_ds_read_tr16_b64_v4i16(LDSP(s16x4, av[1] + 34560));
  DMA(m0a, 92160, vb[6], srdO, hb, 0);
  SB();
  MA1(acc[0][4], WF(7, 0, 0), X[7][0][4]); SB();
  Wh[7][1][2][0] = __builtin_amdgcn_ds_read_tr16_b64_v4i16(LDSP(s16x4, av[2] + 32256));
  SB();
  MA1(acc[1][0], WF(7, 0, 1), X[7][0][0]); SB();
  Wh[7][1][2][1] = __builtin_amdgcn_ds_read_tr16_b64_v4i16(LDSP(s16x4, av[2] + 34560));
  SB();
  MA1(acc[1][1], WF(7, 0, 1), X[7][0][1]); SB();
  Wh[7][1][3][0] = __builtin_amdgcn_ds_read_tr16_b64_v4i16(LDSP(s16x4, av[3] + 32256));
  SB();
  MA1(acc[1][2], WF(7, 0, 1), X[7][0][2]); SB();
  Wh[7][1][3][1] = __builtin_amdgcn_ds_read_tr16_b64_v4i16(LDSP(s16x4, av[3] + 34560));
  SB();
  MA1(acc[1][3], WF(7, 0, 1), X[7][0][3]); SB();
  X[8][1][0] = *LDSP(bf16x8, a1 + 40960);
  SB();
  MA1(acc[1][4], WF(7, 0, 1), X[7][0][4]); SB();
  X[8][1][1] = *LDSP(bf16x8, a1 + 43008);
  DMA(m0a, 97280, vb[5], srdO, hb, 82944);
  SB();
  MA1(acc[2][0], WF(7, 0, 2), X[7][0][0]); SB();
  X[8][1][2] = *LDSP(bf16x8, a1 + 45056);
  SB();
  MA1(acc[2][1], WF(7, 0, 2), X[7][0][1]); SB();
  X[8][1][3] = *LDSP(bf16x8, a1 + 47104);
  SB();
  MA1(acc[2][2], WF(7, 0, 2), X[7][0][2]); SB();
  X[8][1][4] = *LDSP(bf16x8, a1 + 49152);
  SB();
  MA1(acc[2][3], WF(7, 0, 2), X[7][0][3]); SB();
  SB();
  MA1(acc[2][4], WF(7, 0, 2), X[7][0][4]); SB();
  SB();
  MA1(acc[3][0], WF(7, 0, 3), X[7][0][0]); SB();
  DMA(m0a, 101376, vb[6], srdO, hb, 82944);
  SB();
  MA1(acc[3][1], WF(7, 0, 3), X[7][0][1]); SB();
  SB();
  MA1(acc[3][2], WF(7, 0, 3), X[7][0][2]); SB();
  SB();
  MA1(acc[3][3], WF(7, 0, 3), X[7][0][3]); SB();
  SB();
  MA1(acc[3][4], WF(7, 0, 3), X[7][0][4]); SB();
  DMA(mxr, 0, vb[11], srdX, hb, 0);
  SB();
  MA1(acc[0][0], WF(7, 1, 0), X[7][1][0]); SB();
  SB();
  MA1(acc[0][1], WF(7, 1, 0), X[7][1][1]); SB();
  SB();
  MA1(acc[0][2], WF(7, 1, 0), X[7][1][2]); SB();
  SB();
  MA1(acc[0][3], WF(7, 1, 0), X[7][1][3]); SB();
  SB();
  MA1(acc[0][4], WF(7, 1, 0), X[7][1][4]); SB();
  SB();
  MA1(acc[1][0], WF(7, 1, 1), X[7][1][0]); SB();
  DMA(mxr, 1008, vb[11], srdX, hb, 8064);
  SB();
  MA1(acc[1][1], WF(7, 1, 1), X[7][1][1]); SB();
  SB();
  MA1(acc[1][2], WF(7, 1, 1), X[7][1][2]); SB();
  SB();
  MA1(acc[1][3], WF(7, 1, 1), X[7][1][3]); SB();
  SB();
  MA1(acc[1][4], WF(7, 1, 1), X[7][1][4]); SB();
  SB();
  MA1(acc[2][0], WF(7, 1, 2), X[7][1][0]); SB();
  SB();
  MA1(acc[2][1], WF(7, 1, 2), X[7][1][1]); SB();
  DMA(mxr, 2016, vb[11], srdX, hb, 16128);
  SB();
  MA1(acc[2][2], WF(7, 1, 2), X[7][1][2]); SB();
  SB();
  MA1(acc[2][3], WF(7, 1, 2), X[7][1][3]); SB();
  SB();
  MA1(acc[2][4], WF(7, 1, 2), X[7][1][4]); SB();
  SB();
  MA1(acc[3][0], WF(7, 1, 3), X[7][1][0]); SB();
  SB();
  MA1(acc[3][1], WF(7, 1, 3), X[7][1][1]); SB();
  SB();
  MA1(acc[3][2], WF(7, 1, 3), X[7][1][2]); SB();
  SB();
  MA1(acc[3][3], WF(7, 1, 3), X[7][1][3]); SB();
  SB();
  MA1(acc[3][4], WF(7, 1, 3), X[7][1][4]); SB();
  SB();
  VMW(10);
  LGW(); BAR();
  STAMP(11, 10);
  PIN(tpc); PIN(tpi); PIN(tp1); PIN(tp2); f32x2 st[5];
  Wh[8][0][0][0] = __builtin_amdgcn_ds_read_tr16_b64_v4i16(LDSP(s16x4, aw[0] + 0));
  Wh[8][0][0][1] = __builtin_amdgcn_ds_read_tr16_b64_v4i16(LDSP(s16x4, aw[0] + 2304));
  Wh[8][1][0][0] = __builtin_amdgcn_ds_read_tr16_b64_v4i16(LDSP(s16x4, aw[0] + 4608));
  Wh[8][1][0][1] = __builtin_amdgcn_ds_read_tr16_b64_v4i16(LDSP(s16x4, aw[0] + 6912));
  SB();
  MA1(acc[0][0], WF(8, 0, 0), X[8][0][0]); SB();
  Wh[8][0][1][0] = __builtin_amdgcn_ds_read_tr16_b64_v4i16(LDSP(s16x4, aw[1] + 0));
  BLV(vp[6], srdS, 0);
  BLV(vp[4], srdS, 0);
  BLV(vp[4], srdS, 256);
  BLV(vL * 16 | OOB(l < 9), srdS, (u32)cfg::WS_FLAGS + tt * cfg::FST);
  BLW();
  SB();
  MA1(acc[0][1], WF(8, 0, 0), X[8][0][1]); SB();
  Wh[8][0][1][1] = __builtin_amdgcn_ds_read_tr16_b64_v4i16(LDSP(s16x4, aw[1] + 2304));
  SB();
  MA1(acc[0][2], WF(8, 0, 0), X[8][0][2]); SB();
  Wh[8][1][1][0] = __builtin_amdgcn_ds_read_tr16_b64_v4i16(LDSP(s16x4, aw[1] + 4608));
  SB();
  MA1(acc[0][3], WF(8, 0, 0), X[8][0][3]); SB();
  Wh[8][1][1][1] = __builtin_amdgcn_ds_read_tr16_b64_v4i16(LDSP(s16x4, aw[1] + 6912));
  SB();
  MA1(acc[0][4], WF(8, 0, 0), X[8][0][4]); SB();
  Wh[8][0][2][0] = __builtin_amdgcn_ds_read_tr16_b64_v4i16(LDSP(s16x4, aw[2] + 0));
  for (int k = 0; k < 5; ++k) st[k] = *LDSP(f32x2, 161792 + (k * 16 + n) * 8);
  SB();
  MA1(acc[0][0], WF(8, 1, 0), X[8][1][0]); SB();
  Wh[8][0][2][1] = __builtin_amdgcn_ds_read_tr16_b64_v4i16(LDSP(s16x4, aw[2] + 2304));
  SB();
  MA1(acc[0][1], WF(8, 1, 0), X[8][1][1]); SB();
  Wh[8][1][2][0] = __builtin_amdgcn_ds_read_tr16_b64_v4i16(LDSP(s16x4, aw[2] + 4608));
  SB();
  MA1(acc[0][2], WF(8, 1, 0), X[8][1][2]); SB();
  Wh[8][1][2][1] = __builtin_amdgcn_ds_read_tr16_b64_v4i16(LDSP(s16x4, aw[2] + 6912));
  SB();
  MA1(acc[0][3], WF(8, 1, 0), X[8][1][3]); SB();
  Wh[8][0][3][0] = __builtin_amdgcn_ds_read_tr16_b64_v4i16(LDSP(s16x4, aw[3] + 0));
  SB();
  MA1(acc[0][4], WF(8, 1, 0), X[8][1][4]); SB();
  Wh[8][0][3][1] = __builtin_amdgcn_ds_read_tr16_b64_v4i16(LDSP(s16x4, aw[3] + 2304));
  SB();
  MA1(acc[1][0], WF(8, 0, 1), X[8][0][0]); SB();
  Wh[8][1][3][0] = __builtin_amdgcn_ds_read_tr16_b64_v4i16(LDSP(s16x4, aw[3] + 4608));
  SB();
  MA1(acc[1][1], WF(8, 0, 1), X[8][0][1]); SB();
  Wh[8][1][3][1] = __builtin_amdgcn_ds_read_tr16_b64_v4i16(LDSP(s16x4, aw[3] + 6912));
  EPI0(FD1, acc[0][1], acc[0][0], st[1], ae[0], 1280);
  SB();
  MA1(acc[1][2], WF(8, 0, 1), X[8][0][2]); SB();
  SB();
  MA1(acc[1][3], WF(8, 0, 1), X[8][0][3]); SB();
  EPI0(FD1, acc[0][2], acc[0][0], st[2], ae[0], 2560);
  SB();
  MA1(acc[1][4], WF(8, 0, 1), X[8][0][4]); SB();
  SB();
  MA1(acc[1][0], WF(8, 1, 1), X[8][1][0]); SB();
  EPI0(FD1, acc[0][3], acc[0][0], st[3], ae[0], 3840);
  SB();
  MA1(acc[1][1], WF(8, 1, 1), X[8][1][1]); SB();
  SB();
  MA1(acc[1][2], WF(8, 1, 1), X[8][1][2]); SB();
  EPI0(FD1, acc[0][4], acc[0][0], st[4], ae[0], 5120);
  SB();
  MA1(acc[1][3], WF(8, 1, 1), X[8][1][3]); SB();
  SB();
  MA1(acc[1][4], WF(8, 1, 1), X[8][1][4]); SB();
  EPI0(FD0, acc[0][0], acc[0][0], st[0], ae[0], 0);
  SB();
  MA1(acc[2][0], WF(8, 0, 2), X[8][0][0]); SB();
  SB();
  MA1(acc[2][1], WF(8, 0, 2), X[8][0][1]); SB();
  EPI0(FD1, acc[1][1], acc[1][0], st[1], ae[1], 1280);
  SB();
  MA1(acc[2][2], WF(8, 0, 2), X[8][0][2]); SB();
  SB();
  MA1(acc[2][3], WF(8, 0, 2), X[8][0][3]); SB();
  EPI0(FD1, acc[1][2], acc[1][0], st[2], ae[1], 2560);
  SB();
  MA1(acc[2][4], WF(8, 0, 2), X[8][0][4]); SB();
  SB();
  MA1(acc[2][0], WF(8, 1, 2), X[8][1][0]); SB();
  EPI0(FD1, acc[1][3], acc[1][0], st[3], ae[1], 3840);
  SB();
  MA1(acc[2][1], WF(8, 1, 2), X[8][1][1]); SB();
  SB();
  MA1(acc[2][2], WF(8, 1, 2), X[8][1][2]); SB();
  EPI0(FD1, acc[1][4], acc[1][0], st[4], ae[1], 5120);
  SB();
  MA1(acc[2][3], WF(8, 1, 2), X[8][1][3]); SB();
  SB();
  MA1(acc[2][4], WF(8, 1, 2), X[8][1][4]); SB();
  EPI0(FD0, acc[1][0], acc[1][0], st[0], ae[1], 0);
  SB();
  MA1(acc[3][0], WF(8, 0, 3), X[8][0][0]); SB();
  SB();
  MA1(acc[3][1], WF(8, 0, 3), X[8][0][1]); SB();
  EPI0(FD1, acc[2][1], acc[2][0], st[1], ae[2], 1280);
  SB();
  MA1(acc[3][2], WF(8, 0, 3), X[8][0][2]); SB();
  SB();
  MA1(acc[3][3], WF(8, 0, 3), X[8][0][3]); SB();
  EPI0(FD1, acc[2][2], acc[2][0], st[2], ae[2], 2560);
  SB();
  MA1(acc[3][4], WF(8, 0, 3), X[8][0][4]); SB();
  SB();
  MA1(acc[3][0], WF(8, 1, 3), X[8][1][0]); SB();
  EPI0(FD1, acc[2][3], acc[2][0], st[3], ae[2], 3840);
  SB();
  MA1(acc[3][1], WF(8, 1, 3), X[8][1][1]); SB();
  SB();
  MA1(acc[3][2], WF(8, 1, 3), X[8][1][2]); SB();
  EPI0(FD1, acc[2][4], acc[2][0], st[4], ae[2], 5120);
  SB();
  MA1(acc[3][3], WF(8, 1, 3), X[8][1][3]); SB();
  SB();
  MA1(acc[3][4], WF(8, 1, 3), X[8][1][4]); SB();
  EPI0(FD0, acc[2][0], acc[2][0], st[0], ae[2], 0);
  SB();
  NOP(4); SB();
  EPI1(FD1, acc[3][1], acc[3][0], st[1], ae[3], 1280); SB();
  EPI1(FD1, acc[3][2], acc[3][0], st[2], ae[3], 2560); SB();
  EPI1(FD1, acc[3][3], acc[3][0], st[3], ae[3], 3840); SB();
  EPI1(FD1, acc[3][4], acc[3][0], st[4], ae[3], 5120); SB();
  EPI1(FD0, acc[3][0], acc[3][0], st[0], ae[3], 0); SB();
  STAMPF(11);
  }
  SB();
  u32x16 own, ra, rs; u32x8 rb, hp, sg; u32x4 srO, srD; u32x2 mn, tg, fv; u32 stv, sen, t0s;
  asm volatile(
    "s_mov_b32 s78, 0\n"
    "v_mov_b32 v232, 0\n"
    "v_mov_b32 v233, 0\n"
    "s_mov_b32 s76, 0x3eae1a93\n"
    "s_waitcnt vmcnt(5) lgkmcnt(0)\n"
    "s_barrier\n"
    "ds_read_b128 v[64:67], v28 offset:0\n"
    "ds_read_b128 v[80:83], v29 offset:0\n"
    "ds_read_b128 v[84:87], v29 offset:2560\n"
    "ds_read_b128 v[68:71], v28 offset:32\n"
    "ds_read_b128 v[88:91], v29 offset:32\n"
    "ds_read_b128 v[92:95], v29 offset:2592\n"
    "ds_read_b128 v[72:75], v28 offset:6400\n"
    "ds_read_b128 v[96:99], v29 offset:6400\n"
    "ds_read_b128 v[100:103], v29 offset:8960\n"
    "ds_read_b128 v[76:79], v28 offset:6432\n"
    "ds_read_b128 v[104:107], v29 offset:6432\n"
    "ds_read_b128 v[108:111], v29 offset:8992\n"
    "v_readlane_b32 s92, v48, 32\n"
    "v_mov_b32 v61, v48\n"
    "s_load_dwordx2 s[40:41], s[94:95], 0x28\n"
    "s_load_dwordx2 s[48:49], s[94:95], 0x38\n"
    "s_mov_b32 s42, s38\n"
    "s_mov_b32 s43, s47\n"
    "s_mov_b32 s50, s82\n"
    "s_mov_b32 s51, s47\n"
    "s_lshr_b32 s73, s88, 11\n"
    "s_mul_i32 s73, s73, 2359296\n"
    "s_cmp_eq_u32 s88, 0\n"
    "s_cselect_b32 s93, s92, 0\n"
    "s_waitcnt lgkmcnt(11)\n"
    "v_and_b32 v64, v33, v64\n"
    "v_and_b32 v65, v34, v65\n"
    "v_and_b32 v66, v35, v66\n"
    "v_and_b32 v67, v36, v67\n"
    "ds_read_b64_tr_b16 v[112:113], v30 offset:0\n"
    "ds_read_b64_tr_b16 v[114:115], v30 offset:2560\n"
    "ds_read_b64_tr_b16 v[128:129], v30 offset:32\n"
    "ds_read_b64_tr_b16 v[130:131], v30 offset:2592\n"
    "s_waitcnt lgkmcnt(13)\n"
    "v_mfma_f32_16x16x32_bf16 v[136:139], v[80:83], v[64:67], v[16:19]\n"
    "v_mfma_f32_16x16x32_bf16 v[140:143], v[84:87], v[64:67], v[20:23]\n"
    "s_waitcnt lgkmcnt(12)\n"
    "v_and_b32 v68, v32, v68\n"
    "v_and_b32 v69, v33, v69\n"
    "v_and_b32 v70, v34, v70\n"
    "v_and_b32 v71, v35, v71\n"
    "ds_read_b64_tr_b16 v[116:117], v30 offset:40\n"
    "ds_read_b64_tr_b16 v[118:119], v30 offset:2600\n"
    "s_waitcnt lgkmcnt(12)\n"
    "v_mfma_f32_16x16x32_bf16 v[144:147], v[88:91], v[68:71], v[16:19]\n"
    "v_mfma_f32_16x16x32_bf16 v[148:151], v[92:95], v[68:71], v[20:23]\n"
    "s_waitcnt lgkmcnt(11)\n"
    "v_and_b32 v72, v33, v72\n"
    "v_and_b32 v73, v34, v73\n"
    "v_and_b32 v74, v35, v74\n"
    "v_and_b32 v75, v36, v75\n"
    "ds_read_b64_tr_b16 v[120:121], v30 offset:6400\n"
    "ds_read_b64_tr_b16 v[122:123], v30 offset:8960\n"
    "ds_read_b64_tr_b16 v[132:133], v30 offset:6432\n"
    "ds_read_b64_tr_b16 v[134:135], v30 offset:8992\n"
    "s_waitcnt lgkmcnt(13)\n"
    "v_mfma_f32_16x16x32_bf16 v[152:155], v[96:99], v[72:75], v[16:19]\n"
    "v_mfma_f32_16x16x32_bf16 v[156:159], v[100:103], v[72:75], v[20:23]\n"
    "s_waitcnt lgkmcnt(12)\n"
    "v_and_b32 v76, v32, v76\n"
    "v_and_b32 v77, v33, v77\n"
    "v_and_b32 v78, v34, v78\n"
    "v_and_b32 v79, v35, v79\n"
    "ds_read_b64_tr_b16 v[124:125], v30 offset:6440\n"
    "ds_read_b64_tr_b16 v[126:127], v30 offset:9000\n"
    "s_waitcnt lgkmcnt(12)\n"
    "v_mfma_f32_16x16x32_bf16 v[160:163], v[104:107], v[76:79], v[16:19]\n"
    "v_mfma_f32_16x16x32_bf16 v[164:167], v[108:111], v[76:79], v[20:23]\n"
    "ds_read_b64 v[224:225], v247 offset:0\n"
    "ds_read_b64 v[226:227], v247 offset:32\n"
    "ds_read_b64 v[228:229], v247 offset:64\n"
    "ds_read_b64 v[230:231], v247 offset:96\n"
    "s_mov_b32 exec_hi, 0\n"
    "ds_read_b64 v[232:233], v247 offset:128\n"
    "s_mov_b32 exec_hi, -1\n"
    "ds_read_b32 v248, v31 offset:0\n"
    "v_max_f32 v136, v136, v140\n"
    "v_max_f32 v137, v137, v141\n"
    "v_max_f32 v138, v138, v142\n"
    "v_max_f32 v139, v139, v143\n"
    "v_max3_f32 v168, v136, v137, v138\n"
    "v_max_f32 v168, v168, v139\n"
    "v_max_f32 v144, v144, v148\n"
    "v_max_f32 v145, v145, v149\n"
    "v_max_f32 v146, v146, v150\n"
    "v_max_f32 v147, v147, v151\n"
    "v_max3_f32 v170, v144, v145, v146\n"
    "v_max_f32 v170, v170, v147\n"
    "v_max_f32 v152, v152, v156\n"
    "v_max_f32 v153, v153, v157\n"
    "v_max_f32 v154, v154, v158\n"
    "v_max_f32 v155, v155, v159\n"
    "v_max3_f32 v172, v152, v153, v154\n"
    "v_max_f32 v172, v172, v155\n"
    "v_max_f32 v160, v160, v164\n"
    "v_max_f32 v161, v161, v165\n"
    "v_max_f32 v162, v162, v166\n"
    "v_max_f32 v163, v163, v167\n"
    "v_max3_f32 v174, v160, v161, v162\n"
    "v_max_f32 v174, v174, v163\n"
    "s_waitcnt lgkmcnt(0)\n"
    "v_mov_b32 v169, v168\n"
    "v_mov_b32 v171, v170\n"
    "v_mov_b32 v173, v172\n"
    "v_mov_b32 v175, v174\n"
    "v_permlane16_swap_b32 v168, v169\n"
    "v_permlane16_swap_b32 v170, v171\n"
    "v_permlane16_swap_b32 v172, v173\n"
    "v_permlane16_swap_b32 v174, v175\n"
    "v_max_f32 v168, v168, v169\n"
    "v_max_f32 v170, v170, v171\n"
    "v_max_f32 v172, v172, v173\n"
    "v_max_f32 v174, v174, v175\n"
    "v_mov_b32 v169, v168\n"
    "v_mov_b32 v171, v170\n"
    "v_mov_b32 v173, v172\n"
    "v_mov_b32 v175, v174\n"
    "v_permlane32_swap_b32 v168, v169\n"
    "v_permlane32_swap_b32 v170, v171\n"
    "v_permlane32_swap_b32 v172, v173\n"
    "v_permlane32_swap_b32 v174, v175\n"
    "v_max_f32 v168, v168, v169\n"
    "v_max_f32 v170, v170, v171\n"
    "v_max_f32 v172, v172, v173\n"
    "v_max_f32 v174, v174, v175\n"
    "v_mul_f32 v168, 0xbeae1a93, v168\n"
    "v_mul_f32 v170, 0xbeae1a93, v170\n"
    "v_mul_f32 v172, 0xbeae1a93, v172\n"
    "v_mul_f32 v174, 0xbeae1a93, v174\n"
    "v_fma_f32 v136, v136, s76, v168\n"
    "v_fma_f32 v137, v137, s76, v168\n"
    "v_fma_f32 v138, v138, s76, v168\n"
    "v_fma_f32 v139, v139, s76, v168\n"
    "v_exp_f32 v136, v136\n"
    "v_exp_f32 v137, v137\n"
    "v_exp_f32 v138, v138\n"
    "v_exp_f32 v139, v139\n"
    "v_cvt_pk_bf16_f32 v140, v136, v137\n"
    "v_cvt_pk_bf16_f32 v141, v138, v139\n"
    "v_and_b32 v64, v24, v140\n"
    "v_and_b32 v65, v25, v141\n"
    "v_and_b32 v66, v26, v140\n"
    "v_and_b32 v67, v27, v141\n"
    "s_nop 1\n"
    "v_mfma_f32_16x16x32_bf16 v[80:83], v[44:47], v[64:67], 0\n"
    "v_mfma_f32_16x16x32_bf16 v[96:99], v[112:115], v[64:67], 0\n"
    "v_mfma_f32_16x16x32_bf16 v[176:179], v[128:131], v[64:67], 0\n"
    "v_fma_f32 v144, v144, s76, v170\n"
    "v_fma_f32 v145, v145, s76, v170\n"
    "v_fma_f32 v146, v146, s76, v170\n"
    "v_fma_f32 v147, v147, s76, v170\n"
    "v_exp_f32 v144, v144\n"
    "v_exp_f32 v145, v145\n"
    "v_exp_f32 v146, v146\n"
    "v_exp_f32 v147, v147\n"
    "v_cvt_pk_bf16_f32 v148, v144, v145\n"
    "v_cvt_pk_bf16_f32 v149, v146, v147\n"
    "v_and_b32 v68, v24, v148\n"
    "v_and_b32 v69, v25, v149\n"
    "v_and_b32 v70, v26, v148\n"
    "v_and_b32 v71, v27, v149\n"
    "s_nop 1\n"
    "v_mfma_f32_16x16x32_bf16 v[84:87], v[44:47], v[68:71], 0\n"
    "v_mfma_f32_16x16x32_bf16 v[100:103], v[116:119], v[68:71], 0\n"
    "v_mfma_f32_16x16x32_bf16 v[180:183], v[128:131], v[68:71], 0\n"
    "v_fma_f32 v152, v152, s76, v172\n"
    "v_fma_f32 v153, v153, s76, v172\n"
    "v_fma_f32 v154, v154, s76, v172\n"
    "v_fma_f32 v155, v155, s76, v172\n"
    "v_exp_f32 v152, v152\n"
    "v_exp_f32 v153, v153\n"
    "v_exp_f32 v154, v154\n"
    "v_exp_f32 v155, v155\n"
    "v_cvt_pk_bf16_f32 v156, v152, v153\n"
    "v_cvt_pk_bf16_f32 v157, v154, v155\n"
    "v_and_b32 v72, v24, v156\n"
    "v_and_b32 v73, v25, v157\n"
    "v_and_b32 v74, v26, v156\n"
    "v_and_b32 v75, v27, v157\n"
    "s_nop 1\n"
    "v_mfma_f32_16x16x32_bf16 v[88:91], v[44:47], v[72:75], 0\n"
    "v_mfma_f32_16x16x32_bf16 v[104:107], v[120:123], v[72:75], 0\n"
    "v_mfma_f32_16x16x32_bf16 v[184:187], v[132:135], v[72:75], 0\n"
    "v_fma_f32 v160, v160, s76, v174\n"
    "v_fma_f32 v161, v161, s76, v174\n"
    "v_fma_f32 v162, v162, s76, v174\n"
    "v_fma_f32 v163, v163, s76, v174\n"
    "v_exp_f32 v160, v160\n"
    "v_exp_f32 v161, v161\n"
    "v_exp_f32 v162, v162\n"
    "v_exp_f32 v163, v163\n"
    "v_cvt_pk_bf16_f32 v164, v160, v161\n"
    "v_cvt_pk_bf16_f32 v165, v162, v163\n"
    "v_and_b32 v76, v24, v164\n"
    "v_and_b32 v77, v25, v165\n"
    "v_and_b32 v78, v26, v164\n"
    "v_and_b32 v79, v27, v165\n"
    "s_nop 1\n"
    "v_mfma_f32_16x16x32_bf16 v[92:95], v[44:47], v[76:79], 0\n"
    "v_mfma_f32_16x16x32_bf16 v[108:111], v[124:127], v[76:79], 0\n"
    "v_mfma_f32_16x16x32_bf16 v[188:191], v[132:135], v[76:79], 0\n"
    "v_rcp_f32 v80, v80\n"
    "s_nop 0\n"
    "v_pk_mul_f32 v[96:97], v[96:97], v[80:81] op_sel_hi:[1,0]\n"
    "v_pk_mul_f32 v[98:99], v[98:99], v[80:81] op_sel_hi:[1,0]\n"
    "v_pk_mul_f32 v[176:177], v[176:177], v[80:81] op_sel_hi:[1,0]\n"
    "v_cvt_pk_bf16_f32 v136, v96, v97\n"
    "v_cvt_pk_bf16_f32 v137, v98, v99\n"
    "v_cvt_pk_bf16_f32 v144, v176, v177\n"
    "v_min_u32 v136, -2, v136\n"
    "v_min_u32 v137, -2, v137\n"
    "v_min_u32 v144, -2, v144\n"
    "v_rcp_f32 v84, v84\n"
    "s_nop 0\n"
    "v_pk_mul_f32 v[100:101], v[100:101], v[84:85] op_sel_hi:[1,0]\n"
    "v_pk_mul_f32 v[102:103], v[102:103], v[84:85] op_sel_hi:[1,0]\n"
    "v_pk_mul_f32 v[182:183], v[182:183], v[84:85] op_sel_hi:[1,0]\n"
    "v_cvt_pk_bf16_f32 v138, v100, v101\n"
    "v_cvt_pk_bf16_f32 v139, v102, v103\n"
    "v_cvt_pk_bf16_f32 v145, v182, v183\n"
    "v_min_u32 v138, -2, v138\n"
    "v_min_u32 v139, -2, v139\n"
    "v_min_u32 v145, -2, v145\n"
    "buffer_store_dwordx4 v[136:139], v0, s[44:47], s90 offen offset:0 sc1\n"
    "v_rcp_f32 v88, v88\n"
    "s_nop 0\n"
    "v_pk_mul_f32 v[104:105], v[104:105], v[88:89] op_sel_hi:[1,0]\n"
    "v_pk_mul_f32 v[106:107], v[106:107], v[88:89] op_sel_hi:[1,0]\n"
    "v_pk_mul_f32 v[184:185], v[184:185], v[88:89] op_sel_hi:[1,0]\n"
    "v_cvt_pk_bf16_f32 v140, v104, v105\n"
    "v_cvt_pk_bf16_f32 v141, v106, v107\n"
    "v_cvt_pk_bf16_f32 v146, v184, v185\n"
    "v_min_u32 v140, -2, v140\n"
    "v_min_u32 v141, -2, v141\n"
    "v_min_u32 v146, -2, v146\n"
    "v_rcp_f32 v92, v92\n"
    "s_nop 0\n"
    "v_pk_mul_f32 v[108:109], v[108:109], v[92:93] op_sel_hi:[1,0]\n"
    "v_pk_mul_f32 v[110:111], v[110:111], v[92:93] op_sel_hi:[1,0]\n"
    "v_pk_mul_f32 v[190:191], v[190:191], v[92:93] op_sel_hi:[1,0]\n"
    "v_cvt_pk_bf16_f32 v142, v108, v109\n"
    "v_cvt_pk_bf16_f32 v143, v110, v111\n"
    "v_cvt_pk_bf16_f32 v147, v190, v191\n"
    "v_min_u32 v142, -2, v142\n"
    "v_min_u32 v143, -2, v143\n"
    "v_min_u32 v147, -2, v147\n"
    "buffer_store_dwordx4 v[140:143], v0, s[44:47], s90 offen offset:2048 sc1\n"
    "buffer_store_dwordx4 v[144:147], v42, s[44:47], s91 offen sc1\n"
    "ds_read_b64_tr_b16 v[160:161], v240 offset:128\n"
    "ds_read_b64_tr_b16 v[162:163], v240 offset:2432\n"
    "ds_read_b64_tr_b16 v[164:165], v240 offset:4736\n"
    "ds_read_b64_tr_b16 v[166:167], v240 offset:7040\n"
    "ds_read_b64_tr_b16 v[96:97], v240 offset:9344\n"
    "s_mul_i32 s76, s88, 9\n"
    "s_add_u32 m0, s76, 114688\n"
    "s_add_u32 s76, s89, 165888\n"
    "s_cmp_eq_u32 s88, 3072\n"
    "s_cselect_b64 exec, 0, -1\n"
    "buffer_load_dwordx4 v243, s[84:87], s76 offen lds\n"
    "s_mov_b64 exec, -1\n"
    "ds_read_b64_tr_b16 v[98:99], v240 offset:11648\n"
    "ds_read_b64_tr_b16 v[100:101], v240 offset:13952\n"
    "ds_read_b64_tr_b16 v[102:103], v240 offset:16256\n"
    "ds_read_b64_tr_b16 v[104:105], v240 offset:18560\n"
    "s_add_u32 m0, s88, 106496\n"
    "s_add_u32 s76, s89, 165888\n"
    "buffer_load_dwordx4 v37, s[84:87], s76 offen lds\n"
    "ds_read_b64_tr_b16 v[106:107], v240 offset:20864\n"
    "ds_read_b64_tr_b16 v[108:109], v240 offset:23168\n"
    "ds_read_b64_tr_b16 v[110:111], v240 offset:25472\n"
    "s_mov_b32 exec_lo, 0x03000300\n"
    "s_mov_b32 exec_hi, 0x03000300\n"
    "ds_read_b128 v[160:163], v241 offset:11264\n"
    "ds_read_b128 v[164:167], v241 offset:11392\n"
    "ds_read_b128 v[96:99], v241 offset:9216\n"
    "ds_read_b128 v[100:103], v241 offset:9344\n"
    "ds_read_b128 v[104:107], v241 offset:9472\n"
    "ds_read_b128 v[108:111], v241 offset:9600\n"
    "s_mov_b64 exec, -1\n"
    "ds_read_b64_tr_b16 a[0:1], v240 offset:0\n"
    "s_add_u32 m0, s88, 110592\n"
    "s_add_u32 s76, s89, 165888\n"
    "buffer_load_dwordx4 v38, s[84:87], s76 offen lds\n"
    "ds_read_b64_tr_b16 a[2:3], v240 offset:2304\n"
    "ds_read_b64_tr_b16 a[4:5], v240 offset:32\n"
    "ds_read_b64_tr_b16 a[6:7], v240 offset:2336\n"
    "ds_read_b64_tr_b16 a[8:9], v240 offset:64\n"
    "s_add_u32 m0, s88, 115712\n"
    "s_add_u32 s76, s89, 248832\n"
    "buffer_load_dwordx4 v37, s[84:87], s76 offen lds\n"
    "ds_read_b64_tr_b16 a[10:11], v240 offset:2368\n"
    "ds_read_b64_tr_b16 a[12:13], v240 offset:96\n"
    "ds_read_b64_tr_b16 a[14:15], v240 offset:2400\n"
    "ds_read_b64_tr_b16 a[16:17], v240 offset:4608\n"
    "ds_read_b64_tr_b16 a[18:19], v240 offset:6912\n"
    "s_add_u32 m0, s88, 119808\n"
    "s_add_u32 s76, s89, 248832\n"
    "buffer_load_dwordx4 v38, s[84:87], s76 offen lds\n"
    "ds_read_b64_tr_b16 a[20:21], v240 offset:4640\n"
    "ds_read_b64_tr_b16 a[22:23], v240 offset:6944\n"
    "ds_read_b64_tr_b16 a[24:25], v240 offset:4672\n"
    "ds_read_b64_tr_b16 a[26:27], v240 offset:6976\n"
    "s_add_u32 m0, s88, 124928\n"
    "s_add_u32 s76, s89, 331776\n"
    "buffer_load_dwordx4 v37, s[84:87], s76 offen lds\n"
    "ds_read_b64_tr_b16 a[28:29], v240 offset:4704\n"
    "ds_read_b64_tr_b16 a[30:31], v240 offset:7008\n"
    "ds_read_b64_tr_b16 a[32:33], v240 offset:9216\n"
    "ds_read_b64_tr_b16 a[34:35], v240 offset:11520\n"
    "ds_read_b64_tr_b16 a[36:37], v240 offset:9248\n"
    "s_add_u32 m0, s88, 129024\n"
    "s_add_u32 s76, s89, 331776\n"
    "buffer_load_dwordx4 v38, s[84:87], s76 offen lds\n"
    "ds_read_b64_tr_b16 a[38:39], v240 offset:11552\n"
    "ds_read_b64_tr_b16 a[40:41], v240 offset:9280\n"
    "ds_read_b64_tr_b16 a[42:43], v240 offset:11584\n"
    "ds_read_b64_tr_b16 a[44:45], v240 offset:9312\n"
    "s_mul_i32 s76, s88, 9\n"
    "s_add_u32 m0, s76, 142336\n"
    "s_add_u32 s76, s89, 414720\n"
    "s_cmp_eq_u32 s88, 3072\n"
    "s_cselect_b64 exec, 0, -1\n"
    "buffer_load_dwordx4 v243, s[84:87], s76 offen lds\n"
    "s_mov_b64 exec, -1\n"
    "ds_read_b64_tr_b16 a[46:47], v240 offset:11616\n"
    "ds_read_b64_tr_b16 a[48:49], v240 offset:13824\n"
    "ds_read_b64_tr_b16 a[50:51], v240 offset:16128\n"
    "ds_read_b64_tr_b16 a[52:53], v240 offset:13856\n"
    "s_add_u32 m0, s88, 134144\n"
    "s_add_u32 s76, s89, 414720\n"
    "buffer_load_dwordx4 v37, s[84:87], s76 offen lds\n"
    "ds_read_b64_tr_b16 a[54:55], v240 offset:16160\n"
    "ds_read_b64_tr_b16 a[56:57], v240 offset:13888\n"
    "ds_read_b64_tr_b16 a[58:59], v240 offset:16192\n"
    "ds_read_b64_tr_b16 a[60:61], v240 offset:13920\n"
    "ds_read_b64_tr_b16 a[62:63], v240 offset:16224\n"
    "s_add_u32 m0, s88, 138240\n"
    "s_add_u32 s76, s89, 414720\n"
    "buffer_load_dwordx4 v38, s[84:87], s76 offen lds\n"
    "ds_read_b64_tr_b16 a[64:65], v240 offset:18432\n"
    "ds_read_b64_tr_b16 a[66:67], v240 offset:20736\n"
    "ds_read_b64_tr_b16 a[68:69], v240 offset:18464\n"
    "ds_read_b64_tr_b16 a[70:71], v240 offset:20768\n"
    "s_add_u32 m0, s88, 143360\n"
    "s_add_u32 s76, s89, 497664\n"
    "buffer_load_dwordx4 v37, s[84:87], s76 offen lds\n"
    "ds_read_b64_tr_b16 a[72:73], v240 offset:18496\n"
    "ds_read_b64_tr_b16 a[74:75], v240 offset:20800\n"
    "ds_read_b64_tr_b16 a[76:77], v240 offset:18528\n"
    "ds_read_b64_tr_b16 a[78:79], v240 offset:20832\n"
    "ds_read_b64_tr_b16 a[80:81], v240 offset:23040\n"
    "s_add_u32 s79, s33, 32768\n"
    "buffer_load_dwordx4 v[88:91], v0, s[44:47], s79 offen offset:0 sc1\n"
    "ds_read_b64_tr_b16 a[82:83], v240 offset:25344\n"
    "ds_read_b64_tr_b16 a[84:85], v240 offset:23072\n"
    "ds_read_b64_tr_b16 a[86:87], v240 offset:25376\n"
    "ds_read_b64_tr_b16 a[88:89], v240 offset:23104\n"
    "buffer_load_dwordx4 v[92:95], v0, s[44:47], s79 offen offset:2048 sc1\n"
    "ds_read_b64_tr_b16 a[90:91], v240 offset:25408\n"
    "ds_read_b64_tr_b16 a[92:93], v240 offset:23136\n"
    "ds_read_b64_tr_b16 a[94:95], v240 offset:25440\n"
    "v_lshlrev_b32 v200, 16, v224\n"
    "v_and_b32 v201, 0xffff0000, v224\n"
    "v_lshlrev_b32 v202, 16, v225\n"
    "v_and_b32 v203, 0xffff0000, v225\n"
    "s_add_u32 m0, s88, 147456\n"
    "s_add_u32 s76, s89, 497664\n"
    "buffer_load_dwordx4 v38, s[84:87], s76 offen lds\n"
    "v_lshlrev_b32 v204, 16, v226\n"
    "v_and_b32 v205, 0xffff0000, v226\n"
    "v_lshlrev_b32 v206, 16, v227\n"
    "v_and_b32 v207, 0xffff0000, v227\n"
    "v_lshlrev_b32 v208, 16, v228\n"
    "v_and_b32 v209, 0xffff0000, v228\n"
    "v_lshlrev_b32 v210, 16, v229\n"
    "v_and_b32 v211, 0xffff0000, v229\n"
    "s_add_u32 m0, s88, 152576\n"
    "s_add_u32 s76, s89, 580608\n"
    "buffer_load_dwordx4 v37, s[84:87], s76 offen lds\n"
    "v_lshlrev_b32 v212, 16, v230\n"
    "v_and_b32 v213, 0xffff0000, v230\n"
    "v_lshlrev_b32 v214, 16, v231\n"
    "v_and_b32 v215, 0xffff0000, v231\n"
    "v_lshlrev_b32 v216, 16, v232\n"
    "v_and_b32 v217, 0xffff0000, v232\n"
    "v_lshlrev_b32 v218, 16, v233\n"
    "v_and_b32 v219, 0xffff0000, v233\n"
    "s_add_u32 m0, s88, 156672\n"
    "s_add_u32 s76, s89, 580608\n"
    "buffer_load_dwordx4 v38, s[84:87], s76 offen lds\n"
    "s_waitcnt vmcnt(9)\n"
    "s_barrier\n"
    "ds_read_b64_tr_b16 v[112:113], v240 offset:27776\n"
    "ds_read_b64_tr_b16 v[114:115], v240 offset:30080\n"
    "ds_read_b64_tr_b16 v[116:117], v240 offset:32384\n"
    "ds_read_b64_tr_b16 v[118:119], v240 offset:34688\n"
    "ds_read_b64_tr_b16 v[120:121], v240 offset:36992\n"
    "ds_read_b64_tr_b16 v[122:123], v240 offset:39296\n"
    "ds_read_b64_tr_b16 v[124:125], v242 offset:128\n"
    "ds_read_b64_tr_b16 v[126:127], v242 offset:2432\n"
    "ds_read_b64_tr_b16 v[128:129], v242 offset:4736\n"
    "ds_read_b64_tr_b16 v[130:131], v242 offset:7040\n"
    "ds_read_b64_tr_b16 v[132:133], v242 offset:9344\n"
    "ds_read_b64_tr_b16 v[134:135], v242 offset:11648\n"
    "s_mov_b32 exec_lo, 0x03000300\n"
    "s_mov_b32 exec_hi, 0x03000300\n"
    "ds_read_b128 v[112:115], v241 offset:9728\n"
    "ds_read_b128 v[116:119], v241 offset:9856\n"
    "ds_read_b128 v[120:123], v241 offset:9984\n"
    "ds_read_b128 v[124:127], v241 offset:10112\n"
    "ds_read_b128 v[128:131], v241 offset:10240\n"
    "ds_read_b128 v[132:135], v241 offset:10368\n"
    "s_mov_b64 exec, -1\n"
    "ds_read_b64_tr_b16 a[96:97], v240 offset:27648\n"
    "ds_read_b64_tr_b16 a[98:99], v240 offset:29952\n"
    "ds_read_b64_tr_b16 a[100:101], v240 offset:27680\n"
    "ds_read_b64_tr_b16 a[102:103], v240 offset:29984\n"
    "ds_read_b64_tr_b16 a[104:105], v240 offset:27712\n"
    "ds_read_b64_tr_b16 a[106:107], v240 offset:30016\n"
    "s_waitcnt vmcnt(3)\n"
    "v_max3_u32 v247, v88, v89, v90\n"
    "v_max3_u32 v247, v91, v92, v247\n"
    "v_max3_u32 v247, v93, v94, v247\n"
    "v_max_u32 v247, v95, v247\n"
    "v_cmp_eq_u32 vcc, -1, v247\n"
    "s_cbranch_vccnz LC0_k\n"
    "LD0_k:\n"
    "v_lshrrev_b32 v243, 2, v0\n"
    "v_and_b32 v243, 0x7c, v243\n"
    "v_add_u32 v243, s35, v243\n"
    "s_add_u32 s79, s33, 0\n"
    "buffer_load_dwordx4 v[16:19], v0, s[44:47], s79 offen offset:0 sc1\n"
    "ds_read_b64_tr_b16 a[108:109], v240 offset:27744\n"
    "ds_read_b64_tr_b16 a[110:111], v240 offset:30048\n"
    "buffer_load_dwordx4 v[20:23], v0, s[44:47], s79 offen offset:2048 sc1\n"
    "ds_read_b64_tr_b16 a[112:113], v240 offset:32256\n"
    "ds_read_b64_tr_b16 a[114:115], v240 offset:34560\n"
    "ds_read_b64_tr_b16 a[116:117], v240 offset:32288\n"
    "s_add_u32 s79, s33, 4096\n"
    "buffer_load_dwordx4 v[24:27], v0, s[44:47], s79 offen offset:0 sc1\n"
    "ds_read_b64_tr_b16 a[118:119], v240 offset:34592\n"
    "ds_read_b64_tr_b16 a[120:121], v240 offset:32320\n"
    "buffer_load_dwordx4 v[28:31], v0, s[44:47], s79 offen offset:2048 sc1\n"
    "ds_read_b64_tr_b16 a[122:123], v240 offset:34624\n"
    "ds_read_b64_tr_b16 a[124:125], v240 offset:32352\n"
    "ds_read_b64_tr_b16 a[126:127], v240 offset:34656\n"
    "s_add_u32 s79, s33, 8192\n"
    "buffer_load_dwordx4 v[32:35], v0, s[44:47], s79 offen offset:0 sc1\n"
    "ds_read_b64_tr_b16 a[128:129], v240 offset:36864\n"
    "ds_read_b64_tr_b16 a[130:131], v240 offset:39168\n"
    "buffer_load_dwordx4 v[36:39], v0, s[44:47], s79 offen offset:2048 sc1\n"
    "ds_read_b64_tr_b16 a[132:133], v240 offset:36896\n"
    "ds_read_b64_tr_b16 a[134:135], v240 offset:39200\n"
    "ds_read_b64_tr_b16 a[136:137], v240 offset:36928\n"
    "s_add_u32 s79, s33, 12288\n"
    "buffer_load_dwordx4 v[40:43], v0, s[44:47], s79 offen offset:0 sc1\n"
    "ds_read_b64_tr_b16 a[138:139], v240 offset:39232\n"
    "ds_read_b64_tr_b16 a[140:141], v240 offset:36960\n"
    "buffer_load_dwordx4 v[44:47], v0, s[44:47], s79 offen offset:2048 sc1\n"
    "ds_read_b64_tr_b16 a[142:143], v240 offset:39264\n"
    "ds_read_b64_tr_b16 a[144:145], v242 offset:0\n"
    "ds_read_b64_tr_b16 a[146:147], v242 offset:2304\n"
    "s_add_u32 s79, s33, 16384\n"
    "buffer_load_dwordx4 v[48:51], v0, s[44:47], s79 offen offset:0 sc1\n"
    "ds_read_b64_tr_b16 a[148:149], v242 offset:32\n"
    "ds_read_b64_tr_b16 a[150:151], v242 offset:2336\n"
    "buffer_load_dwordx4 v[52:55], v0, s[44:47], s79 offen offset:2048 sc1\n"
    "ds_read_b64_tr_b16 a[152:153], v242 offset:64\n"
    "ds_read_b64_tr_b16 a[154:155], v242 offset:2368\n"
    "ds_read_b64_tr_b16 a[156:157], v242 offset:96\n"
    "s_add_u32 s79, s33, 20480\n"
    "buffer_load_dwordx4 v[64:67], v0, s[44:47], s79 offen offset:0 sc1\n"
    "ds_read_b64_tr_b16 a[158:159], v242 offset:2400\n"
    "ds_read_b64_tr_b16 a[160:161], v242 offset:4608\n"
    "buffer_load_dwordx4 v[68:71], v0, s[44:47], s79 offen offset:2048 sc1\n"
    "ds_read_b64_tr_b16 a[162:163], v242 offset:6912\n"
    "ds_read_b64_tr_b16 a[164:165], v242 offset:4640\n"
    "ds_read_b64_tr_b16 a[166:167], v242 offset:6944\n"
    "s_add_u32 s79, s33, 24576\n"
    "buffer_load_dwordx4 v[72:75], v0, s[44:47], s79 offen offset:0 sc1\n"
    "ds_read_b64_tr_b16 a[168:169], v242 offset:4672\n"
    "ds_read_b64_tr_b16 a[170:171], v242 offset:6976\n"
    "buffer_load_dwordx4 v[76:79], v0, s[44:47], s79 offen offset:2048 sc1\n"
    "ds_read_b64_tr_b16 a[172:173], v242 offset:4704\n"
    "ds_read_b64_tr_b16 a[174:175], v242 offset:7008\n"
    "ds_read_b64_tr_b16 a[176:177], v242 offset:9216\n"
    "s_add_u32 s79, s33, 28672\n"
    "buffer_load_dwordx4 v[80:83], v0, s[44:47], s79 offen offset:0 sc1\n"
    "ds_read_b64_tr_b16 a[178:179], v242 offset:11520\n"
    "ds_read_b64_tr_b16 a[180:181], v242 offset:9248\n"
    "buffer_load_dwordx4 v[84:87], v0, s[44:47], s79 offen offset:2048 sc1\n"
    "ds_read_b64_tr_b16 a[182:183], v242 offset:11552\n"
    "ds_read_b64_tr_b16 a[184:185], v242 offset:9280\n"
    "ds_read_b64_tr_b16 a[186:187], v242 offset:11584\n"
    "ds_read_b64_tr_b16 a[188:189], v242 offset:9312\n"
    "ds_read_b64_tr_b16 a[190:191], v242 offset:11616\n"
    "s_waitcnt vmcnt(16)\n"
    "s_barrier\n"
    "ds_read_b64_tr_b16 v[148:149], v242 offset:27776\n"
    "ds_read_b64_tr_b16 v[150:151], v242 offset:30080\n"
    "ds_read_b64_tr_b16 v[152:153], v242 offset:32384\n"
    "ds_read_b64_tr_b16 v[154:155], v242 offset:34688\n"
    "ds_read_b64_tr_b16 v[156:157], v242 offset:36992\n"
    "ds_read_b64_tr_b16 v[158:159], v242 offset:39296\n"
    "ds_read_b64_tr_b16 v[136:137], v242 offset:13952\n"
    "s_waitcnt lgkmcnt(15)\n"
    "v_mfma_f32_16x16x32_bf16 v[216:219], v[160:163], v[88:91], v[216:219]\n"
    "ds_read_b64_tr_b16 v[138:139], v242 offset:16256\n"
    "s_add_u32 m0, s52, 16384\n"
    "s_add_u32 s79, s54, 589824\n"
    "s_add_u32 s79, s79, s73\n"
    "buffer_load_dwordx4 v1, s[36:39], s79 offen lds\n"
    "v_mfma_f32_16x16x32_bf16 v[200:203], a[0:3], v[88:91], v[200:203]\n"
    "ds_read_b64_tr_b16 v[140:141], v242 offset:18560\n"
    "s_add_u32 s79, s79, 589824\n"
    "buffer_load_dwordx4 v2, s[36:39], s79 offen offset:1024 lds\n"
    "v_mfma_f32_16x16x32_bf16 v[204:207], a[4:7], v[88:91], v[204:207]\n"
    "ds_read_b64_tr_b16 v[142:143], v242 offset:20864\n"
    "v_mfma_f32_16x16x32_bf16 v[208:211], a[8:11], v[88:91], v[208:211]\n"
    "ds_read_b64_tr_b16 v[144:145], v242 offset:23168\n"
    "v_mfma_f32_16x16x32_bf16 v[212:215], a[12:15], v[88:91], v[212:215]\n"
    "ds_read_b64_tr_b16 v[146:147], v242 offset:25472\n"
    "v_mfma_f32_16x16x32_bf16 v[216:219], v[164:167], v[92:95], v[216:219]\n"
    "s_mov_b32 exec_lo, 0x03000300\n"
    "s_mov_b32 exec_hi, 0x03000300\n"
    "ds_read_b128 v[136:139], v241 offset:10496\n"
    "ds_read_b128 v[140:143], v241 offset:10624\n"
    "ds_read_b128 v[144:147], v241 offset:10752\n"
    "ds_read_b128 v[148:151], v241 offset:10880\n"
    "ds_read_b128 v[152:155], v241 offset:11008\n"
    "ds_read_b128 v[156:159], v241 offset:11136\n"
    "s_mov_b64 exec, -1\n"
    "s_add_u32 s79, s79, 589824\n"
    "buffer_load_dwordx4 v3, s[36:39], s79 offen offset:2048 lds\n"
    "v_mfma_f32_16x16x32_bf16 v[200:203], a[16:19], v[92:95], v[200:203]\n"
    "ds_read_b64_tr_b16 a[192:193], v242 offset:13824\n"
    "s_add_u32 s79, s79, 589824\n"
    "buffer_load_dwordx4 v4, s[36:39], s79 offen offset:3072 lds\n"
    "v_mfma_f32_16x16x32_bf16 v[204:207], a[20:23], v[92:95], v[204:207]\n"
    "ds_read_b64_tr_b16 a[194:195], v242 offset:16128\n"
    "s_waitcnt vmcnt(18)\n"
    "v_max3_u32 v247, v16, v17, v18\n"
    "v_max3_u32 v247, v19, v20, v247\n"
    "v_mfma_f32_16x16x32_bf16 v[208:211], a[24:27], v[92:95], v[208:211]\n"
    "ds_read_b64_tr_b16 a[196:197], v242 offset:13856\n"
    "v_max3_u32 v247, v21, v22, v247\n"
    "v_max_u32 v247, v23, v247\n"
    "v_mfma_f32_16x16x32_bf16 v[212:215], a[28:31], v[92:95], v[212:215]\n"
    "ds_read_b64_tr_b16 a[198:199], v242 offset:16160\n"
    "v_cmp_eq_u32 vcc, -1, v247\n"
    "s_cbranch_vccnz LC2_k\n"
    "LD2_k:\n"
    "v_mfma_f32_16x16x32_bf16 v[216:219], v[96:99], v[16:19], v[216:219]\n"
    "ds_read_b64_tr_b16 a[200:201], v242 offset:13888\n"
    "s_add_u32 m0, s52, 32768\n"
    "s_add_u32 s79, s54, 0\n"
    "buffer_load_dwordx4 v1, s[36:39], s79 offen lds\n"
    "v_mfma_f32_16x16x32_bf16 v[200:203], a[32:35], v[16:19], v[200:203]\n"
    "ds_read_b64_tr_b16 a[202:203], v242 offset:16192\n"
    "buffer_load_dwordx4 v2, s[36:39], s79 offen offset:1024 lds\n"
    "v_mfma_f32_16x16x32_bf16 v[204:207], a[36:39], v[16:19], v[204:207]\n"
    "ds_read_b64_tr_b16 a[204:205], v242 offset:13920\n"
    "v_mfma_f32_16x16x32_bf16 v[208:211], a[40:43], v[16:19], v[208:211]\n"
    "ds_read_b64_tr_b16 a[206:207], v242 offset:16224\n"
    "v_mfma_f32_16x16x32_bf16 v[212:215], a[44:47], v[16:19], v[212:215]\n"
    "ds_read_b64_tr_b16 a[208:209], v242 offset:18432\n"
    "v_mfma_f32_16x16x32_bf16 v[216:219], v[100:103], v[20:23], v[216:219]\n"
    "ds_read_b64_tr_b16 a[210:211], v242 offset:20736\n"
    "buffer_load_dwordx4 v3, s[36:39], s79 offen offset:2048 lds\n"
    "v_mfma_f32_16x16x32_bf16 v[200:203], a[48:51], v[20:23], v[200:203]\n"
    "ds_read_b64_tr_b16 a[212:213], v242 offset:18464\n"
    "buffer_load_dwordx4 v4, s[36:39], s79 offen offset:3072 lds\n"
    "v_mfma_f32_16x16x32_bf16 v[204:207], a[52:55], v[20:23], v[204:207]\n"
    "ds_read_b64_tr_b16 a[214:215], v242 offset:20768\n"
    "s_waitcnt vmcnt(20)\n"
    "v_max3_u32 v247, v24, v25, v26\n"
    "v_max3_u32 v247, v27, v28, v247\n"
    "v_mfma_f32_16x16x32_bf16 v[208:211], a[56:59], v[20:23], v[208:211]\n"
    "ds_read_b64_tr_b16 a[216:217], v242 offset:18496\n"
    "v_max3_u32 v247, v29, v30, v247\n"
    "v_max_u32 v247, v31, v247\n"
    "v_mfma_f32_16x16x32_bf16 v[212:215], a[60:63], v[20:23], v[212:215]\n"
    "ds_read_b64_tr_b16 a[218:219], v242 offset:20800\n"
    "v_cmp_eq_u32 vcc, -1, v247\n"
    "s_cbranch_vccnz LC4_k\n"
    "LD4_k:\n"
    "v_mfma_f32_16x16x32_bf16 v[216:219], v[104:107], v[24:27], v[216:219]\n"
    "ds_read_b64_tr_b16 a[220:221], v242 offset:18528\n"
    "s_add_u32 m0, s52, 49152\n"
    "s_add_u32 s79, s54, 663552\n"
    "buffer_load_dwordx4 v1, s[36:39], s79 offen lds\n"
    "v_mfma_f32_16x16x32_bf16 v[200:203], a[64:67], v[24:27], v[200:203]\n"
    "ds_read_b64_tr_b16 a[222:223], v242 offset:20832\n"
    "buffer_load_dwordx4 v2, s[36:39], s79 offen offset:1024 lds\n"
    "v_mfma_f32_16x16x32_bf16 v[204:207], a[68:71], v[24:27], v[204:207]\n"
    "ds_read_b64_tr_b16 a[224:225], v242 offset:23040\n"
    "v_mfma_f32_16x16x32_bf16 v[208:211], a[72:75], v[24:27], v[208:211]\n"
    "ds_read_b64_tr_b16 a[226:227], v242 offset:25344\n"
    "v_mfma_f32_16x16x32_bf16 v[212:215], a[76:79], v[24:27], v[212:215]\n"
    "ds_read_b64_tr_b16 a[228:229], v242 offset:23072\n"
    "v_mfma_f32_16x16x32_bf16 v[216:219], v[108:111], v[28:31], v[216:219]\n"
    "ds_read_b64_tr_b16 a[230:231], v242 offset:25376\n"
    "buffer_load_dwordx4 v3, s[36:39], s79 offen offset:2048 lds\n"
    "v_mfma_f32_16x16x32_bf16 v[200:203], a[80:83], v[28:31], v[200:203]\n"
    "ds_read_b64_tr_b16 a[232:233], v242 offset:23104\n"
    "buffer_load_dwordx4 v4, s[36:39], s79 offen offset:3072 lds\n"
    "v_mfma_f32_16x16x32_bf16 v[204:207], a[84:87], v[28:31], v[204:207]\n"
    "ds_read_b64_tr_b16 a[234:235], v242 offset:25408\n"
    "s_waitcnt vmcnt(22)\n"
    "v_max3_u32 v247, v32, v33, v34\n"
    "v_max3_u32 v247, v35, v36, v247\n"
    "v_mfma_f32_16x16x32_bf16 v[208:211], a[88:91], v[28:31], v[208:211]\n"
    "ds_read_b64_tr_b16 a[236:237], v242 offset:23136\n"
    "v_max3_u32 v247, v37, v38, v247\n"
    "v_max_u32 v247, v39, v247\n"
    "v_mfma_f32_16x16x32_bf16 v[212:215], a[92:95], v[28:31], v[212:215]\n"
    "ds_read_b64_tr_b16 a[238:239], v242 offset:25440\n"
    "v_cmp_eq_u32 vcc, -1, v247\n"
    "s_cbranch_vccnz LC6_k\n"
    "LD6_k:\n"
    "v_mfma_f32_16x16x32_bf16 v[216:219], v[112:115], v[32:35], v[216:219]\n"
    "ds_read_b64_tr_b16 a[240:241], v242 offset:27648\n"
    "v_mfma_f32_16x16x32_bf16 v[200:203], a[96:99], v[32:35], v[200:203]\n"
    "ds_read_b64_tr_b16 a[242:243], v242 offset:29952\n"
    "v_mfma_f32_16x16x32_bf16 v[204:207], a[100:103], v[32:35], v[204:207]\n"
    "ds_read_b64_tr_b16 a[244:245], v242 offset:27680\n"
    "v_mfma_f32_16x16x32_bf16 v[208:211], a[104:107], v[32:35], v[208:211]\n"
    "ds_read_b64_tr_b16 a[246:247], v242 offset:29984\n"
    "v_mfma_f32_16x16x32_bf16 v[212:215], a[108:111], v[32:35], v[212:215]\n"
    "ds_read_b64_tr_b16 a[248:249], v242 offset:27712\n"
    "v_mfma_f32_16x16x32_bf16 v[216:219], v[116:119], v[36:39], v[216:219]\n"
    "ds_read_b64_tr_b16 a[250:251], v242 offset:30016\n"
    "v_mfma_f32_16x16x32_bf16 v[200:203], a[112:115], v[36:39], v[200:203]\n"
    "ds_read_b64_tr_b16 a[252:253], v242 offset:27744\n"
    "v_mfma_f32_16x16x32_bf16 v[204:207], a[116:119], v[36:39], v[204:207]\n"
    "ds_read_b64_tr_b16 a[254:255], v242 offset:30048\n"
    "s_waitcnt vmcnt(20)\n"
    "v_max3_u32 v247, v40, v41, v42\n"
    "v_max3_u32 v247, v43, v44, v247\n"
    "v_mfma_f32_16x16x32_bf16 v[208:211], a[120:123], v[36:39], v[208:211]\n"
    "ds_read_b64_tr_b16 v[168:169], v242 offset:32256\n"
    "v_max3_u32 v247, v45, v46, v247\n"
    "v_max_u32 v247, v47, v247\n"
    "v_mfma_f32_16x16x32_bf16 v[212:215], a[124:127], v[36:39], v[212:215]\n"
    "ds_read_b64_tr_b16 v[170:171], v242 offset:34560\n"
    "v_cmp_eq_u32 vcc, -1, v247\n"
    "s_cbranch_vccnz LC8_k\n"
    "LD8_k:\n"
    "v_mfma_f32_16x16x32_bf16 v[216:219], v[120:123], v[40:43], v[216:219]\n"
    "ds_read_b64_tr_b16 v[172:173], v242 offset:32288\n"
    "v_mfma_f32_16x16x32_bf16 v[200:203], a[128:131], v[40:43], v[200:203]\n"
    "ds_read_b64_tr_b16 v[174:175], v242 offset:34592\n"
    "v_mfma_f32_16x16x32_bf16 v[204:207], a[132:135], v[40:43], v[204:207]\n"
    "ds_read_b64_tr_b16 v[176:177], v242 offset:32320\n"
    "v_mfma_f32_16x16x32_bf16 v[208:211], a[136:139], v[40:43], v[208:211]\n"
    "ds_read_b64_tr_b16 v[178:179], v242 offset:34624\n"
    "v_mfma_f32_16x16x32_bf16 v[212:215], a[140:143], v[40:43], v[212:215]\n"
    "ds_read_b64_tr_b16 v[180:181], v242 offset:32352\n"
    "v_mfma_f32_16x16x32_bf16 v[216:219], v[124:127], v[44:47], v[216:219]\n"
    "ds_read_b64_tr_b16 v[182:183], v242 offset:34656\n"
    "v_mfma_f32_16x16x32_bf16 v[200:203], a[144:147], v[44:47], v[200:203]\n"
    "ds_read_b64_tr_b16 v[184:185], v242 offset:36864\n"
    "v_mfma_f32_16x16x32_bf16 v[204:207], a[148:151], v[44:47], v[204:207]\n"
    "ds_read_b64_tr_b16 v[186:187], v242 offset:39168\n"
    "s_waitcnt vmcnt(18)\n"
    "v_max3_u32 v247, v48, v49, v50\n"
    "v_max3_u32 v247, v51, v52, v247\n"
    "v_mfma_f32_16x16x32_bf16 v[208:211], a[152:155], v[44:47], v[208:211]\n"
    "ds_read_b64_tr_b16 v[188:189], v242 offset:36896\n"
    "v_max3_u32 v247, v53, v54, v247\n"
    "v_max_u32 v247, v55, v247\n"
    "v_mfma_f32_16x16x32_bf16 v[212:215], a[156:159], v[44:47], v[212:215]\n"
    "ds_read_b64_tr_b16 v[190:191], v242 offset:39200\n"
    "v_cmp_eq_u32 vcc, -1, v247\n"
    "s_cbranch_vccnz LC10_k\n"
    "LD10_k:\n"
    "v_mfma_f32_16x16x32_bf16 v[216:219], v[128:131], v[48:51], v[216:219]\n"
    "ds_read_b64_tr_b16 v[192:193], v242 offset:36928\n"
    "v_mfma_f32_16x16x32_bf16 v[200:203], a[160:163], v[48:51], v[200:203]\n"
    "ds_read_b64_tr_b16 v[194:195], v242 offset:39232\n"
    "v_mfma_f32_16x16x32_bf16 v[204:207], a[164:167], v[48:51], v[204:207]\n"
    "ds_read_b64_tr_b16 v[196:197], v242 offset:36960\n"
    "v_mfma_f32_16x16x32_bf16 v[208:211], a[168:171], v[48:51], v[208:211]\n"
    "ds_read_b64_tr_b16 v[198:199], v242 offset:39264\n"
    "v_mfma_f32_16x16x32_bf16 v[212:215], a[172:175], v[48:51], v[212:215]\n"
    "s_waitcnt lgkmcnt(15)\n"
    "v_mfma_f32_16x16x32_bf16 v[216:219], v[132:135], v[52:55], v[216:219]\n"
    "v_mfma_f32_16x16x32_bf16 v[200:203], a[176:179], v[52:55], v[200:203]\n"
    "v_mfma_f32_16x16x32_bf16 v[204:207], a[180:183], v[52:55], v[204:207]\n"
    "s_waitcnt vmcnt(16)\n"
    "v_max3_u32 v247, v64, v65, v66\n"
    "v_max3_u32 v247, v67, v68, v247\n"
    "v_mfma_f32_16x16x32_bf16 v[208:211], a[184:187], v[52:55], v[208:211]\n"
    "v_max3_u32 v247, v69, v70, v247\n"
    "v_max_u32 v247, v71, v247\n"
    "v_mfma_f32_16x16x32_bf16 v[212:215], a[188:191], v[52:55], v[212:215]\n"
    "v_cmp_eq_u32 vcc, -1, v247\n"
    "s_cbranch_vccnz LC12_k\n"
    "LD12_k:\n"
    "v_mfma_f32_16x16x32_bf16 v[216:219], v[136:139], v[64:67], v[216:219]\n"
    "v_mfma_f32_16x16x32_bf16 v[200:203], a[192:195], v[64:67], v[200:203]\n"
    "v_mfma_f32_16x16x32_bf16 v[204:207], a[196:199], v[64:67], v[204:207]\n"
    "v_mfma_f32_16x16x32_bf16 v[208:211], a[200:203], v[64:67], v[208:211]\n"
    "v_mfma_f32_16x16x32_bf16 v[212:215], a[204:207], v[64:67], v[212:215]\n"
    "s_waitcnt lgkmcnt(0)\n"
    "s_barrier\n"
    "v_mfma_f32_16x16x32_bf16 v[216:219], v[140:143], v[68:71], v[216:219]\n"
    "s_add_u32 m0, s52, 0\n"
    "s_add_u32 s79, s54, 1327104\n"
    "buffer_load_dwordx4 v1, s[36:39], s79 offen lds\n"
    "v_mfma_f32_16x16x32_bf16 v[200:203], a[208:211], v[68:71], v[200:203]\n"
    "v_mfma_f32_16x16x32_bf16 v[204:207], a[212:215], v[68:71], v[204:207]\n"
    "s_waitcnt vmcnt(15)\n"
    "v_max3_u32 v247, v72, v73, v74\n"
    "v_max3_u32 v247, v75, v76, v247\n"
    "v_mfma_f32_16x16x32_bf16 v[208:211], a[216:219], v[68:71], v[208:211]\n"
    "v_max3_u32 v247, v77, v78, v247\n"
    "v_max_u32 v247, v79, v247\n"
    "v_mfma_f32_16x16x32_bf16 v[212:215], a[220:223], v[68:71], v[212:215]\n"
    "v_cmp_eq_u32 vcc, -1, v247\n"
    "s_cbranch_vccnz LC14_k\n"
    "LD14_k:\n"
    "v_mfma_f32_16x16x32_bf16 v[216:219], v[144:147], v[72:75], v[216:219]\n"
    "buffer_load_dwordx4 v2, s[36:39], s79 offen offset:1024 lds\n"
    "v_mfma_f32_16x16x32_bf16 v[200:203], a[224:227], v[72:75], v[200:203]\n"
    "v_mfma_f32_16x16x32_bf16 v[204:207], a[228:231], v[72:75], v[204:207]\n"
    "v_mfma_f32_16x16x32_bf16 v[208:211], a[232:235], v[72:75], v[208:211]\n"
    "v_mfma_f32_16x16x32_bf16 v[212:215], a[236:239], v[72:75], v[212:215]\n"
    "v_mfma_f32_16x16x32_bf16 v[216:219], v[148:151], v[76:79], v[216:219]\n"
    "buffer_load_dwordx4 v3, s[36:39], s79 offen offset:2048 lds\n"
    "v_mfma_f32_16x16x32_bf16 v[200:203], a[240:243], v[76:79], v[200:203]\n"
    "v_mfma_f32_16x16x32_bf16 v[204:207], a[244:247], v[76:79], v[204:207]\n"
    "s_waitcnt vmcnt(15)\n"
    "v_max3_u32 v247, v80, v81, v82\n"
    "v_max3_u32 v247, v83, v84, v247\n"
    "v_mfma_f32_16x16x32_bf16 v[208:211], a[248:251], v[76:79], v[208:211]\n"
    "v_max3_u32 v247, v85, v86, v247\n"
    "v_max_u32 v247, v87, v247\n"
    "v_mfma_f32_16x16x32_bf16 v[212:215], a[252:255], v[76:79], v[212:215]\n"
    "v_cmp_eq_u32 vcc, -1, v247\n"
    "s_cbranch_vccnz LC16_k\n"
    "LD16_k:\n"
    "v_mfma_f32_16x16x32_bf16 v[216:219], v[152:155], v[80:83], v[216:219]\n"
    "buffer_load_dwordx4 v4, s[36:39], s79 offen offset:3072 lds\n"
    "v_mfma_f32_16x16x32_bf16 v[200:203], v[168:171], v[80:83], v[200:203]\n"
    "v_mfma_f32_16x16x32_bf16 v[204:207], v[172:175], v[80:83], v[204:207]\n"
    "v_mfma_f32_16x16x32_bf16 v[208:211], v[176:179], v[80:83], v[208:211]\n"
    "v_mfma_f32_16x16x32_bf16 v[212:215], v[180:183], v[80:83], v[212:215]\n"
    "v_mfma_f32_16x16x32_bf16 v[216:219], v[156:159], v[84:87], v[216:219]\n"
    "s_add_u32 m0, s52, 65536\n"
    "s_add_u32 s79, s54, 1990656\n"
    "buffer_load_dwordx4 v1, s[36:39], s79 offen lds\n"
    "v_mfma_f32_16x16x32_bf16 v[200:203], v[184:187], v[84:87], v[200:203]\n"
    "v_mfma_f32_16x16x32_bf16 v[204:207], v[188:191], v[84:87], v[204:207]\n"
    "v_mfma_f32_16x16x32_bf16 v[208:211], v[192:195], v[84:87], v[208:211]\n"
    "v_mfma_f32_16x16x32_bf16 v[212:215], v[196:199], v[84:87], v[212:215]\n"
    "s_nop 0\n"
    "v_add_f32 v220, v216, v217\n"
    "v_mov_b32 v221, v220\n"
    "s_nop 1\n"
    "v_permlane32_swap_b32 v220, v221\n"
    "v_mov_b32 v220, v221\n"
    "s_nop 1\n"
    "v_permlane16_swap_b32 v220, v221\n"
    "v_add_f32 v220, v220, v248\n"
    "v_pk_add_f32 v[200:201], v[200:201], v[220:221] op_sel_hi:[1,0] neg_lo:[0,1] neg_hi:[0,1]\n"
    "v_pk_add_f32 v[202:203], v[202:203], v[220:221] op_sel_hi:[1,0] neg_lo:[0,1] neg_hi:[0,1]\n"
    "v_cvt_pk_bf16_f32 v224, v200, v201\n"
    "v_cvt_pk_bf16_f32 v225, v202, v203\n"
    "v_min_u32 v224, -2, v224\n"
    "v_min_u32 v225, -2, v225\n"
    "v_pk_add_f32 v[204:205], v[204:205], v[220:221] op_sel_hi:[1,0] neg_lo:[0,1] neg_hi:[0,1]\n"
    "v_pk_add_f32 v[206:207], v[206:207], v[220:221] op_sel_hi:[1,0] neg_lo:[0,1] neg_hi:[0,1]\n"
    "v_cvt_pk_bf16_f32 v226, v204, v205\n"
    "v_cvt_pk_bf16_f32 v227, v206, v207\n"
    "v_min_u32 v226, -2, v226\n"
    "v_min_u32 v227, -2, v227\n"
    "buffer_store_dwordx4 v[224:227], v0, s[44:47], s34 offen offset:0 sc1\n"
    "v_pk_add_f32 v[208:209], v[208:209], v[220:221] op_sel_hi:[1,0] neg_lo:[0,1] neg_hi:[0,1]\n"
    "v_pk_add_f32 v[210:211], v[210:211], v[220:221] op_sel_hi:[1,0] neg_lo:[0,1] neg_hi:[0,1]\n"
    "v_cvt_pk_bf16_f32 v228, v208, v209\n"
    "v_cvt_pk_bf16_f32 v229, v210, v211\n"
    "v_min_u32 v228, -2, v228\n"
    "v_min_u32 v229, -2, v229\n"
    "v_pk_add_f32 v[212:213], v[212:213], v[220:221] op_sel_hi:[1,0] neg_lo:[0,1] neg_hi:[0,1]\n"
    "v_pk_add_f32 v[214:215], v[214:215], v[220:221] op_sel_hi:[1,0] neg_lo:[0,1] neg_hi:[0,1]\n"
    "v_cvt_pk_bf16_f32 v230, v212, v213\n"
    "v_cvt_pk_bf16_f32 v231, v214, v215\n"
    "v_min_u32 v230, -2, v230\n"
    "v_min_u32 v231, -2, v231\n"
    "buffer_store_dwordx4 v[228:231], v0, s[44:47], s34 offen offset:2048 sc1\n"
    "v_pk_add_f32 v[216:217], v[216:217], v[220:221] op_sel_hi:[1,0] neg_lo:[0,1] neg_hi:[0,1]\n"
    "v_pk_add_f32 v[218:219], v[218:219], v[220:221] op_sel_hi:[1,0] neg_lo:[0,1] neg_hi:[0,1]\n"
    "v_cvt_pk_bf16_f32 v232, v216, v217\n"
    "v_cvt_pk_bf16_f32 v233, v218, v219\n"
    "v_min_u32 v232, -2, v232\n"
    "v_min_u32 v233, -2, v233\n"
    "buffer_store_dwordx2 v[232:233], v245, s[44:47], s34 offen sc1\n"
    "v_dot2_f32_bf16 v222, v224, v224, 0\n"
    "v_dot2_f32_bf16 v223, v225, v225, 0\n"
    "v_dot2_f32_bf16 v222, v226, v226, v222\n"
    "v_dot2_f32_bf16 v223, v227, v227, v223\n"
    "v_dot2_f32_bf16 v222, v228, v228, v222\n"
    "v_dot2_f32_bf16 v223, v229, v229, v223\n"
    "v_dot2_f32_bf16 v222, v230, v230, v222\n"
    "v_dot2_f32_bf16 v223, v231, v231, v223\n"
    "s_mov_b32 exec_hi, 0\n"
    "v_dot2_f32_bf16 v222, v232, v232, v222\n"
    "v_dot2_f32_bf16 v223, v233, v233, v223\n"
    "s_mov_b32 exec_hi, -1\n"
    "v_cmp_gt_u32 vcc, 0x100, v0\n"
    "s_nop 1\n"
    "v_add_f32 v222, v222, v223\n"
    "v_mov_b32 v223, v222\n"
    "s_nop 1\n"
    "v_permlane16_swap_b32 v222, v223\n"
    "v_add_f32 v222, v222, v223\n"
    "v_mov_b32 v223, v222\n"
    "s_nop 1\n"
    "v_permlane32_swap_b32 v222, v223\n"
    "v_add_f32 v222, v222, v223\n"
    "v_cndmask_b32 v222, v220, v222, vcc\n"
    "buffer_store_dword v222, v246, s[44:47], 0 offen sc1\n"
    "buffer_load_dwordx4 v2, s[36:39], s79 offen offset:1024 lds\n"
    "buffer_load_dwordx4 v3, s[36:39], s79 offen offset:2048 lds\n"
    "buffer_load_dwordx4 v4, s[36:39], s79 offen offset:3072 lds\n"
    "s_add_u32 m0, s52, 81920\n"
    "s_add_u32 s79, s54, 2654208\n"
    "buffer_load_dwordx4 v1, s[36:39], s79 offen lds\n"
    "buffer_load_dwordx4 v2, s[36:39], s79 offen offset:1024 lds\n"
    "buffer_load_dwordx4 v3, s[36:39], s79 offen offset:2048 lds\n"
    "buffer_load_dwordx4 v4, s[36:39], s79 offen offset:3072 lds\n"
    "s_nop 1\n"
    : "+{v[0:3]}"(vi0), "+{v[4:5]}"(vi1), "+{s[32:35]}"(se), "={s[40:43]}"(srD), "={s[48:51]}"(srO), "={s78}"(sen), "={s[92:93]}"(tg), "={v[60:61]}"(fv), "+{s[88:91]}"(sd), "+{s[52:55]}"(sa0),
      "+{v[16:31]}"(va), "+{v[32:47]}"(vb), "+{v[240:247]}"(vp), "+{v[252:253]}"(vt), "+{v48}"(tag)
    : "{s[44:47]}"(srdS), "{s[94:95]}"(__builtin_amdgcn_kernarg_segment_ptr()), "{s[80:87]}"(sc), "{s[36:39]}"(srdG)
    : "v6", "v7", "v8", "v9", "v10", "v11", "v12", "v13", "v14", "v15", "v49", "v50", "v51", "v52", "v53", "v54", "v55", "v56", "v57", "v58", "v59", "v62", "v63", "v64", "v65", "v66", "v67", "v68", "v69", "v70", "v71", "v72", "v73", "v74", "v75", "v76", "v77", "v78", "v79", "v80", "v81", "v82", "v83", "v84", "v85", "v86", "v87", "v88", "v89", "v90", "v91", "v92", "v93", "v94", "v95", "v96", "v97", "v98", "v99", "v100", "v101", "v102", "v103", "v104", "v105", "v106", "v107", "v108", "v109", "v110", "v111", "v112", "v113", "v114", "v115", "v116", "v117", "v118", "v119", "v120", "v121", "v122", "v123", "v124", "v125", "v126", "v127", "v128", "v129", "v130", "v131", "v132", "v133", "v134", "v135", "v136", "v137", "v138", "v139", "v140", "v141", "v142", "v143", "v144", "v145", "v146", "v147", "v148", "v149", "v150", "v151", "v152", "v153", "v154", "v155", "v156", "v157", "v158", "v159", "v160", "v161", "v162", "v163", "v164", "v165", "v166", "v167", "v168", "v169", "v170", "v171", "v172", "v173", "v174", "v175", "v176", "v177", "v178", "v179", "v180", "v181", "v182", "v183", "v184", "v185", "v186", "v187", "v188", "v189", "v190", "v191", "v192", "v193", "v194", "v195", "v196", "v197", "v198", "v199", "v200", "v201", "v202", "v203", "v204", "v205", "v206", "v207", "v208", "v209", "v210", "v211", "v212", "v213", "v214", "v215", "v216", "v217", "v218", "v219", "v220", "v221", "v222", "v223", "v224", "v225", "v226", "v227", "v228", "v229", "v230", "v231", "v232", "v233", "v234", "v235", "v236", "v237", "v238", "v239", "v248", "v249", "v250", "v251", "v254", "v255", "a0", "a1", "a2", "a3", "a4", "a5", "a6", "a7", "a8", "a9", "a10", "a11", "a12", "a13", "a14", "a15", "a16", "a17", "a18", "a19", "a20", "a21", "a22", "a23", "a24", "a25", "a26", "a27", "a28", "a29", "a30", "a31", "a32", "a33", "a34", "a35", "a36", "a37", "a38", "a39", "a40", "a41", "a42", "a43", "a44", "a45", "a46", "a47", "a48", "a49", "a50", "a51", "a52", "a53", "a54", "a55", "a56", "a57", "a58", "a59", "a60", "a61", "a62", "a63", "a64", "a65", "a66", "a67", "a68", "a69", "a70", "a71", "a72", "a73", "a74", "a75", "a76", "a77", "a78", "a79", "a80", "a81", "a82", "a83", "a84", "a85", "a86", "a87", "a88", "a89", "a90", "a91", "a92", "a93", "a94", "a95", "a96", "a97", "a98", "a99", "a100", "a101", "a102", "a103", "a104", "a105", "a106", "a107", "a108", "a109", "a110", "a111", "a112", "a113", "a114", "a115", "a116", "a117", "a118", "a119", "a120", "a121", "a122", "a123", "a124", "a125", "a126", "a127", "a128", "a129", "a130", "a131", "a132", "a133", "a134", "a135", "a136", "a137", "a138", "a139", "a140", "a141", "a142", "a143", "a144", "a145", "a146", "a147", "a148", "a149", "a150", "a151", "a152", "a153", "a154", "a155", "a156", "a157", "a158", "a159", "a160", "a161", "a162", "a163", "a164", "a165", "a166", "a167", "a168", "a169", "a170", "a171", "a172", "a173", "a174", "a175", "a176", "a177", "a178", "a179", "a180", "a181", "a182", "a183", "a184", "a185", "a186", "a187", "a188", "a189", "a190", "a191", "a192", "a193", "a194", "a195", "a196", "a197", "a198", "a199", "a200", "a201", "a202", "a203", "a204", "a205", "a206", "a207", "a208", "a209", "a210", "a211", "a212", "a213", "a214", "a215", "a216", "a217", "a218", "a219", "a220", "a221", "a222", "a223", "a224", "a225", "a226", "a227", "a228", "a229", "a230", "a231", "a232", "a233", "a234", "a235", "a236", "a237", "a238", "a239", "a240", "a241", "a242", "a243", "a244", "a245", "a246", "a247", "a248", "a249", "a250", "a251", "a252", "a253", "a254", "a255", "s73", "s76", "s77", "s79", "vcc", "scc", "memory");
  {
  const u32 bid = RFL(se[0]), m0a = RFL(sd[0]), w = m0a >> 10, vL = vi0[0], l = vL >> 4, q = l >> 4, n = l & 15, xl = bid & 7, idx = bid >> 3;
  const u32 mt = (xl & 1) * 4 + (idx & 3), kq = xl >> 1, f7 = idx >> 2, fgrp = (u32)cfg::WS_FLAGS + mt * 4 * cfg::FST, box = (u32)cfg::WS_SLAB + bid * BOX;
  const u32 fj = kq * 8 + f7, w0 = ((w + 3) >> 2) << 31, mt4 = mt * 2 + (f7 & 1), dj = f7 >> 1, t4 = mt4 * 4 + w, t3 = mt * 8 + 2 * w;
  const u32 fp2 = RFL(se[3]);
  u32 f3a = (u32)cfg::WS_FL2 + (mt * 4 + dj) * 16384 + (kq * 2 + (f7 & 1)) * 16 + w * 4 + l * 512 | OOB(l < 8); PIN(f3a);
  const u32 st = ((l & 7) >> 1) ^ (q & 3), sw = (2 * q + (n >> 3)) & 3, rr = (4 * q + (n >> 2)), i6 = rr * 128 + sw * 32 + (n & 3) * 8;
  u32x16 vi = {vL, vi0[1], vi0[2], vi0[3], vi1[0], vi1[1], i6, i6 ^ 32, i6 ^ 64, i6 ^ 96, 147456 + rr * 32 + (n & 3) * 8, fgrp + kq * cfg::FST + (l & 7) * 4 + w0, fgrp + kq * cfg::FST + f7 * 4 | OOB(l + w == 0),
               (u32)cfg::WS_STATS + (mt * 4 + w) * 2048 + q * 512 + n * 16, (l >> 3) * 1152 + (st ^ (2 * (kq & 1))) * 32 + (l & 1) * 16, (l >> 1) * 1152 + (l & 1) * 16};
  const u32 tl = ((xl & 1) * 32 + idx) * 164 + w * 41 + l; u32 b0 = kq * 1179648, b1 = 3538944 + kq * 147456; PINS(b0); PINS(b1);
  u32x4 vr = {hpos(dj * 32 + kq * 8 + q, n), hpos(dj * 32 + kq * 8 + 4 + q, n), hpos(128 + dj * 4 + q, n), (tl < 9216 ? b0 : b1) + tl * 128 | OOB(l < 41 && tl < 10368)};

  asm volatile(
    "s_nop 1\n"
    "s_waitcnt vmcnt(7)\n"
    "buffer_store_dword v61, v244, s[44:47], 0 offen offset:0 sc1\n"
    "s_nop 1\n"
    "v_mov_b32 v61, s92\n"
    "v_mov_b32 v16, -1\n"
    "v_mov_b32 v17, -1\n"
    "v_mov_b32 v18, -1\n"
    "v_mov_b32 v19, -1\n"
    "s_mul_i32 s76, s32, 29696\n"
    "s_add_u32 s76, s76, 23855104\n"
    "s_add_u32 s77, s76, s88\n"
    "s_add_u32 s76, s77, s88\n"
    "s_add_u32 s77, s77, 24576\n"
    "buffer_store_dwordx4 v[16:19], v0, s[44:47], s76 offen offset:0 sc1\n"
    "buffer_store_dwordx4 v[16:19], v0, s[44:47], s76 offen offset:1024 sc1\n"
    "s_nop 1\n"
    : "+{v[0:15]}"(vi), "+{v[56:59]}"(vr), "+{v[60:61]}"(fv), "+{v[240:247]}"(vp), "+{v[252:253]}"(vt)
    : "{s[44:47]}"(srdS), "{s[92:93]}"(tg), "{s32}"(bid), "{s88}"(m0a)
    : "s76", "s77", "v16", "v17", "v18", "v19", "v254", "scc", "memory");
  SB();
  const u32 act3 = (u32)cfg::WS_ACT + t3 * 294912 + kq * 73728, cb = dj * 256 + ((w & 1) ^ (kq >> 1)) * 128, cs = 1024 + dj * 32, pbox = (u32)cfg::WS_SLAB + (idx * 8 + (xl & 1)) * BOX;
  const u32x16 sa = {0, 0, 0, 0, (u32)cfg::WS_HFRAG + t3 * 36864, act3 + f7 * 2048, act3 + 65536 + (f7 >> 1) * 2048 + (f7 & 1) * 512,
              (kq * 1024 + (w >> 1) * (128 / 2)) * 1152 + cb, (4096 + kq * 128 + (w >> 1) * 32) * 1152 + cb, (kq * 1024 + w * (128 / 2)) * 1152 + cs, (4096 + kq * 128 + w * 32) * 1152 + cs, (u32)cfg::WS_ACT + t4 * 294912 + kq * 73728, box + w * 2048,
              pbox + (kq ^ 1) * (2 * BOX) + w * 2048, pbox + (kq ^ 2) * (2 * BOX) + 8192 + w * 2048, pbox + (kq ^ 3) * (2 * BOX) + 16384 + w * 2048};
  const u32x8 sb = {pbox + w * (2 * BOX) + 24576 + (((w ^ kq) - 1) & 3) * 1024, box + 24576, (u32)cfg::WS_HFRAG + t4 * 36864, idx >> 2,
             (u32)cfg::WS_MEAN + (t4 >> 1) * 256 + (t4 & 1) * 8, (w >> 1) * (256 * 9216), t4 * 36864 + dj * 256 + kq * 64, w == kq ? t4 * 36864 + cs : 0x80000000u};
  vp[5] = f3a;

  asm volatile(
    "s_nop 1\n"
    "v_mov_b32 v32, -1\n"
    "v_mov_b32 v33, -1\n"
    "v_mov_b32 v34, -1\n"
    "v_mov_b32 v35, -1\n"
    "s_barrier\n"
    "ds_read_b64_tr_b16 v[16:17], v6 offset:16384\n"
    "ds_read_b64_tr_b16 v[18:19], v6 offset:18432\n"
    "ds_read_b64_tr_b16 v[20:21], v7 offset:16384\n"
    "ds_read_b64_tr_b16 v[22:23], v7 offset:18432\n"
    "ds_read_b64_tr_b16 v[24:25], v8 offset:16384\n"
    "ds_read_b64_tr_b16 v[26:27], v8 offset:18432\n"
    "s_add_u32 s33, s56, 32768\n"
    "buffer_load_dwordx4 v[64:67], v0, s[44:47], s33 offen offset:0 sc1\n"
    "buffer_load_dwordx4 v[68:71], v0, s[44:47], s33 offen offset:1024 sc1\n"
    "s_add_u32 s34, s56, 69632\n"
    "buffer_load_dwordx4 v[72:75], v0, s[44:47], s34 offen offset:0 sc1\n"
    "buffer_load_dwordx4 v[76:79], v0, s[44:47], s34 offen offset:1024 sc1\n"
    "buffer_load_dwordx4 v[80:83], v0, s[44:47], s33 offen offset:2048 sc1\n"
    "buffer_load_dwordx4 v[84:87], v0, s[44:47], s33 offen offset:3072 sc1\n"
    "buffer_load_dwordx4 v[88:91], v0, s[44:47], s34 offen offset:2048 sc1\n"
    "buffer_load_dwordx4 v[92:95], v0, s[44:47], s34 offen offset:3072 sc1\n"
    "s_add_u32 s76, s64, 8192\n"
    "buffer_store_dwordx4 v[32:35], v0, s[44:47], s76 offen offset:0 sc1\n"
    "buffer_store_dwordx4 v[32:35], v0, s[44:47], s76 offen offset:1024 sc1\n"
    "s_add_u32 s76, s64, 16384\n"
    "buffer_store_dwordx4 v[32:35], v0, s[44:47], s76 offen offset:0 sc1\n"
    "s_add_u32 m0, s52, 98304\n"
    "s_add_u32 s79, s54, 3317760\n"
    "buffer_load_dwordx4 v1, s[36:39], s79 offen lds\n"
    "buffer_load_dwordx4 v2, s[36:39], s79 offen offset:1024 lds\n"
    "buffer_load_dwordx4 v3, s[36:39], s79 offen offset:2048 lds\n"
    "buffer_load_dwordx4 v4, s[36:39], s79 offen offset:3072 lds\n"
    "s_waitcnt vmcnt(11)\n"
    "v_max3_u32 v247, v64, v65, v66\n"
    "v_max3_u32 v247, v67, v68, v247\n"
    "v_max3_u32 v247, v69, v70, v247\n"
    "v_max_u32 v247, v71, v247\n"
    "v_max3_u32 v247, v72, v73, v247\n"
    "v_max3_u32 v247, v74, v79, v247\n"
    "v_max3_u32 v247, v75, v76, v247\n"
    "v_max3_u32 v247, v77, v78, v247\n"
    "s_waitcnt vmcnt(7)\n"
    "v_max3_u32 v247, v80, v81, v247\n"
    "v_max3_u32 v247, v82, v87, v247\n"
    "v_max3_u32 v247, v83, v84, v247\n"
    "v_max3_u32 v247, v85, v86, v247\n"
    "v_max3_u32 v247, v88, v89, v247\n"
    "v_max3_u32 v247, v90, v95, v247\n"
    "v_max3_u32 v247, v91, v92, v247\n"
    "v_max3_u32 v247, v93, v94, v247\n"
    "v_cmp_eq_u32 vcc, -1, v247\n"
    "s_cbranch_vccnz LH_k\n"
    "LI_k:\n"
    "s_waitcnt lgkmcnt(4)\n"
    "v_mfma_f32_16x16x32_bf16 v[96:99], v[16:19], v[64:67], 0\n"
    "ds_read_b64_tr_b16 v[28:29], v9 offset:16384\n"
    "s_add_u32 s33, s56, 0\n"
    "v_mfma_f32_16x16x32_bf16 v[100:103], v[16:19], v[68:71], 0\n"
    "ds_read_b64_tr_b16 v[30:31], v9 offset:18432\n"
    "v_mfma_f32_16x16x32_bf16 v[104:107], v[16:19], v[72:75], 0\n"
    "buffer_load_dwordx4 a[0:3], v0, s[44:47], s33 offen offset:0 sc1\n"
    "v_mfma_f32_16x16x32_bf16 v[108:111], v[16:19], v[76:79], 0\n"
    "buffer_load_dwordx4 a[4:7], v0, s[44:47], s33 offen offset:1024 sc1\n"
    "s_waitcnt lgkmcnt(4)\n"
    "v_mfma_f32_16x16x32_bf16 v[128:131], v[20:23], v[64:67], 0\n"
    "ds_read_b64_tr_b16 v[16:17], v6 offset:20480\n"
    "s_add_u32 s34, s56, 36864\n"
    "v_mfma_f32_16x16x32_bf16 v[132:135], v[20:23], v[68:71], 0\n"
    "ds_read_b64_tr_b16 v[18:19], v6 offset:22528\n"
    "v_mfma_f32_16x16x32_bf16 v[136:139], v[20:23], v[72:75], 0\n"
    "buffer_load_dwordx4 a[8:11], v0, s[44:47], s34 offen offset:0 sc1\n"
    "v_mfma_f32_16x16x32_bf16 v[140:143], v[20:23], v[76:79], 0\n"
    "buffer_load_dwordx4 a[12:15], v0, s[44:47], s34 offen offset:1024 sc1\n"
    "s_waitcnt lgkmcnt(2)\n"
    "v_mfma_f32_16x16x32_bf16 v[160:163], v[24:27], v[64:67], 0\n"
    "ds_read_b64_tr_b16 v[20:21], v7 offset:20480\n"
    "buffer_load_dword v60, v243, s[44:47], 0 offen sc1\n"
    "v_mfma_f32_16x16x32_bf16 v[164:167], v[24:27], v[68:71], 0\n"
    "ds_read_b64_tr_b16 v[22:23], v7 offset:22528\n"
    "v_mfma_f32_16x16x32_bf16 v[168:171], v[24:27], v[72:75], 0\n"
    "buffer_load_dwordx4 a[16:19], v0, s[44:47], s33 offen offset:2048 sc1\n"
    "v_mfma_f32_16x16x32_bf16 v[172:175], v[24:27], v[76:79], 0\n"
    "buffer_load_dwordx4 a[20:23], v0, s[44:47], s33 offen offset:3072 sc1\n"
    "v_mfma_f32_16x16x32_bf16 v[192:195], v[28:31], v[64:67], 0\n"
    "ds_read_b64_tr_b16 v[24:25], v8 offset:20480\n"
    "buffer_load_dwordx4 a[24:27], v0, s[44:47], s34 offen offset:2048 sc1\n"
    "v_mfma_f32_16x16x32_bf16 v[196:199], v[28:31], v[68:71], 0\n"
    "ds_read_b64_tr_b16 v[26:27], v8 offset:22528\n"
    "v_mfma_f32_16x16x32_bf16 v[200:203], v[28:31], v[72:75], 0\n"
    "buffer_load_dwordx4 a[28:31], v0, s[44:47], s34 offen offset:3072 sc1\n"
    "v_mfma_f32_16x16x32_bf16 v[204:207], v[28:31], v[76:79], 0\n"
    "s_add_u32 m0, s52, 114688\n"
    "s_waitcnt lgkmcnt(2)\n"
    "v_mfma_f32_16x16x32_bf16 v[112:115], v[16:19], v[64:67], 0\n"
    "ds_read_b64_tr_b16 v[28:29], v9 offset:20480\n"
    "s_add_u32 s79, s54, 3981312\n"
    "v_mfma_f32_16x16x32_bf16 v[116:119], v[16:19], v[68:71], 0\n"
    "ds_read_b64_tr_b16 v[30:31], v9 offset:22528\n"
    "v_mfma_f32_16x16x32_bf16 v[120:123], v[16:19], v[72:75], 0\n"
    "buffer_load_dwordx4 v1, s[36:39], s79 offen lds\n"
    "v_mfma_f32_16x16x32_bf16 v[124:127], v[16:19], v[76:79], 0\n"
    "buffer_load_dwordx4 v2, s[36:39], s79 offen offset:1024 lds\n"
    "v_mfma_f32_16x16x32_bf16 v[144:147], v[20:23], v[64:67], 0\n"
    "ds_read_b64_tr_b16 v[16:17], v6 offset:24576\n"
    "buffer_load_dwordx4 v3, s[36:39], s79 offen offset:2048 lds\n"
    "v_mfma_f32_16x16x32_bf16 v[148:151], v[20:23], v[68:71], 0\n"
    "ds_read_b64_tr_b16 v[18:19], v6 offset:26624\n"
    "v_mfma_f32_16x16x32_bf16 v[152:155], v[20:23], v[72:75], 0\n"
    "buffer_load_dwordx4 v4, s[36:39], s79 offen offset:3072 lds\n"
    "v_mfma_f32_16x16x32_bf16 v[156:159], v[20:23], v[76:79], 0\n"
    "s_add_u32 m0, s52, 131072\n"
    "s_waitcnt lgkmcnt(2)\n"
    "v_mfma_f32_16x16x32_bf16 v[176:179], v[24:27], v[64:67], 0\n"
    "ds_read_b64_tr_b16 v[20:21], v7 offset:24576\n"
    "s_add_u32 s79, s54, 4644864\n"
    "v_mfma_f32_16x16x32_bf16 v[180:183], v[24:27], v[68:71], 0\n"
    "ds_read_b64_tr_b16 v[22:23], v7 offset:26624\n"
    "v_mfma_f32_16x16x32_bf16 v[184:187], v[24:27], v[72:75], 0\n"
    "buffer_load_dwordx4 v1, s[36:39], s79 offen lds\n"
    "v_mfma_f32_16x16x32_bf16 v[188:191], v[24:27], v[76:79], 0\n"
    "buffer_load_dwordx4 v2, s[36:39], s79 offen offset:1024 lds\n"
    "v_mfma_f32_16x16x32_bf16 v[208:211], v[28:31], v[64:67], 0\n"
    "ds_read_b64_tr_b16 v[24:25], v8 offset:24576\n"
    "buffer_load_dwordx4 v3, s[36:39], s79 offen offset:2048 lds\n"
    "v_mfma_f32_16x16x32_bf16 v[212:215], v[28:31], v[68:71], 0\n"
    "ds_read_b64_tr_b16 v[26:27], v8 offset:26624\n"
    "v_mfma_f32_16x16x32_bf16 v[216:219], v[28:31], v[72:75], 0\n"
    "buffer_load_dwordx4 v4, s[36:39], s79 offen offset:3072 lds\n"
    "v_mfma_f32_16x16x32_bf16 v[220:223], v[28:31], v[76:79], 0\n"
    "s_waitcnt lgkmcnt(2)\n"
    "v_mfma_f32_16x16x32_bf16 v[96:99], v[16:19], v[80:83], v[96:99]\n"
    "ds_read_b64_tr_b16 v[28:29], v9 offset:24576\n"
    "v_mfma_f32_16x16x32_bf16 v[100:103], v[16:19], v[84:87], v[100:103]\n"
    "ds_read_b64_tr_b16 v[30:31], v9 offset:26624\n"
    "v_mfma_f32_16x16x32_bf16 v[104:107], v[16:19], v[88:91], v[104:107]\n"
    "v_mfma_f32_16x16x32_bf16 v[108:111], v[16:19], v[92:95], v[108:111]\n"
    "v_mfma_f32_16x16x32_bf16 v[128:131], v[20:23], v[80:83], v[128:131]\n"
    "ds_read_b64_tr_b16 v[16:17], v6 offset:28672\n"
    "v_mfma_f32_16x16x32_bf16 v[132:135], v[20:23], v[84:87], v[132:135]\n"
    "ds_read_b64_tr_b16 v[18:19], v6 offset:30720\n"
    "v_mfma_f32_16x16x32_bf16 v[136:139], v[20:23], v[88:91], v[136:139]\n"
    "v_mfma_f32_16x16x32_bf16 v[140:143], v[20:23], v[92:95], v[140:143]\n"
    "s_waitcnt vmcnt(12)\n"
    "v_cmp_ne_u32 vcc, s92, v60\n"
    "s_cbranch_vccnz LS6_k\n"
    "LB6_k:\n"
    "s_waitcnt lgkmcnt(2)\n"
    "v_mfma_f32_16x16x32_bf16 v[160:163], v[24:27], v[80:83], v[160:163]\n"
    "ds_read_b64_tr_b16 v[20:21], v7 offset:28672\n"
    "s_add_u32 s33, s56, 4096\n"
    "v_mfma_f32_16x16x32_bf16 v[164:167], v[24:27], v[84:87], v[164:167]\n"
    "ds_read_b64_tr_b16 v[22:23], v7 offset:30720\n"
    "v_mfma_f32_16x16x32_bf16 v[168:171], v[24:27], v[88:91], v[168:171]\n"
    "buffer_load_dwordx4 a[32:35], v0, s[44:47], s33 offen offset:0 sc1\n"
    "v_mfma_f32_16x16x32_bf16 v[172:175], v[24:27], v[92:95], v[172:175]\n"
    "buffer_load_dwordx4 a[36:39], v0, s[44:47], s33 offen offset:1024 sc1\n"
    "v_mfma_f32_16x16x32_bf16 v[192:195], v[28:31], v[80:83], v[192:195]\n"
    "ds_read_b64_tr_b16 v[24:25], v8 offset:28672\n"
    "s_add_u32 s34, s56, 40960\n"
    "v_mfma_f32_16x16x32_bf16 v[196:199], v[28:31], v[84:87], v[196:199]\n"
    "ds_read_b64_tr_b16 v[26:27], v8 offset:30720\n"
    "v_accvgpr_read_b32 v32, a0\n"
    "v_accvgpr_read_b32 v33, a1\n"
    "v_accvgpr_read_b32 v34, a2\n"
    "v_mfma_f32_16x16x32_bf16 v[200:203], v[28:31], v[88:91], v[200:203]\n"
    "buffer_load_dwordx4 a[40:43], v0, s[44:47], s34 offen offset:0 sc1\n"
    "v_max3_u32 v32, v32, v33, v34\n"
    "v_accvgpr_read_b32 v35, a3\n"
    "v_mfma_f32_16x16x32_bf16 v[204:207], v[28:31], v[92:95], v[204:207]\n"
    "buffer_load_dwordx4 a[44:47], v0, s[44:47], s34 offen offset:1024 sc1\n"
    "v_accvgpr_read_b32 v36, a4\n"
    "s_waitcnt lgkmcnt(2)\n"
    "v_mfma_f32_16x16x32_bf16 v[112:115], v[16:19], v[80:83], v[112:115]\n"
    "ds_read_b64_tr_b16 v[28:29], v9 offset:28672\n"
    "buffer_load_dwordx4 a[48:51], v0, s[44:47], s33 offen offset:2048 sc1\n"
    "v_accvgpr_read_b32 v37, a5\n"
    "v_mfma_f32_16x16x32_bf16 v[116:119], v[16:19], v[84:87], v[116:119]\n"
    "ds_read_b64_tr_b16 v[30:31], v9 offset:30720\n"
    "v_max3_u32 v35, v35, v36, v37\n"
    "v_accvgpr_read_b32 v38, a6\n"
    "v_accvgpr_read_b32 v39, a7\n"
    "v_mfma_f32_16x16x32_bf16 v[120:123], v[16:19], v[88:91], v[120:123]\n"
    "buffer_load_dwordx4 a[52:55], v0, s[44:47], s33 offen offset:3072 sc1\n"
    "v_accvgpr_read_b32 v40, a8\n"
    "v_accvgpr_read_b32 v41, a9\n"
    "v_mfma_f32_16x16x32_bf16 v[124:127], v[16:19], v[92:95], v[124:127]\n"
    "v_max3_u32 v38, v38, v39, v40\n"
    "v_accvgpr_read_b32 v42, a10\n"
    "s_barrier\n"
    "v_mfma_f32_16x16x32_bf16 v[144:147], v[20:23], v[80:83], v[144:147]\n"
    "ds_read_b64_tr_b16 v[16:17], v6 offset:32768\n"
    "buffer_load_dwordx4 a[56:59], v0, s[44:47], s34 offen offset:2048 sc1\n"
    "v_accvgpr_read_b32 v43, a11\n"
    "v_mfma_f32_16x16x32_bf16 v[148:151], v[20:23], v[84:87], v[148:151]\n"
    "ds_read_b64_tr_b16 v[18:19], v6 offset:34816\n"
    "v_max3_u32 v41, v41, v42, v43\n"
    "v_max3_u32 v32, v32, v35, v38\n"
    "v_accvgpr_read_b32 v44, a12\n"
    "v_mfma_f32_16x16x32_bf16 v[152:155], v[20:23], v[88:91], v[152:155]\n"
    "buffer_load_dwordx4 a[60:63], v0, s[44:47], s34 offen offset:3072 sc1\n"
    "v_accvgpr_read_b32 v45, a13\n"
    "v_mfma_f32_16x16x32_bf16 v[156:159], v[20:23], v[92:95], v[156:159]\n"
    "s_add_u32 s33, s56, 8192\n"
    "v_accvgpr_read_b32 v46, a14\n"
    "s_waitcnt lgkmcnt(2)\n"
    "v_mfma_f32_16x16x32_bf16 v[176:179], v[24:27], v[80:83], v[176:179]\n"
    "ds_read_b64_tr_b16 v[20:21], v7 offset:32768\n"
    "buffer_load_dwordx4 a[64:67], v0, s[44:47], s33 offen offset:0 sc1\n"
    "v_mfma_f32_16x16x32_bf16 v[180:183], v[24:27], v[84:87], v[180:183]\n"
    "ds_read_b64_tr_b16 v[22:23], v7 offset:34816\n"
    "v_accvgpr_read_b32 v47, a15\n"
    "v_max3_u32 v44, v44, v45, v46\n"
    "v_mfma_f32_16x16x32_bf16 v[184:187], v[24:27], v[88:91], v[184:187]\n"
    "buffer_load_dwordx4 a[68:71], v0, s[44:47], s33 offen offset:1024 sc1\n"
    "v_max3_u32 v41, v41, v44, v47\n"
    "v_mfma_f32_16x16x32_bf16 v[188:191], v[24:27], v[92:95], v[188:191]\n"
    "s_add_u32 s34, s56, 45056\n"
    "buffer_load_dwordx4 a[72:75], v0, s[44:47], s34 offen offset:0 sc1\n"
    "v_mfma_f32_16x16x32_bf16 v[208:211], v[28:31], v[80:83], v[208:211]\n"
    "ds_read_b64_tr_b16 v[24:25], v8 offset:32768\n"
    "buffer_load_dwordx4 a[76:79], v0, s[44:47], s34 offen offset:1024 sc1\n"
    "v_mfma_f32_16x16x32_bf16 v[212:215], v[28:31], v[84:87], v[212:215]\n"
    "ds_read_b64_tr_b16 v[26:27], v8 offset:34816\n"
    "v_max_u32 v32, v32, v41\n"
    "v_mfma_f32_16x16x32_bf16 v[216:219], v[28:31], v[88:91], v[216:219]\n"
    "buffer_load_dwordx4 a[80:83], v0, s[44:47], s33 offen offset:2048 sc1\n"
    "v_mfma_f32_16x16x32_bf16 v[220:223], v[28:31], v[92:95], v[220:223]\n"
    "v_cmp_eq_u32 vcc, -1, v32\n"
    "s_cbranch_vccnz LV0_k\n"
    "LW0_k:\n"
    "s_waitcnt lgkmcnt(2)\n"
    "v_mfma_f32_16x16x32_bf16 v[96:99], v[16:19], a[0:3], v[96:99]\n"
    "ds_read_b64_tr_b16 v[28:29], v9 offset:32768\n"
    "v_mfma_f32_16x16x32_bf16 v[100:103], v[16:19], a[4:7], v[100:103]\n"
    "ds_read_b64_tr_b16 v[30:31], v9 offset:34816\n"
    "v_mfma_f32_16x16x32_bf16 v[104:107], v[16:19], a[8:11], v[104:107]\n"
    "v_mfma_f32_16x16x32_bf16 v[108:111], v[16:19], a[12:15], v[108:111]\n"
    "buffer_load_dwordx4 a[84:87], v0, s[44:47], s33 offen offset:3072 sc1\n"
    "v_mfma_f32_16x16x32_bf16 v[128:131], v[20:23], a[0:3], v[128:131]\n"
    "ds_read_b64_tr_b16 v[16:17], v6 offset:36864\n"
    "v_mfma_f32_16x16x32_bf16 v[132:135], v[20:23], a[4:7], v[132:135]\n"
    "ds_read_b64_tr_b16 v[18:19], v6 offset:38912\n"
    "v_mfma_f32_16x16x32_bf16 v[136:139], v[20:23], a[8:11], v[136:139]\n"
    "v_mfma_f32_16x16x32_bf16 v[140:143], v[20:23], a[12:15], v[140:143]\n"
    "s_waitcnt lgkmcnt(2)\n"
    "v_mfma_f32_16x16x32_bf16 v[160:163], v[24:27], a[0:3], v[160:163]\n"
    "ds_read_b64_tr_b16 v[20:21], v7 offset:36864\n"
    "v_mfma_f32_16x16x32_bf16 v[164:167], v[24:27], a[4:7], v[164:167]\n"
    "ds_read_b64_tr_b16 v[22:23], v7 offset:38912\n"
    "v_mfma_f32_16x16x32_bf16 v[168:171], v[24:27], a[8:11], v[168:171]\n"
    "buffer_load_dwordx4 a[88:91], v0, s[44:47], s34 offen offset:2048 sc1\n"
    "v_mfma_f32_16x16x32_bf16 v[172:175], v[24:27], a[12:15], v[172:175]\n"
    "v_mfma_f32_16x16x32_bf16 v[192:195], v[28:31], a[0:3], v[192:195]\n"
    "ds_read_b64_tr_b16 v[24:25], v8 offset:36864\n"
    "s_waitcnt vmcnt(23)\n"
    "v_mfma_f32_16x16x32_bf16 v[196:199], v[28:31], a[4:7], v[196:199]\n"
    "ds_read_b64_tr_b16 v[26:27], v8 offset:38912\n"
    "v_accvgpr_read_b32 v32, a16\n"
    "v_accvgpr_read_b32 v33, a17\n"
    "v_mfma_f32_16x16x32_bf16 v[200:203], v[28:31], a[8:11], v[200:203]\n"
    "buffer_load_dwordx4 a[92:95], v0, s[44:47], s34 offen offset:3072 sc1\n"
    "v_accvgpr_read_b32 v34, a18\n"
    "v_mfma_f32_16x16x32_bf16 v[204:207], v[28:31], a[12:15], v[204:207]\n"
    "v_max3_u32 v32, v32, v33, v34\n"
    "v_accvgpr_read_b32 v35, a19\n"
    "s_waitcnt lgkmcnt(2)\n"
    "v_mfma_f32_16x16x32_bf16 v[112:115], v[16:19], a[0:3], v[112:115]\n"
    "ds_read_b64_tr_b16 v[28:29], v9 offset:36864\n"
    "v_accvgpr_read_b32 v36, a20\n"
    "v_accvgpr_read_b32 v37, a21\n"
    "v_mfma_f32_16x16x32_bf16 v[116:119], v[16:19], a[4:7], v[116:119]\n"
    "ds_read_b64_tr_b16 v[30:31], v9 offset:38912\n"
    "s_add_u32 s33, s56, 12288\n"
    "v_max3_u32 v35, v35, v36, v37\n"
    "v_mfma_f32_16x16x32_bf16 v[120:123], v[16:19], a[8:11], v[120:123]\n"
    "buffer_load_dwordx4 a[96:99], v0, s[44:47], s33 offen offset:0 sc1\n"
    "v_accvgpr_read_b32 v38, a22\n"
    "v_mfma_f32_16x16x32_bf16 v[124:127], v[16:19], a[12:15], v[124:127]\n"
    "v_accvgpr_read_b32 v39, a23\n"
    "v_accvgpr_read_b32 v40, a24\n"
    "v_accvgpr_read_b32 v41, a25\n"
    "v_mfma_f32_16x16x32_bf16 v[144:147], v[20:23], a[0:3], v[144:147]\n"
    "ds_read_b64_tr_b16 v[16:17], v6 offset:40960\n"
    "v_max3_u32 v38, v38, v39, v40\n"
    "v_accvgpr_read_b32 v42, a26\n"
    "v_mfma_f32_16x16x32_bf16 v[148:151], v[20:23], a[4:7], v[148:151]\n"
    "ds_read_b64_tr_b16 v[18:19], v6 offset:43008\n"
    "v_accvgpr_read_b32 v43, a27\n"
    "v_max3_u32 v32, v32, v35, v38\n"
    "v_mfma_f32_16x16x32_bf16 v[152:155], v[20:23], a[8:11], v[152:155]\n"
    "buffer_load_dwordx4 a[100:103], v0, s[44:47], s33 offen offset:1024 sc1\n"
    "v_max3_u32 v41, v41, v42, v43\n"
    "v_mfma_f32_16x16x32_bf16 v[156:159], v[20:23], a[12:15], v[156:159]\n"
    "v_accvgpr_read_b32 v44, a28\n"
    "v_accvgpr_read_b32 v45, a29\n"
    "s_waitcnt lgkmcnt(2)\n"
    "v_mfma_f32_16x16x32_bf16 v[176:179], v[24:27], a[0:3], v[176:179]\n"
    "ds_read_b64_tr_b16 v[20:21], v7 offset:40960\n"
    "v_accvgpr_read_b32 v46, a30\n"
    "v_mfma_f32_16x16x32_bf16 v[180:183], v[24:27], a[4:7], v[180:183]\n"
    "ds_read_b64_tr_b16 v[22:23], v7 offset:43008\n"
    "v_accvgpr_read_b32 v47, a31\n"
    "v_mfma_f32_16x16x32_bf16 v[184:187], v[24:27], a[8:11], v[184:187]\n"
    "v_max3_u32 v44, v44, v45, v46\n"
    "v_mfma_f32_16x16x32_bf16 v[188:191], v[24:27], a[12:15], v[188:191]\n"
    "s_add_u32 s34, s56, 49152\n"
    "buffer_load_dwordx4 a[104:107], v0, s[44:47], s34 offen offset:0 sc1\n"
    "v_mfma_f32_16x16x32_bf16 v[208:211], v[28:31], a[0:3], v[208:211]\n"
    "ds_read_b64_tr_b16 v[24:25], v8 offset:40960\n"
    "v_max3_u32 v41, v41, v44, v47\n"
    "v_mfma_f32_16x16x32_bf16 v[212:215], v[28:31], a[4:7], v[212:215]\n"
    "ds_read_b64_tr_b16 v[26:27], v8 offset:43008\n"
    "v_max_u32 v32, v32, v41\n"
    "v_mfma_f32_16x16x32_bf16 v[216:219], v[28:31], a[8:11], v[216:219]\n"
    "v_cmp_eq_u32 vcc, -1, v32\n"
    "s_cbranch_vccnz LV1_k\n"
    "LW1_k:\n"
    "v_mfma_f32_16x16x32_bf16 v[220:223], v[28:31], a[12:15], v[220:223]\n"
    "s_waitcnt lgkmcnt(2)\n"
    "v_mfma_f32_16x16x32_bf16 v[96:99], v[16:19], a[16:19], v[96:99]\n"
    "ds_read_b64_tr_b16 v[28:29], v9 offset:40960\n"
    "v_mfma_f32_16x16x32_bf16 v[100:103], v[16:19], a[20:23], v[100:103]\n"
    "ds_read_b64_tr_b16 v[30:31], v9 offset:43008\n"
    "v_mfma_f32_16x16x32_bf16 v[104:107], v[16:19], a[24:27], v[104:107]\n"
    "buffer_load_dwordx4 a[108:111], v0, s[44:47], s34 offen offset:1024 sc1\n"
    "v_mfma_f32_16x16x32_bf16 v[108:111], v[16:19], a[28:31], v[108:111]\n"
    "v_mfma_f32_16x16x32_bf16 v[128:131], v[20:23], a[16:19], v[128:131]\n"
    "ds_read_b64_tr_b16 v[16:17], v6 offset:45056\n"
    "v_mfma_f32_16x16x32_bf16 v[132:135], v[20:23], a[20:23], v[132:135]\n"
    "ds_read_b64_tr_b16 v[18:19], v6 offset:47104\n"
    "v_mfma_f32_16x16x32_bf16 v[136:139], v[20:23], a[24:27], v[136:139]\n"
    "buffer_load_dwordx4 a[112:115], v0, s[44:47], s33 offen offset:2048 sc1\n"
    "v_mfma_f32_16x16x32_bf16 v[140:143], v[20:23], a[28:31], v[140:143]\n"
    "s_waitcnt lgkmcnt(2)\n"
    "v_mfma_f32_16x16x32_bf16 v[160:163], v[24:27], a[16:19], v[160:163]\n"
    "ds_read_b64_tr_b16 v[20:21], v7 offset:45056\n"
    "v_mfma_f32_16x16x32_bf16 v[164:167], v[24:27], a[20:23], v[164:167]\n"
    "ds_read_b64_tr_b16 v[22:23], v7 offset:47104\n"
    "v_mfma_f32_16x16x32_bf16 v[168:171], v[24:27], a[24:27], v[168:171]\n"
    "buffer_load_dwordx4 a[116:119], v0, s[44:47], s33 offen offset:3072 sc1\n"
    "v_mfma_f32_16x16x32_bf16 v[172:175], v[24:27], a[28:31], v[172:175]\n"
    "v_mfma_f32_16x16x32_bf16 v[192:195], v[28:31], a[16:19], v[192:195]\n"
    "ds_read_b64_tr_b16 v[24:25], v8 offset:45056\n"
    "v_mfma_f32_16x16x32_bf16 v[196:199], v[28:31], a[20:23], v[196:199]\n"
    "ds_read_b64_tr_b16 v[26:27], v8 offset:47104\n"
    "v_mfma_f32_16x16x32_bf16 v[200:203], v[28:31], a[24:27], v[200:203]\n"
    "buffer_load_dwordx4 v[224:227], v13, s[44:47], 0 offen offset:0 sc1\n"
    "v_mfma_f32_16x16x32_bf16 v[204:207], v[28:31], a[28:31], v[204:207]\n"
    "buffer_load_dwordx4 v[228:231], v13, s[44:47], 0 offen offset:256 sc1\n"
    "s_waitcnt lgkmcnt(2)\n"
    "v_mfma_f32_16x16x32_bf16 v[112:115], v[16:19], a[16:19], v[112:115]\n"
    "ds_read_b64_tr_b16 v[28:29], v9 offset:45056\n"
    "v_mfma_f32_16x16x32_bf16 v[116:119], v[16:19], a[20:23], v[116:119]\n"
    "ds_read_b64_tr_b16 v[30:31], v9 offset:47104\n"
    "v_mfma_f32_16x16x32_bf16 v[120:123], v[16:19], a[24:27], v[120:123]\n"
    "buffer_load_dwordx4 a[120:123], v0, s[44:47], s34 offen offset:2048 sc1\n"
    "v_mfma_f32_16x16x32_bf16 v[124:127], v[16:19], a[28:31], v[124:127]\n"
    "s_barrier\n"
    "v_mfma_f32_16x16x32_bf16 v[144:147], v[20:23], a[16:19], v[144:147]\n"
    "ds_read_b64_tr_b16 v[16:17], v6 offset:49152\n"
    "v_mfma_f32_16x16x32_bf16 v[148:151], v[20:23], a[20:23], v[148:151]\n"
    "ds_read_b64_tr_b16 v[18:19], v6 offset:51200\n"
    "v_mfma_f32_16x16x32_bf16 v[152:155], v[20:23], a[24:27], v[152:155]\n"
    "buffer_load_dwordx4 a[124:127], v0, s[44:47], s34 offen offset:3072 sc1\n"
    "v_mfma_f32_16x16x32_bf16 v[156:159], v[20:23], a[28:31], v[156:159]\n"
    "s_waitcnt lgkmcnt(2)\n"
    "v_mfma_f32_16x16x32_bf16 v[176:179], v[24:27], a[16:19], v[176:179]\n"
    "ds_read_b64_tr_b16 v[20:21], v7 offset:49152\n"
    "v_mfma_f32_16x16x32_bf16 v[180:183], v[24:27], a[20:23], v[180:183]\n"
    "ds_read_b64_tr_b16 v[22:23], v7 offset:51200\n"
    "v_mfma_f32_16x16x32_bf16 v[184:187], v[24:27], a[24:27], v[184:187]\n"
    "s_add_u32 s33, s56, 16384\n"
    "v_mfma_f32_16x16x32_bf16 v[188:191], v[24:27], a[28:31], v[188:191]\n"
    "buffer_load_dwordx4 a[128:131], v0, s[44:47], s33 offen offset:0 sc1\n"
    "v_mfma_f32_16x16x32_bf16 v[208:211], v[28:31], a[16:19], v[208:211]\n"
    "ds_read_b64_tr_b16 v[24:25], v8 offset:49152\n"
    "v_mfma_f32_16x16x32_bf16 v[212:215], v[28:31], a[20:23], v[212:215]\n"
    "ds_read_b64_tr_b16 v[26:27], v8 offset:51200\n"
    "v_mfma_f32_16x16x32_bf16 v[216:219], v[28:31], a[24:27], v[216:219]\n"
    "v_mfma_f32_16x16x32_bf16 v[220:223], v[28:31], a[28:31], v[220:223]\n"
    "buffer_load_dwordx4 a[132:135], v0, s[44:47], s33 offen offset:1024 sc1\n"
    "s_waitcnt vmcnt(24) lgkmcnt(2)\n"
    "v_mfma_f32_16x16x32_bf16 v[96:99], v[16:19], a[32:35], v[96:99]\n"
    "ds_read_b64_tr_b16 v[28:29], v9 offset:49152\n"
    "v_mfma_f32_16x16x32_bf16 v[100:103], v[16:19], a[36:39], v[100:103]\n"
    "ds_read_b64_tr_b16 v[30:31], v9 offset:51200\n"
    "v_mfma_f32_16x16x32_bf16 v[104:107], v[16:19], a[40:43], v[104:107]\n"
    "v_mfma_f32_16x16x32_bf16 v[108:111], v[16:19], a[44:47], v[108:111]\n"
    "s_add_u32 s34, s56, 53248\n"
    "buffer_load_dwordx4 a[136:139], v0, s[44:47], s34 offen offset:0 sc1\n"
    "v_mfma_f32_16x16x32_bf16 v[128:131], v[20:23], a[32:35], v[128:131]\n"
    "ds_read_b64_tr_b16 v[16:17], v6 offset:53248\n"
    "v_mfma_f32_16x16x32_bf16 v[132:135], v[20:23], a[36:39], v[132:135]\n"
    "ds_read_b64_tr_b16 v[18:19], v6 offset:55296\n"
    "v_mfma_f32_16x16x32_bf16 v[136:139], v[20:23], a[40:43], v[136:139]\n"
    "v_mfma_f32_16x16x32_bf16 v[140:143], v[20:23], a[44:47], v[140:143]\n"
    "s_waitcnt lgkmcnt(2)\n"
    "v_mfma_f32_16x16x32_bf16 v[160:163], v[24:27], a[32:35], v[160:163]\n"
    "ds_read_b64_tr_b16 v[20:21], v7 offset:53248\n"
    "v_mfma_f32_16x16x32_bf16 v[164:167], v[24:27], a[36:39], v[164:167]\n"
    "ds_read_b64_tr_b16 v[22:23], v7 offset:55296\n"
    "v_mfma_f32_16x16x32_bf16 v[168:171], v[24:27], a[40:43], v[168:171]\n"
    "buffer_load_dwordx4 a[140:143], v0, s[44:47], s34 offen offset:1024 sc1\n"
    "v_mfma_f32_16x16x32_bf16 v[172:175], v[24:27], a[44:47], v[172:175]\n"
    "v_mfma_f32_16x16x32_bf16 v[192:195], v[28:31], a[32:35], v[192:195]\n"
    "ds_read_b64_tr_b16 v[24:25], v8 offset:53248\n"
    "v_mfma_f32_16x16x32_bf16 v[196:199], v[28:31], a[36:39], v[196:199]\n"
    "ds_read_b64_tr_b16 v[26:27], v8 offset:55296\n"
    "v_mfma_f32_16x16x32_bf16 v[200:203], v[28:31], a[40:43], v[200:203]\n"
    "buffer_load_dwordx4 a[144:147], v0, s[44:47], s33 offen offset:2048 sc1\n"
    "v_mfma_f32_16x16x32_bf16 v[204:207], v[28:31], a[44:47], v[204:207]\n"
    "s_waitcnt lgkmcnt(2)\n"
    "v_mfma_f32_16x16x32_bf16 v[112:115], v[16:19], a[32:35], v[112:115]\n"
    "ds_read_b64_tr_b16 v[28:29], v9 offset:53248\n"
    "v_mfma_f32_16x16x32_bf16 v[116:119], v[16:19], a[36:39], v[116:119]\n"
    "ds_read_b64_tr_b16 v[30:31], v9 offset:55296\n"
    "v_mfma_f32_16x16x32_bf16 v[120:123], v[16:19], a[40:43], v[120:123]\n"
    "buffer_load_dwordx4 a[148:151], v0, s[44:47], s33 offen offset:3072 sc1\n"
    "v_mfma_f32_16x16x32_bf16 v[124:127], v[16:19], a[44:47], v[124:127]\n"
    "v_mfma_f32_16x16x32_bf16 v[144:147], v[20:23], a[32:35], v[144:147]\n"
    "ds_read_b64_tr_b16 v[16:17], v6 offset:57344\n"
    "v_mfma_f32_16x16x32_bf16 v[148:151], v[20:23], a[36:39], v[148:151]\n"
    "ds_read_b64_tr_b16 v[18:19], v6 offset:59392\n"
    "v_mfma_f32_16x16x32_bf16 v[152:155], v[20:23], a[40:43], v[152:155]\n"
    "buffer_load_dwordx4 a[152:155], v0, s[44:47], s34 offen offset:2048 sc1\n"
    "v_mfma_f32_16x16x32_bf16 v[156:159], v[20:23], a[44:47], v[156:159]\n"
    "s_waitcnt lgkmcnt(2)\n"
    "v_mfma_f32_16x16x32_bf16 v[176:179], v[24:27], a[32:35], v[176:179]\n"
    "ds_read_b64_tr_b16 v[20:21], v7 offset:57344\n"
    "v_mfma_f32_16x16x32_bf16 v[180:183], v[24:27], a[36:39], v[180:183]\n"
    "ds_read_b64_tr_b16 v[22:23], v7 offset:59392\n"
    "v_mfma_f32_16x16x32_bf16 v[184:187], v[24:27], a[40:43], v[184:187]\n"
    "v_mfma_f32_16x16x32_bf16 v[188:191], v[24:27], a[44:47], v[188:191]\n"
    "buffer_load_dwordx4 a[156:159], v0, s[44:47], s34 offen offset:3072 sc1\n"
    "v_mfma_f32_16x16x32_bf16 v[208:211], v[28:31], a[32:35], v[208:211]\n"
    "ds_read_b64_tr_b16 v[24:25], v8 offset:57344\n"
    "v_mfma_f32_16x16x32_bf16 v[212:215], v[28:31], a[36:39], v[212:215]\n"
    "ds_read_b64_tr_b16 v[26:27], v8 offset:59392\n"
    "v_mfma_f32_16x16x32_bf16 v[216:219], v[28:31], a[40:43], v[216:219]\n"
    "v_mfma_f32_16x16x32_bf16 v[220:223], v[28:31], a[44:47], v[220:223]\n"
    "s_waitcnt vmcnt(26) lgkmcnt(2)\n"
    "v_mfma_f32_16x16x32_bf16 v[96:99], v[16:19], a[48:51], v[96:99]\n"
    "ds_read_b64_tr_b16 v[28:29], v9 offset:57344\n"
    "s_add_u32 s33, s56, 20480\n"
    "v_mfma_f32_16x16x32_bf16 v[100:103], v[16:19], a[52:55], v[100:103]\n"
    "ds_read_b64_tr_b16 v[30:31], v9 offset:59392\n"
    "v_mfma_f32_16x16x32_bf16 v[104:107], v[16:19], a[56:59], v[104:107]\n"
    "buffer_load_dwordx4 a[160:163], v0, s[44:47], s33 offen offset:0 sc1\n"
    "v_mfma_f32_16x16x32_bf16 v[108:111], v[16:19], a[60:63], v[108:111]\n"
    "v_mfma_f32_16x16x32_bf16 v[128:131], v[20:23], a[48:51], v[128:131]\n"
    "ds_read_b64_tr_b16 v[16:17], v6 offset:61440\n"
    "v_mfma_f32_16x16x32_bf16 v[132:135], v[20:23], a[52:55], v[132:135]\n"
    "ds_read_b64_tr_b16 v[18:19], v6 offset:63488\n"
    "v_mfma_f32_16x16x32_bf16 v[136:139], v[20:23], a[56:59], v[136:139]\n"
    "buffer_load_dwordx4 a[164:167], v0, s[44:47], s33 offen offset:1024 sc1\n"
    "v_mfma_f32_16x16x32_bf16 v[140:143], v[20:23], a[60:63], v[140:143]\n"
    "s_waitcnt lgkmcnt(2)\n"
    "v_mfma_f32_16x16x32_bf16 v[160:163], v[24:27], a[48:51], v[160:163]\n"
    "ds_read_b64_tr_b16 v[20:21], v7 offset:61440\n"
    "v_mfma_f32_16x16x32_bf16 v[164:167], v[24:27], a[52:55], v[164:167]\n"
    "ds_read_b64_tr_b16 v[22:23], v7 offset:63488\n"
    "v_mfma_f32_16x16x32_bf16 v[168:171], v[24:27], a[56:59], v[168:171]\n"
    "s_add_u32 s34, s56, 57344\n"
    "v_mfma_f32_16x16x32_bf16 v[172:175], v[24:27], a[60:63], v[172:175]\n"
    "buffer_load_dwordx4 a[168:171], v0, s[44:47], s34 offen offset:0 sc1\n"
    "v_mfma_f32_16x16x32_bf16 v[192:195], v[28:31], a[48:51], v[192:195]\n"
    "ds_read_b64_tr_b16 v[24:25], v8 offset:61440\n"
    "v_mfma_f32_16x16x32_bf16 v[196:199], v[28:31], a[52:55], v[196:199]\n"
    "ds_read_b64_tr_b16 v[26:27], v8 offset:63488\n"
    "v_mfma_f32_16x16x32_bf16 v[200:203], v[28:31], a[56:59], v[200:203]\n"
    "buffer_load_dwordx4 a[172:175], v0, s[44:47], s34 offen offset:1024 sc1\n"
    "v_mfma_f32_16x16x32_bf16 v[204:207], v[28:31], a[60:63], v[204:207]\n"
    "s_waitcnt lgkmcnt(2)\n"
    "v_mfma_f32_16x16x32_bf16 v[112:115], v[16:19], a[48:51], v[112:115]\n"
    "ds_read_b64_tr_b16 v[28:29], v9 offset:61440\n"
    "v_mfma_f32_16x16x32_bf16 v[116:119], v[16:19], a[52:55], v[116:119]\n"
    "ds_read_b64_tr_b16 v[30:31], v9 offset:63488\n"
    "v_mfma_f32_16x16x32_bf16 v[120:123], v[16:19], a[56:59], v[120:123]\n"
    "v_mfma_f32_16x16x32_bf16 v[124:127], v[16:19], a[60:63], v[124:127]\n"
    "s_barrier\n"
    "v_mfma_f32_16x16x32_bf16 v[144:147], v[20:23], a[48:51], v[144:147]\n"
    "ds_read_b64_tr_b16 v[16:17], v6 offset:0\n"
    "buffer_load_dwordx4 a[176:179], v0, s[44:47], s33 offen offset:2048 sc1\n"
    "v_mfma_f32_16x16x32_bf16 v[148:151], v[20:23], a[52:55], v[148:151]\n"
    "ds_read_b64_tr_b16 v[18:19], v6 offset:2048\n"
    "v_mfma_f32_16x16x32_bf16 v[152:155], v[20:23], a[56:59], v[152:155]\n"
    "v_mfma_f32_16x16x32_bf16 v[156:159], v[20:23], a[60:63], v[156:159]\n"
    "s_waitcnt lgkmcnt(2)\n"
    "v_mfma_f32_16x16x32_bf16 v[176:179], v[24:27], a[48:51], v[176:179]\n"
    "ds_read_b64_tr_b16 v[20:21], v7 offset:0\n"
    "v_mfma_f32_16x16x32_bf16 v[180:183], v[24:27], a[52:55], v[180:183]\n"
    "ds_read_b64_tr_b16 v[22:23], v7 offset:2048\n"
    "v_mfma_f32_16x16x32_bf16 v[184:187], v[24:27], a[56:59], v[184:187]\n"
    "buffer_load_dwordx4 a[180:183], v0, s[44:47], s33 offen offset:3072 sc1\n"
    "v_mfma_f32_16x16x32_bf16 v[188:191], v[24:27], a[60:63], v[188:191]\n"
    "v_mfma_f32_16x16x32_bf16 v[208:211], v[28:31], a[48:51], v[208:211]\n"
    "ds_read_b64_tr_b16 v[24:25], v8 offset:0\n"
    "v_mfma_f32_16x16x32_bf16 v[212:215], v[28:31], a[52:55], v[212:215]\n"
    "ds_read_b64_tr_b16 v[26:27], v8 offset:2048\n"
    "v_mfma_f32_16x16x32_bf16 v[216:219], v[28:31], a[56:59], v[216:219]\n"
    "buffer_load_dwordx4 a[184:187], v0, s[44:47], s34 offen offset:2048 sc1\n"
    "v_mfma_f32_16x16x32_bf16 v[220:223], v[28:31], a[60:63], v[220:223]\n"
    "s_waitcnt vmcnt(29) lgkmcnt(2)\n"
    "v_mfma_f32_16x16x32_bf16 v[96:99], v[16:19], a[64:67], v[96:99]\n"
    "ds_read_b64_tr_b16 v[28:29], v9 offset:0\n"
    "v_mfma_f32_16x16x32_bf16 v[100:103], v[16:19], a[68:71], v[100:103]\n"
    "ds_read_b64_tr_b16 v[30:31], v9 offset:2048\n"
    "v_mfma_f32_16x16x32_bf16 v[104:107], v[16:19], a[72:75], v[104:107]\n"
    "buffer_load_dwordx4 a[188:191], v0, s[44:47], s34 offen offset:3072 sc1\n"
    "v_mfma_f32_16x16x32_bf16 v[108:111], v[16:19], a[76:79], v[108:111]\n"
    "v_mfma_f32_16x16x32_bf16 v[128:131], v[20:23], a[64:67], v[128:131]\n"
    "ds_read_b64_tr_b16 v[16:17], v6 offset:4096\n"
    "v_mfma_f32_16x16x32_bf16 v[132:135], v[20:23], a[68:71], v[132:135]\n"
    "ds_read_b64_tr_b16 v[18:19], v6 offset:6144\n"
    "v_mfma_f32_16x16x32_bf16 v[136:139], v[20:23], a[72:75], v[136:139]\n"
    "s_add_u32 s33, s56, 24576\n"
    "buffer_load_dwordx4 a[192:195], v0, s[44:47], s33 offen offset:0 sc1\n"
    "v_mfma_f32_16x16x32_bf16 v[140:143], v[20:23], a[76:79], v[140:143]\n"
    "s_waitcnt lgkmcnt(2)\n"
    "v_mfma_f32_16x16x32_bf16 v[160:163], v[24:27], a[64:67], v[160:163]\n"
    "ds_read_b64_tr_b16 v[20:21], v7 offset:4096\n"
    "v_mfma_f32_16x16x32_bf16 v[164:167], v[24:27], a[68:71], v[164:167]\n"
    "ds_read_b64_tr_b16 v[22:23], v7 offset:6144\n"
    "v_mfma_f32_16x16x32_bf16 v[168:171], v[24:27], a[72:75], v[168:171]\n"
    "v_mfma_f32_16x16x32_bf16 v[172:175], v[24:27], a[76:79], v[172:175]\n"
    "buffer_load_dwordx4 a[196:199], v0, s[44:47], s33 offen offset:1024 sc1\n"
    "v_mfma_f32_16x16x32_bf16 v[192:195], v[28:31], a[64:67], v[192:195]\n"
    "ds_read_b64_tr_b16 v[24:25], v8 offset:4096\n"
    "v_mfma_f32_16x16x32_bf16 v[196:199], v[28:31], a[68:71], v[196:199]\n"
    "ds_read_b64_tr_b16 v[26:27], v8 offset:6144\n"
    "v_mfma_f32_16x16x32_bf16 v[200:203], v[28:31], a[72:75], v[200:203]\n"
    "v_mfma_f32_16x16x32_bf16 v[204:207], v[28:31], a[76:79], v[204:207]\n"
    "s_waitcnt lgkmcnt(2)\n"
    "v_mfma_f32_16x16x32_bf16 v[112:115], v[16:19], a[64:67], v[112:115]\n"
    "ds_read_b64_tr_b16 v[28:29], v9 offset:4096\n"
    "s_add_u32 s34, s56, 61440\n"
    "v_mfma_f32_16x16x32_bf16 v[116:119], v[16:19], a[68:71], v[116:119]\n"
    "ds_read_b64_tr_b16 v[30:31], v9 offset:6144\n"
    "v_mfma_f32_16x16x32_bf16 v[120:123], v[16:19], a[72:75], v[120:123]\n"
    "buffer_load_dwordx4 a[200:203], v0, s[44:47], s34 offen offset:0 sc1\n"
    "v_mfma_f32_16x16x32_bf16 v[124:127], v[16:19], a[76:79], v[124:127]\n"
    "v_mfma_f32_16x16x32_bf16 v[144:147], v[20:23], a[64:67], v[144:147]\n"
    "ds_read_b64_tr_b16 v[16:17], v6 offset:8192\n"
    "v_mfma_f32_16x16x32_bf16 v[148:151], v[20:23], a[68:71], v[148:151]\n"
    "ds_read_b64_tr_b16 v[18:19], v6 offset:10240\n"
    "v_mfma_f32_16x16x32_bf16 v[152:155], v[20:23], a[72:75], v[152:155]\n"
    "buffer_load_dwordx4 a[204:207], v0, s[44:47], s34 offen offset:1024 sc1\n"
    "v_mfma_f32_16x16x32_bf16 v[156:159], v[20:23], a[76:79], v[156:159]\n"
    "s_waitcnt lgkmcnt(2)\n"
    "v_mfma_f32_16x16x32_bf16 v[176:179], v[24:27], a[64:67], v[176:179]\n"
    "ds_read_b64_tr_b16 v[20:21], v7 offset:8192\n"
    "v_mfma_f32_16x16x32_bf16 v[180:183], v[24:27], a[68:71], v[180:183]\n"
    "ds_read_b64_tr_b16 v[22:23], v7 offset:10240\n"
    "v_mfma_f32_16x16x32_bf16 v[184:187], v[24:27], a[72:75], v[184:187]\n"
    "buffer_load_dwordx4 a[208:211], v0, s[44:47], s33 offen offset:2048 sc1\n"
    "v_mfma_f32_16x16x32_bf16 v[188:191], v[24:27], a[76:79], v[188:191]\n"
    "v_mfma_f32_16x16x32_bf16 v[208:211], v[28:31], a[64:67], v[208:211]\n"
    "ds_read_b64_tr_b16 v[24:25], v8 offset:8192\n"
    "v_mfma_f32_16x16x32_bf16 v[212:215], v[28:31], a[68:71], v[212:215]\n"
    "ds_read_b64_tr_b16 v[26:27], v8 offset:10240\n"
    "v_mfma_f32_16x16x32_bf16 v[216:219], v[28:31], a[72:75], v[216:219]\n"
    "buffer_load_dwordx4 a[212:215], v0, s[44:47], s33 offen offset:3072 sc1\n"
    "v_mfma_f32_16x16x32_bf16 v[220:223], v[28:31], a[76:79], v[220:223]\n"
    "s_waitcnt vmcnt(32) lgkmcnt(2)\n"
    "v_mfma_f32_16x16x32_bf16 v[96:99], v[16:19], a[80:83], v[96:99]\n"
    "ds_read_b64_tr_b16 v[28:29], v9 offset:8192\n"
    "v_mfma_f32_16x16x32_bf16 v[100:103], v[16:19], a[84:87], v[100:103]\n"
    "ds_read_b64_tr_b16 v[30:31], v9 offset:10240\n"
    "v_mfma_f32_16x16x32_bf16 v[104:107], v[16:19], a[88:91], v[104:107]\n"
    "v_mfma_f32_16x16x32_bf16 v[108:111], v[16:19], a[92:95], v[108:111]\n"
    "buffer_load_dwordx4 a[216:219], v0, s[44:47], s34 offen offset:2048 sc1\n"
    "v_mfma_f32_16x16x32_bf16 v[128:131], v[20:23], a[80:83], v[128:131]\n"
    "ds_read_b64_tr_b16 v[16:17], v6 offset:12288\n"
    "v_mfma_f32_16x16x32_bf16 v[132:135], v[20:23], a[84:87], v[132:135]\n"
    "ds_read_b64_tr_b16 v[18:19], v6 offset:14336\n"
    "v_mfma_f32_16x16x32_bf16 v[136:139], v[20:23], a[88:91], v[136:139]\n"
    "v_mfma_f32_16x16x32_bf16 v[140:143], v[20:23], a[92:95], v[140:143]\n"
    "buffer_load_dwordx4 a[220:223], v0, s[44:47], s34 offen offset:3072 sc1\n"
    "s_waitcnt lgkmcnt(2)\n"
    "v_mfma_f32_16x16x32_bf16 v[160:163], v[24:27], a[80:83], v[160:163]\n"
    "ds_read_b64_tr_b16 v[20:21], v7 offset:12288\n"
    "v_mfma_f32_16x16x32_bf16 v[164:167], v[24:27], a[84:87], v[164:167]\n"
    "ds_read_b64_tr_b16 v[22:23], v7 offset:14336\n"
    "v_mfma_f32_16x16x32_bf16 v[168:171], v[24:27], a[88:91], v[168:171]\n"
    "v_mfma_f32_16x16x32_bf16 v[172:175], v[24:27], a[92:95], v[172:175]\n"
    "v_mfma_f32_16x16x32_bf16 v[192:195], v[28:31], a[80:83], v[192:195]\n"
    "ds_read_b64_tr_b16 v[24:25], v8 offset:12288\n"
    "s_add_u32 s33, s56, 28672\n"
    "v_mfma_f32_16x16x32_bf16 v[196:199], v[28:31], a[84:87], v[196:199]\n"
    "ds_read_b64_tr_b16 v[26:27], v8 offset:14336\n"
    "v_mfma_f32_16x16x32_bf16 v[200:203], v[28:31], a[88:91], v[200:203]\n"
    "buffer_load_dwordx4 a[224:227], v0, s[44:47], s33 offen offset:0 sc1\n"
    "v_mfma_f32_16x16x32_bf16 v[204:207], v[28:31], a[92:95], v[204:207]\n"
    "s_waitcnt lgkmcnt(2)\n"
    "v_mfma_f32_16x16x32_bf16 v[112:115], v[16:19], a[80:83], v[112:115]\n"
    "ds_read_b64_tr_b16 v[28:29], v9 offset:12288\n"
    "v_mfma_f32_16x16x32_bf16 v[116:119], v[16:19], a[84:87], v[116:119]\n"
    "ds_read_b64_tr_b16 v[30:31], v9 offset:14336\n"
    "v_mfma_f32_16x16x32_bf16 v[120:123], v[16:19], a[88:91], v[120:123]\n"
    "buffer_load_dwordx4 a[228:231], v0, s[44:47], s33 offen offset:1024 sc1\n"
    "v_mfma_f32_16x16x32_bf16 v[124:127], v[16:19], a[92:95], v[124:127]\n"
    "s_barrier\n"
    "v_mfma_f32_16x16x32_bf16 v[144:147], v[20:23], a[80:83], v[144:147]\n"
    "v_add_u32 v6, 0x10000, v6\n"
    "ds_read_b64_tr_b16 v[16:17], v6 offset:0\n"
    "v_mfma_f32_16x16x32_bf16 v[148:151], v[20:23], a[84:87], v[148:151]\n"
    "ds_read_b64_tr_b16 v[18:19], v6 offset:2048\n"
    "v_mfma_f32_16x16x32_bf16 v[152:155], v[20:23], a[88:91], v[152:155]\n"
    "s_add_u32 s34, s56, 65536\n"
    "buffer_load_dwordx4 a[232:235], v0, s[44:47], s34 offen offset:0 sc1\n"
    "v_mfma_f32_16x16x32_bf16 v[156:159], v[20:23], a[92:95], v[156:159]\n"
    "s_waitcnt lgkmcnt(2)\n"
    "v_mfma_f32_16x16x32_bf16 v[176:179], v[24:27], a[80:83], v[176:179]\n"
    "v_add_u32 v7, 0x10000, v7\n"
    "ds_read_b64_tr_b16 v[20:21], v7 offset:0\n"
    "v_mfma_f32_16x16x32_bf16 v[180:183], v[24:27], a[84:87], v[180:183]\n"
    "ds_read_b64_tr_b16 v[22:23], v7 offset:2048\n"
    "v_mfma_f32_16x16x32_bf16 v[184:187], v[24:27], a[88:91], v[184:187]\n"
    "buffer_load_dwordx4 a[236:239], v0, s[44:47], s34 offen offset:1024 sc1\n"
    "v_mfma_f32_16x16x32_bf16 v[188:191], v[24:27], a[92:95], v[188:191]\n"
    "v_mfma_f32_16x16x32_bf16 v[208:211], v[28:31], a[80:83], v[208:211]\n"
    "v_add_u32 v8, 0x10000, v8\n"
    "ds_read_b64_tr_b16 v[24:25], v8 offset:0\n"
    "v_mfma_f32_16x16x32_bf16 v[212:215], v[28:31], a[84:87], v[212:215]\n"
    "ds_read_b64_tr_b16 v[26:27], v8 offset:2048\n"
    "v_mfma_f32_16x16x32_bf16 v[216:219], v[28:31], a[88:91], v[216:219]\n"
    "v_mfma_f32_16x16x32_bf16 v[220:223], v[28:31], a[92:95], v[220:223]\n"
    "buffer_load_dwordx4 a[240:243], v0, s[44:47], s33 offen offset:2048 sc1\n"
    "s_waitcnt vmcnt(35) lgkmcnt(2)\n"
    "v_mfma_f32_16x16x32_bf16 v[96:99], v[16:19], a[96:99], v[96:99]\n"
    "v_add_u32 v9, 0x10000, v9\n"
    "ds_read_b64_tr_b16 v[28:29], v9 offset:0\n"
    "s_waitcnt vmcnt(31)\n"
    "v_mfma_f32_16x16x32_bf16 v[100:103], v[16:19], a[100:103], v[100:103]\n"
    "ds_read_b64_tr_b16 v[30:31], v9 offset:2048\n"
    "v_add_f32 v224, v224, v228\n"
    "v_mfma_f32_16x16x32_bf16 v[104:107], v[16:19], a[104:107], v[104:107]\n"
    "v_add_f32 v225, v225, v229\n"
    "v_add_f32 v226, v226, v230\n"
    "v_mfma_f32_16x16x32_bf16 v[108:111], v[16:19], a[108:111], v[108:111]\n"
    "v_add_f32 v227, v227, v231\n"
    "v_mov_b32 v228, v224\n"
    "v_mfma_f32_16x16x32_bf16 v[128:131], v[20:23], a[96:99], v[128:131]\n"
    "ds_read_b64_tr_b16 v[16:17], v6 offset:4096\n"
    "v_mov_b32 v229, v225\n"
    "v_mfma_f32_16x16x32_bf16 v[132:135], v[20:23], a[100:103], v[132:135]\n"
    "ds_read_b64_tr_b16 v[18:19], v6 offset:6144\n"
    "v_mov_b32 v230, v226\n"
    "v_mfma_f32_16x16x32_bf16 v[136:139], v[20:23], a[104:107], v[136:139]\n"
    "buffer_load_dwordx4 a[244:247], v0, s[44:47], s33 offen offset:3072 sc1\n"
    "v_mfma_f32_16x16x32_bf16 v[140:143], v[20:23], a[108:111], v[140:143]\n"
    "v_mov_b32 v231, v227\n"
    "s_waitcnt lgkmcnt(2)\n"
    "v_mfma_f32_16x16x32_bf16 v[160:163], v[24:27], a[96:99], v[160:163]\n"
    "ds_read_b64_tr_b16 v[20:21], v7 offset:4096\n"
    "v_permlane16_swap_b32 v224, v228\n"
    "v_mfma_f32_16x16x32_bf16 v[164:167], v[24:27], a[100:103], v[164:167]\n"
    "ds_read_b64_tr_b16 v[22:23], v7 offset:6144\n"
    "v_permlane16_swap_b32 v225, v229\n"
    "v_mfma_f32_16x16x32_bf16 v[168:171], v[24:27], a[104:107], v[168:171]\n"
    "s_mov_b32 exec_hi, 15\n"
    "buffer_load_dword v252, v253, s[36:39], 0 offen offset:1024\n"
    "s_mov_b32 exec_hi, -1\n"
    "v_mfma_f32_16x16x32_bf16 v[172:175], v[24:27], a[108:111], v[172:175]\n"
    "v_permlane16_swap_b32 v226, v230\n"
    "v_permlane16_swap_b32 v227, v231\n"
    "v_mfma_f32_16x16x32_bf16 v[192:195], v[28:31], a[96:99], v[192:195]\n"
    "ds_read_b64_tr_b16 v[24:25], v8 offset:4096\n"
    "v_add_f32 v224, v224, v228\n"
    "v_mfma_f32_16x16x32_bf16 v[196:199], v[28:31], a[100:103], v[196:199]\n"
    "ds_read_b64_tr_b16 v[26:27], v8 offset:6144\n"
    "v_add_f32 v225, v225, v229\n"
    "v_mfma_f32_16x16x32_bf16 v[200:203], v[28:31], a[104:107], v[200:203]\n"
    "buffer_load_dwordx4 a[248:251], v0, s[44:47], s34 offen offset:2048 sc1\n"
    "v_mfma_f32_16x16x32_bf16 v[204:207], v[28:31], a[108:111], v[204:207]\n"
    "v_add_f32 v226, v226, v230\n"
    "s_waitcnt lgkmcnt(2)\n"
    "v_mfma_f32_16x16x32_bf16 v[112:115], v[16:19], a[96:99], v[112:115]\n"
    "ds_read_b64_tr_b16 v[28:29], v9 offset:4096\n"
    "v_add_f32 v227, v227, v231\n"
    "v_mfma_f32_16x16x32_bf16 v[116:119], v[16:19], a[100:103], v[116:119]\n"
    "ds_read_b64_tr_b16 v[30:31], v9 offset:6144\n"
    "v_mov_b32 v228, v224\n"
    "v_mfma_f32_16x16x32_bf16 v[120:123], v[16:19], a[104:107], v[120:123]\n"
    "buffer_load_dwordx4 a[252:255], v0, s[44:47], s34 offen offset:3072 sc1\n"
    "v_mfma_f32_16x16x32_bf16 v[124:127], v[16:19], a[108:111], v[124:127]\n"
    "v_mov_b32 v229, v225\n"
    "v_mov_b32 v230, v226\n"
    "v_mfma_f32_16x16x32_bf16 v[144:147], v[20:23], a[96:99], v[144:147]\n"
    "ds_read_b64_tr_b16 v[16:17], v6 offset:8192\n"
    "v_mov_b32 v231, v227\n"
    "v_mfma_f32_16x16x32_bf16 v[148:151], v[20:23], a[100:103], v[148:151]\n"
    "ds_read_b64_tr_b16 v[18:19], v6 offset:10240\n"
    "v_permlane32_swap_b32 v224, v228\n"
    "v_mfma_f32_16x16x32_bf16 v[152:155], v[20:23], a[104:107], v[152:155]\n"
    "v_permlane32_swap_b32 v225, v229\n"
    "v_permlane32_swap_b32 v226, v230\n"
    "v_mfma_f32_16x16x32_bf16 v[156:159], v[20:23], a[108:111], v[156:159]\n"
    "v_permlane32_swap_b32 v227, v231\n"
    "s_waitcnt lgkmcnt(2)\n"
    "v_mfma_f32_16x16x32_bf16 v[176:179], v[24:27], a[96:99], v[176:179]\n"
    "ds_read_b64_tr_b16 v[20:21], v7 offset:8192\n"
    "v_add_f32 v224, v224, v228\n"
    "v_mfma_f32_16x16x32_bf16 v[180:183], v[24:27], a[100:103], v[180:183]\n"
    "ds_read_b64_tr_b16 v[22:23], v7 offset:10240\n"
    "v_add_f32 v225, v225, v229\n"
    "v_mfma_f32_16x16x32_bf16 v[184:187], v[24:27], a[104:107], v[184:187]\n"
    "v_add_f32 v226, v226, v230\n"
    "v_add_f32 v227, v227, v231\n"
    "v_mfma_f32_16x16x32_bf16 v[188:191], v[24:27], a[108:111], v[188:191]\n"
    "v_mul_f32 v224, 0x3ae38e39, v224\n"
    "v_mul_f32 v225, 0x3ae38e39, v225\n"
    "v_mfma_f32_16x16x32_bf16 v[208:211], v[28:31], a[96:99], v[208:211]\n"
    "ds_read_b64_tr_b16 v[24:25], v8 offset:8192\n"
    "v_mul_f32 v226, 0x3ae38e39, v226\n"
    "v_mfma_f32_16x16x32_bf16 v[212:215], v[28:31], a[100:103], v[212:215]\n"
    "ds_read_b64_tr_b16 v[26:27], v8 offset:10240\n"
    "v_mul_f32 v227, 0x3ae38e39, v227\n"
    "v_mfma_f32_16x16x32_bf16 v[216:219], v[28:31], a[104:107], v[216:219]\n"
    "v_add_f32 v224, 0x3727c5ac, v224\n"
    "v_add_f32 v225, 0x3727c5ac, v225\n"
    "v_mfma_f32_16x16x32_bf16 v[220:223], v[28:31], a[108:111], v[220:223]\n"
    "v_add_f32 v226, 0x3727c5ac, v226\n"
    "s_waitcnt vmcnt(33) lgkmcnt(2)\n"
    "v_mfma_f32_16x16x32_bf16 v[96:99], v[16:19], a[112:115], v[96:99]\n"
    "ds_read_b64_tr_b16 v[28:29], v9 offset:8192\n"
    "v_add_f32 v227, 0x3727c5ac, v227\n"
    "v_mfma_f32_16x16x32_bf16 v[100:103], v[16:19], a[116:119], v[100:103]\n"
    "ds_read_b64_tr_b16 v[30:31], v9 offset:10240\n"
    "v_mfma_f32_16x16x32_bf16 v[104:107], v[16:19], a[120:123], v[104:107]\n"
    "v_rsq_f32 v228, v224\n"
    "v_mfma_f32_16x16x32_bf16 v[108:111], v[16:19], a[124:127], v[108:111]\n"
    "v_rsq_f32 v229, v225\n"
    "v_mfma_f32_16x16x32_bf16 v[128:131], v[20:23], a[112:115], v[128:131]\n"
    "ds_read_b64_tr_b16 v[16:17], v6 offset:12288\n"
    "v_mul_f32 v48, 0xbfb8aa3b, v228\n"
    "v_mfma_f32_16x16x32_bf16 v[132:135], v[20:23], a[116:119], v[132:135]\n"
    "ds_read_b64_tr_b16 v[18:19], v6 offset:14336\n"
    "v_mul_f32 v49, 0xbfb8aa3b, v229\n"
    "v_mfma_f32_16x16x32_bf16 v[136:139], v[20:23], a[120:123], v[136:139]\n"
    "v_rsq_f32 v230, v226\n"
    "v_mfma_f32_16x16x32_bf16 v[140:143], v[20:23], a[124:127], v[140:143]\n"
    "v_mul_f32 v50, 0xbfb8aa3b, v230\n"
    "s_waitcnt lgkmcnt(2)\n"
    "v_mfma_f32_16x16x32_bf16 v[160:163], v[24:27], a[112:115], v[160:163]\n"
    "ds_read_b64_tr_b16 v[20:21], v7 offset:12288\n"
    "v_mul_f32 v52, v224, v228\n"
    "v_mfma_f32_16x16x32_bf16 v[164:167], v[24:27], a[116:119], v[164:167]\n"
    "ds_read_b64_tr_b16 v[22:23], v7 offset:14336\n"
    "v_mul_f32 v53, v225, v229\n"
    "v_mfma_f32_16x16x32_bf16 v[168:171], v[24:27], a[120:123], v[168:171]\n"
    "v_rsq_f32 v231, v227\n"
    "v_mfma_f32_16x16x32_bf16 v[172:175], v[24:27], a[124:127], v[172:175]\n"
    "v_mul_f32 v51, 0xbfb8aa3b, v231\n"
    "v_mul_f32 v54, v226, v230\n"
    "v_mfma_f32_16x16x32_bf16 v[192:195], v[28:31], a[112:115], v[192:195]\n"
    "ds_read_b64_tr_b16 v[24:25], v8 offset:12288\n"
    "v_mul_f32 v55, v227, v231\n"
    "v_mfma_f32_16x16x32_bf16 v[196:199], v[28:31], a[116:119], v[196:199]\n"
    "ds_read_b64_tr_b16 v[26:27], v8 offset:14336\n"
    "v_mfma_f32_16x16x32_bf16 v[200:203], v[28:31], a[120:123], v[200:203]\n"
    "v_mfma_f32_16x16x32_bf16 v[204:207], v[28:31], a[124:127], v[204:207]\n"
    "s_waitcnt lgkmcnt(2)\n"
    "v_mfma_f32_16x16x32_bf16 v[112:115], v[16:19], a[112:115], v[112:115]\n"
    "ds_read_b64_tr_b16 v[28:29], v9 offset:12288\n"
    "v_mfma_f32_16x16x32_bf16 v[116:119], v[16:19], a[116:119], v[116:119]\n"
    "ds_read_b64_tr_b16 v[30:31], v9 offset:14336\n"
    "v_mfma_f32_16x16x32_bf16 v[120:123], v[16:19], a[120:123], v[120:123]\n"
    "v_mfma_f32_16x16x32_bf16 v[124:127], v[16:19], a[124:127], v[124:127]\n"
    "s_barrier\n"
    "v_mfma_f32_16x16x32_bf16 v[144:147], v[20:23], a[112:115], v[144:147]\n"
    "ds_read_b64_tr_b16 v[16:17], v6 offset:16384\n"
    "v_mfma_f32_16x16x32_bf16 v[148:151], v[20:23], a[116:119], v[148:151]\n"
    "ds_read_b64_tr_b16 v[18:19], v6 offset:18432\n"
    "v_mfma_f32_16x16x32_bf16 v[152:155], v[20:23], a[120:123], v[152:155]\n"
    "v_mfma_f32_16x16x32_bf16 v[156:159], v[20:23], a[124:127], v[156:159]\n"
    "s_waitcnt lgkmcnt(2)\n"
    "v_mfma_f32_16x16x32_bf16 v[176:179], v[24:27], a[112:115], v[176:179]\n"
    "ds_read_b64_tr_b16 v[20:21], v7 offset:16384\n"
    "v_mfma_f32_16x16x32_bf16 v[180:183], v[24:27], a[116:119], v[180:183]\n"
    "ds_read_b64_tr_b16 v[22:23], v7 offset:18432\n"
    "v_mfma_f32_16x16x32_bf16 v[184:187], v[24:27], a[120:123], v[184:187]\n"
    "v_mfma_f32_16x16x32_bf16 v[188:191], v[24:27], a[124:127], v[188:191]\n"
    "v_mfma_f32_16x16x32_bf16 v[208:211], v[28:31], a[112:115], v[208:211]\n"
    "ds_read_b64_tr_b16 v[24:25], v8 offset:16384\n"
    "v_mfma_f32_16x16x32_bf16 v[212:215], v[28:31], a[116:119], v[212:215]\n"
    "ds_read_b64_tr_b16 v[26:27], v8 offset:18432\n"
    "v_mfma_f32_16x16x32_bf16 v[216:219], v[28:31], a[120:123], v[216:219]\n"
    "v_mfma_f32_16x16x32_bf16 v[220:223], v[28:31], a[124:127], v[220:223]\n"
    "s_waitcnt vmcnt(29) lgkmcnt(2)\n"
    "v_mfma_f32_16x16x32_bf16 v[96:99], v[16:19], a[128:131], v[96:99]\n"
    "ds_read_b64_tr_b16 v[28:29], v9 offset:16384\n"
    "v_mfma_f32_16x16x32_bf16 v[100:103], v[16:19], a[132:135], v[100:103]\n"
    "ds_read_b64_tr_b16 v[30:31], v9 offset:18432\n"
    "v_mfma_f32_16x16x32_bf16 v[104:107], v[16:19], a[136:139], v[104:107]\n"
    "v_mfma_f32_16x16x32_bf16 v[108:111], v[16:19], a[140:143], v[108:111]\n"
    "v_mfma_f32_16x16x32_bf16 v[128:131], v[20:23], a[128:131], v[128:131]\n"
    "ds_read_b64_tr_b16 v[16:17], v6 offset:20480\n"
    "v_mfma_f32_16x16x32_bf16 v[132:135], v[20:23], a[132:135], v[132:135]\n"
    "ds_read_b64_tr_b16 v[18:19], v6 offset:22528\n"
    "v_mfma_f32_16x16x32_bf16 v[136:139], v[20:23], a[136:139], v[136:139]\n"
    "v_mfma_f32_16x16x32_bf16 v[140:143], v[20:23], a[140:143], v[140:143]\n"
    "s_waitcnt lgkmcnt(2)\n"
    "v_mfma_f32_16x16x32_bf16 v[160:163], v[24:27], a[128:131], v[160:163]\n"
    "ds_read_b64_tr_b16 v[20:21], v7 offset:20480\n"
    "v_mfma_f32_16x16x32_bf16 v[164:167], v[24:27], a[132:135], v[164:167]\n"
    "ds_read_b64_tr_b16 v[22:23], v7 offset:22528\n"
    "v_mfma_f32_16x16x32_bf16 v[168:171], v[24:27], a[136:139], v[168:171]\n"
    "v_mfma_f32_16x16x32_bf16 v[172:175], v[24:27], a[140:143], v[172:175]\n"
    "v_mfma_f32_16x16x32_bf16 v[192:195], v[28:31], a[128:131], v[192:195]\n"
    "ds_read_b64_tr_b16 v[24:25], v8 offset:20480\n"
    "v_mfma_f32_16x16x32_bf16 v[196:199], v[28:31], a[132:135], v[196:199]\n"
    "ds_read_b64_tr_b16 v[26:27], v8 offset:22528\n"
    "v_mfma_f32_16x16x32_bf16 v[200:203], v[28:31], a[136:139], v[200:203]\n"
    "v_mfma_f32_16x16x32_bf16 v[204:207], v[28:31], a[140:143], v[204:207]\n"
    "s_waitcnt lgkmcnt(2)\n"
    "v_mfma_f32_16x16x32_bf16 v[112:115], v[16:19], a[128:131], v[112:115]\n"
    "ds_read_b64_tr_b16 v[28:29], v9 offset:20480\n"
    "v_mfma_f32_16x16x32_bf16 v[116:119], v[16:19], a[132:135], v[116:119]\n"
    "ds_read_b64_tr_b16 v[30:31], v9 offset:22528\n"
    "v_mfma_f32_16x16x32_bf16 v[120:123], v[16:19], a[136:139], v[120:123]\n"
    "v_mfma_f32_16x16x32_bf16 v[124:127], v[16:19], a[140:143], v[124:127]\n"
    "v_mfma_f32_16x16x32_bf16 v[144:147], v[20:23], a[128:131], v[144:147]\n"
    "ds_read_b64_tr_b16 v[16:17], v6 offset:24576\n"
    "v_mfma_f32_16x16x32_bf16 v[148:151], v[20:23], a[132:135], v[148:151]\n"
    "ds_read_b64_tr_b16 v[18:19], v6 offset:26624\n"
    "v_mfma_f32_16x16x32_bf16 v[152:155], v[20:23], a[136:139], v[152:155]\n"
    "v_mfma_f32_16x16x32_bf16 v[156:159], v[20:23], a[140:143], v[156:159]\n"
    "s_waitcnt lgkmcnt(2)\n"
    "v_mfma_f32_16x16x32_bf16 v[176:179], v[24:27], a[128:131], v[176:179]\n"
    "ds_read_b64_tr_b16 v[20:21], v7 offset:24576\n"
    "v_mfma_f32_16x16x32_bf16 v[180:183], v[24:27], a[132:135], v[180:183]\n"
    "ds_read_b64_tr_b16 v[22:23], v7 offset:26624\n"
    "v_mfma_f32_16x16x32_bf16 v[184:187], v[24:27], a[136:139], v[184:187]\n"
    "v_mfma_f32_16x16x32_bf16 v[188:191], v[24:27], a[140:143], v[188:191]\n"
    "v_mfma_f32_16x16x32_bf16 v[208:211], v[28:31], a[128:131], v[208:211]\n"
    "ds_read_b64_tr_b16 v[24:25], v8 offset:24576\n"
    "v_mfma_f32_16x16x32_bf16 v[212:215], v[28:31], a[132:135], v[212:215]\n"
    "ds_read_b64_tr_b16 v[26:27], v8 offset:26624\n"
    "v_mfma_f32_16x16x32_bf16 v[216:219], v[28:31], a[136:139], v[216:219]\n"
    "v_mfma_f32_16x16x32_bf16 v[220:223], v[28:31], a[140:143], v[220:223]\n"
    "s_waitcnt vmcnt(25) lgkmcnt(2)\n"
    "v_mfma_f32_16x16x32_bf16 v[96:99], v[16:19], a[144:147], v[96:99]\n"
    "ds_read_b64_tr_b16 v[28:29], v9 offset:24576\n"
    "v_mfma_f32_16x16x32_bf16 v[100:103], v[16:19], a[148:151], v[100:103]\n"
    "ds_read_b64_tr_b16 v[30:31], v9 offset:26624\n"
    "v_mfma_f32_16x16x32_bf16 v[104:107], v[16:19], a[152:155], v[104:107]\n"
    "v_mfma_f32_16x16x32_bf16 v[108:111], v[16:19], a[156:159], v[108:111]\n"
    "v_mfma_f32_16x16x32_bf16 v[128:131], v[20:23], a[144:147], v[128:131]\n"
    "ds_read_b64_tr_b16 v[16:17], v6 offset:28672\n"
    "v_mfma_f32_16x16x32_bf16 v[132:135], v[20:23], a[148:151], v[132:135]\n"
    "ds_read_b64_tr_b16 v[18:19], v6 offset:30720\n"
    "v_mfma_f32_16x16x32_bf16 v[136:139], v[20:23], a[152:155], v[136:139]\n"
    "v_mfma_f32_16x16x32_bf16 v[140:143], v[20:23], a[156:159], v[140:143]\n"
    "s_waitcnt lgkmcnt(2)\n"
    "v_mfma_f32_16x16x32_bf16 v[160:163], v[24:27], a[144:147], v[160:163]\n"
    "ds_read_b64_tr_b16 v[20:21], v7 offset:28672\n"
    "v_mfma_f32_16x16x32_bf16 v[164:167], v[24:27], a[148:151], v[164:167]\n"
    "ds_read_b64_tr_b16 v[22:23], v7 offset:30720\n"
    "v_mfma_f32_16x16x32_bf16 v[168:171], v[24:27], a[152:155], v[168:171]\n"
    "v_mfma_f32_16x16x32_bf16 v[172:175], v[24:27], a[156:159], v[172:175]\n"
    "v_mfma_f32_16x16x32_bf16 v[192:195], v[28:31], a[144:147], v[192:195]\n"
    "ds_read_b64_tr_b16 v[24:25], v8 offset:28672\n"
    "v_mfma_f32_16x16x32_bf16 v[196:199], v[28:31], a[148:151], v[196:199]\n"
    "ds_read_b64_tr_b16 v[26:27], v8 offset:30720\n"
    "v_mfma_f32_16x16x32_bf16 v[200:203], v[28:31], a[152:155], v[200:203]\n"
    "v_mfma_f32_16x16x32_bf16 v[204:207], v[28:31], a[156:159], v[204:207]\n"
    "s_waitcnt lgkmcnt(2)\n"
    "v_mfma_f32_16x16x32_bf16 v[112:115], v[16:19], a[144:147], v[112:115]\n"
    "ds_read_b64_tr_b16 v[28:29], v9 offset:28672\n"
    "v_mfma_f32_16x16x32_bf16 v[116:119], v[16:19], a[148:151], v[116:119]\n"
    "ds_read_b64_tr_b16 v[30:31], v9 offset:30720\n"
    "v_mfma_f32_16x16x32_bf16 v[120:123], v[16:19], a[152:155], v[120:123]\n"
    "v_mfma_f32_16x16x32_bf16 v[124:127], v[16:19], a[156:159], v[124:127]\n"
    "s_barrier\n"
    "v_mfma_f32_16x16x32_bf16 v[144:147], v[20:23], a[144:147], v[144:147]\n"
    "ds_read_b64_tr_b16 v[16:17], v6 offset:32768\n"
    "v_mfma_f32_16x16x32_bf16 v[148:151], v[20:23], a[148:151], v[148:151]\n"
    "ds_read_b64_tr_b16 v[18:19], v6 offset:34816\n"
    "v_mfma_f32_16x16x32_bf16 v[152:155], v[20:23], a[152:155], v[152:155]\n"
    "v_mfma_f32_16x16x32_bf16 v[156:159], v[20:23], a[156:159], v[156:159]\n"
    "s_waitcnt lgkmcnt(2)\n"
    "v_mfma_f32_16x16x32_bf16 v[176:179], v[24:27], a[144:147], v[176:179]\n"
    "ds_read_b64_tr_b16 v[20:21], v7 offset:32768\n"
    "v_mfma_f32_16x16x32_bf16 v[180:183], v[24:27], a[148:151], v[180:183]\n"
    "ds_read_b64_tr_b16 v[22:23], v7 offset:34816\n"
    "v_mfma_f32_16x16x32_bf16 v[184:187], v[24:27], a[152:155], v[184:187]\n"
    "v_mfma_f32_16x16x32_bf16 v[188:191], v[24:27], a[156:159], v[188:191]\n"
    "v_mfma_f32_16x16x32_bf16 v[208:211], v[28:31], a[144:147], v[208:211]\n"
    "ds_read_b64_tr_b16 v[24:25], v8 offset:32768\n"
    "v_mfma_f32_16x16x32_bf16 v[212:215], v[28:31], a[148:151], v[212:215]\n"
    "ds_read_b64_tr_b16 v[26:27], v8 offset:34816\n"
    "v_mfma_f32_16x16x32_bf16 v[216:219], v[28:31], a[152:155], v[216:219]\n"
    "v_mfma_f32_16x16x32_bf16 v[220:223], v[28:31], a[156:159], v[220:223]\n"
    "s_waitcnt vmcnt(21) lgkmcnt(2)\n"
    "v_mfma_f32_16x16x32_bf16 v[96:99], v[16:19], a[160:163], v[96:99]\n"
    "ds_read_b64_tr_b16 v[28:29], v9 offset:32768\n"
    "v_mfma_f32_16x16x32_bf16 v[100:103], v[16:19], a[164:167], v[100:103]\n"
    "ds_read_b64_tr_b16 v[30:31], v9 offset:34816\n"
    "v_mfma_f32_16x16x32_bf16 v[104:107], v[16:19], a[168:171], v[104:107]\n"
    "v_mfma_f32_16x16x32_bf16 v[108:111], v[16:19], a[172:175], v[108:111]\n"
    "v_mfma_f32_16x16x32_bf16 v[128:131], v[20:23], a[160:163], v[128:131]\n"
    "ds_read_b64_tr_b16 v[16:17], v6 offset:36864\n"
    "v_mfma_f32_16x16x32_bf16 v[132:135], v[20:23], a[164:167], v[132:135]\n"
    "ds_read_b64_tr_b16 v[18:19], v6 offset:38912\n"
    "v_mfma_f32_16x16x32_bf16 v[136:139], v[20:23], a[168:171], v[136:139]\n"
    "v_mfma_f32_16x16x32_bf16 v[140:143], v[20:23], a[172:175], v[140:143]\n"
    "s_waitcnt lgkmcnt(2)\n"
    "v_mfma_f32_16x16x32_bf16 v[160:163], v[24:27], a[160:163], v[160:163]\n"
    "ds_read_b64_tr_b16 v[20:21], v7 offset:36864\n"
    "v_mfma_f32_16x16x32_bf16 v[164:167], v[24:27], a[164:167], v[164:167]\n"
    "ds_read_b64_tr_b16 v[22:23], v7 offset:38912\n"
    "v_mfma_f32_16x16x32_bf16 v[168:171], v[24:27], a[168:171], v[168:171]\n"
    "v_mfma_f32_16x16x32_bf16 v[172:175], v[24:27], a[172:175], v[172:175]\n"
    "v_mfma_f32_16x16x32_bf16 v[192:195], v[28:31], a[160:163], v[192:195]\n"
    "ds_read_b64_tr_b16 v[24:25], v8 offset:36864\n"
    "v_mfma_f32_16x16x32_bf16 v[196:199], v[28:31], a[164:167], v[196:199]\n"
    "ds_read_b64_tr_b16 v[26:27], v8 offset:38912\n"
    "v_mfma_f32_16x16x32_bf16 v[200:203], v[28:31], a[168:171], v[200:203]\n"
    "v_mfma_f32_16x16x32_bf16 v[204:207], v[28:31], a[172:175], v[204:207]\n"
    "s_waitcnt lgkmcnt(2)\n"
    "v_mfma_f32_16x16x32_bf16 v[112:115], v[16:19], a[160:163], v[112:115]\n"
    "ds_read_b64_tr_b16 v[28:29], v9 offset:36864\n"
    "v_mfma_f32_16x16x32_bf16 v[116:119], v[16:19], a[164:167], v[116:119]\n"
    "ds_read_b64_tr_b16 v[30:31], v9 offset:38912\n"
    "v_mfma_f32_16x16x32_bf16 v[120:123], v[16:19], a[168:171], v[120:123]\n"
    "v_mfma_f32_16x16x32_bf16 v[124:127], v[16:19], a[172:175], v[124:127]\n"
    "v_mfma_f32_16x16x32_bf16 v[144:147], v[20:23], a[160:163], v[144:147]\n"
    "ds_read_b64_tr_b16 v[16:17], v6 offset:40960\n"
    "v_mfma_f32_16x16x32_bf16 v[148:151], v[20:23], a[164:167], v[148:151]\n"
    "ds_read_b64_tr_b16 v[18:19], v6 offset:43008\n"
    "v_mfma_f32_16x16x32_bf16 v[152:155], v[20:23], a[168:171], v[152:155]\n"
    "v_mfma_f32_16x16x32_bf16 v[156:159], v[20:23], a[172:175], v[156:159]\n"
    "s_waitcnt lgkmcnt(2)\n"
    "v_mfma_f32_16x16x32_bf16 v[176:179], v[24:27], a[160:163], v[176:179]\n"
    "ds_read_b64_tr_b16 v[20:21], v7 offset:40960\n"
    "v_mfma_f32_16x16x32_bf16 v[180:183], v[24:27], a[164:167], v[180:183]\n"
    "ds_read_b64_tr_b16 v[22:23], v7 offset:43008\n"
    "v_mfma_f32_16x16x32_bf16 v[184:187], v[24:27], a[168:171], v[184:187]\n"
    "v_mfma_f32_16x16x32_bf16 v[188:191], v[24:27], a[172:175], v[188:191]\n"
    "v_mfma_f32_16x16x32_bf16 v[208:211], v[28:31], a[160:163], v[208:211]\n"
    "ds_read_b64_tr_b16 v[24:25], v8 offset:40960\n"
    "v_mfma_f32_16x16x32_bf16 v[212:215], v[28:31], a[164:167], v[212:215]\n"
    "ds_read_b64_tr_b16 v[26:27], v8 offset:43008\n"
    "v_mfma_f32_16x16x32_bf16 v[216:219], v[28:31], a[168:171], v[216:219]\n"
    "v_mfma_f32_16x16x32_bf16 v[220:223], v[28:31], a[172:175], v[220:223]\n"
    "s_waitcnt vmcnt(17) lgkmcnt(2)\n"
    "v_mfma_f32_16x16x32_bf16 v[96:99], v[16:19], a[176:179], v[96:99]\n"
    "ds_read_b64_tr_b16 v[28:29], v9 offset:40960\n"
    "v_mfma_f32_16x16x32_bf16 v[100:103], v[16:19], a[180:183], v[100:103]\n"
    "ds_read_b64_tr_b16 v[30:31], v9 offset:43008\n"
    "v_mfma_f32_16x16x32_bf16 v[104:107], v[16:19], a[184:187], v[104:107]\n"
    "v_mfma_f32_16x16x32_bf16 v[108:111], v[16:19], a[188:191], v[108:111]\n"
    "v_mfma_f32_16x16x32_bf16 v[128:131], v[20:23], a[176:179], v[128:131]\n"
    "ds_read_b64_tr_b16 v[16:17], v6 offset:45056\n"
    "v_mfma_f32_16x16x32_bf16 v[132:135], v[20:23], a[180:183], v[132:135]\n"
    "ds_read_b64_tr_b16 v[18:19], v6 offset:47104\n"
    "v_mfma_f32_16x16x32_bf16 v[136:139], v[20:23], a[184:187], v[136:139]\n"
    "v_mfma_f32_16x16x32_bf16 v[140:143], v[20:23], a[188:191], v[140:143]\n"
    "s_waitcnt lgkmcnt(2)\n"
    "v_mfma_f32_16x16x32_bf16 v[160:163], v[24:27], a[176:179], v[160:163]\n"
    "ds_read_b64_tr_b16 v[20:21], v7 offset:45056\n"
    "v_mfma_f32_16x16x32_bf16 v[164:167], v[24:27], a[180:183], v[164:167]\n"
    "ds_read_b64_tr_b16 v[22:23], v7 offset:47104\n"
    "v_mfma_f32_16x16x32_bf16 v[168:171], v[24:27], a[184:187], v[168:171]\n"
    "v_mfma_f32_16x16x32_bf16 v[172:175], v[24:27], a[188:191], v[172:175]\n"
    "v_mfma_f32_16x16x32_bf16 v[192:195], v[28:31], a[176:179], v[192:195]\n"
    "ds_read_b64_tr_b16 v[24:25], v8 offset:45056\n"
    "v_mfma_f32_16x16x32_bf16 v[196:199], v[28:31], a[180:183], v[196:199]\n"
    "ds_read_b64_tr_b16 v[26:27], v8 offset:47104\n"
    "v_mfma_f32_16x16x32_bf16 v[200:203], v[28:31], a[184:187], v[200:203]\n"
    "v_mov_b32 v32, -1\n"
    "v_mov_b32 v33, -1\n"
    "v_mfma_f32_16x16x32_bf16 v[204:207], v[28:31], a[188:191], v[204:207]\n"
    "v_mov_b32 v34, -1\n"
    "s_waitcnt lgkmcnt(2)\n"
    "v_mfma_f32_16x16x32_bf16 v[112:115], v[16:19], a[176:179], v[112:115]\n"
    "ds_read_b64_tr_b16 v[28:29], v9 offset:45056\n"
    "v_mov_b32 v35, -1\n"
    "v_mfma_f32_16x16x32_bf16 v[116:119], v[16:19], a[180:183], v[116:119]\n"
    "ds_read_b64_tr_b16 v[30:31], v9 offset:47104\n"
    "s_add_u32 s76, s64, 16384\n"
    "v_mfma_f32_16x16x32_bf16 v[120:123], v[16:19], a[184:187], v[120:123]\n"
    "buffer_store_dwordx4 v[32:35], v0, s[44:47], s76 offen offset:1024 sc1\n"
    "v_mfma_f32_16x16x32_bf16 v[124:127], v[16:19], a[188:191], v[124:127]\n"
    "s_barrier\n"
    "v_mfma_f32_16x16x32_bf16 v[144:147], v[20:23], a[176:179], v[144:147]\n"
    "ds_read_b64_tr_b16 v[16:17], v6 offset:49152\n"
    "v_mfma_f32_16x16x32_bf16 v[148:151], v[20:23], a[180:183], v[148:151]\n"
    "ds_read_b64_tr_b16 v[18:19], v6 offset:51200\n"
    "v_mfma_f32_16x16x32_bf16 v[152:155], v[20:23], a[184:187], v[152:155]\n"
    "v_mfma_f32_16x16x32_bf16 v[156:159], v[20:23], a[188:191], v[156:159]\n"
    "s_waitcnt lgkmcnt(2)\n"
    "v_mfma_f32_16x16x32_bf16 v[176:179], v[24:27], a[176:179], v[176:179]\n"
    "ds_read_b64_tr_b16 v[20:21], v7 offset:49152\n"
    "v_mfma_f32_16x16x32_bf16 v[180:183], v[24:27], a[180:183], v[180:183]\n"
    "ds_read_b64_tr_b16 v[22:23], v7 offset:51200\n"
    "v_mfma_f32_16x16x32_bf16 v[184:187], v[24:27], a[184:187], v[184:187]\n"
    "v_mfma_f32_16x16x32_bf16 v[188:191], v[24:27], a[188:191], v[188:191]\n"
    "v_mfma_f32_16x16x32_bf16 v[208:211], v[28:31], a[176:179], v[208:211]\n"
    "ds_read_b64_tr_b16 v[24:25], v8 offset:49152\n"
    "v_mfma_f32_16x16x32_bf16 v[212:215], v[28:31], a[180:183], v[212:215]\n"
    "ds_read_b64_tr_b16 v[26:27], v8 offset:51200\n"
    "v_mfma_f32_16x16x32_bf16 v[216:219], v[28:31], a[184:187], v[216:219]\n"
    "s_add_u32 s76, s69, s88\n"
    "buffer_store_dwordx4 v[32:35], v0, s[44:47], s76 offen offset:0 sc1\n"
    "v_mfma_f32_16x16x32_bf16 v[220:223], v[28:31], a[188:191], v[220:223]\n"
    "s_waitcnt vmcnt(15) lgkmcnt(2)\n"
    "v_mfma_f32_16x16x32_bf16 v[96:99], v[16:19], a[192:195], v[96:99]\n"
    "ds_read_b64_tr_b16 v[28:29], v9 offset:49152\n"
    "v_mfma_f32_16x16x32_bf16 v[100:103], v[16:19], a[196:199], v[100:103]\n"
    "ds_read_b64_tr_b16 v[30:31], v9 offset:51200\n"
    "v_mfma_f32_16x16x32_bf16 v[104:107], v[16:19], a[200:203], v[104:107]\n"
    "v_mfma_f32_16x16x32_bf16 v[108:111], v[16:19], a[204:207], v[108:111]\n"
    "v_mfma_f32_16x16x32_bf16 v[128:131], v[20:23], a[192:195], v[128:131]\n"
    "ds_read_b64_tr_b16 v[16:17], v6 offset:53248\n"
    "v_mfma_f32_16x16x32_bf16 v[132:135], v[20:23], a[196:199], v[132:135]\n"
    "ds_read_b64_tr_b16 v[18:19], v6 offset:55296\n"
    "v_mfma_f32_16x16x32_bf16 v[136:139], v[20:23], a[200:203], v[136:139]\n"
    "v_mfma_f32_16x16x32_bf16 v[140:143], v[20:23], a[204:207], v[140:143]\n"
    "s_waitcnt lgkmcnt(2)\n"
    "v_mfma_f32_16x16x32_bf16 v[160:163], v[24:27], a[192:195], v[160:163]\n"
    "ds_read_b64_tr_b16 v[20:21], v7 offset:53248\n"
    "v_mfma_f32_16x16x32_bf16 v[164:167], v[24:27], a[196:199], v[164:167]\n"
    "ds_read_b64_tr_b16 v[22:23], v7 offset:55296\n"
    "v_mfma_f32_16x16x32_bf16 v[168:171], v[24:27], a[200:203], v[168:171]\n"
    "v_mfma_f32_16x16x32_bf16 v[172:175], v[24:27], a[204:207], v[172:175]\n"
    "v_mfma_f32_16x16x32_bf16 v[192:195], v[28:31], a[192:195], v[192:195]\n"
    "ds_read_b64_tr_b16 v[24:25], v8 offset:53248\n"
    "v_mfma_f32_16x16x32_bf16 v[196:199], v[28:31], a[196:199], v[196:199]\n"
    "ds_read_b64_tr_b16 v[26:27], v8 offset:55296\n"
    "v_mfma_f32_16x16x32_bf16 v[200:203], v[28:31], a[200:203], v[200:203]\n"
    "v_mfma_f32_16x16x32_bf16 v[204:207], v[28:31], a[204:207], v[204:207]\n"
    "s_waitcnt lgkmcnt(2)\n"
    "v_mfma_f32_16x16x32_bf16 v[112:115], v[16:19], a[192:195], v[112:115]\n"
    "ds_read_b64_tr_b16 v[28:29], v9 offset:53248\n"
    "s_add_u32 m0, s52, 0\n"
    "v_mfma_f32_16x16x32_bf16 v[116:119], v[16:19], a[196:199], v[116:119]\n"
    "ds_read_b64_tr_b16 v[30:31], v9 offset:55296\n"
    "s_add_u32 s79, s54, 1024\n"
    "v_mfma_f32_16x16x32_bf16 v[120:123], v[16:19], a[200:203], v[120:123]\n"
    "buffer_load_dwordx4 v1, s[36:39], s79 offen lds\n"
    "v_mfma_f32_16x16x32_bf16 v[124:127], v[16:19], a[204:207], v[124:127]\n"
    "buffer_load_dwordx4 v2, s[36:39], s79 offen offset:1024 lds\n"
    "v_mfma_f32_16x16x32_bf16 v[144:147], v[20:23], a[192:195], v[144:147]\n"
    "ds_read_b64_tr_b16 v[16:17], v6 offset:57344\n"
    "buffer_load_dwordx4 v3, s[36:39], s79 offen offset:2048 lds\n"
    "v_mfma_f32_16x16x32_bf16 v[148:151], v[20:23], a[196:199], v[148:151]\n"
    "ds_read_b64_tr_b16 v[18:19], v6 offset:59392\n"
    "v_mfma_f32_16x16x32_bf16 v[152:155], v[20:23], a[200:203], v[152:155]\n"
    "buffer_load_dwordx4 v4, s[36:39], s79 offen offset:3072 lds\n"
    "v_mfma_f32_16x16x32_bf16 v[156:159], v[20:23], a[204:207], v[156:159]\n"
    "s_add_u32 m0, s52, 16384\n"
    "s_waitcnt lgkmcnt(2)\n"
    "v_mfma_f32_16x16x32_bf16 v[176:179], v[24:27], a[192:195], v[176:179]\n"
    "ds_read_b64_tr_b16 v[20:21], v7 offset:57344\n"
    "s_add_u32 s79, s54, 664576\n"
    "v_mfma_f32_16x16x32_bf16 v[180:183], v[24:27], a[196:199], v[180:183]\n"
    "ds_read_b64_tr_b16 v[22:23], v7 offset:59392\n"
    "v_mfma_f32_16x16x32_bf16 v[184:187], v[24:27], a[200:203], v[184:187]\n"
    "buffer_load_dwordx4 v1, s[36:39], s79 offen lds\n"
    "v_mfma_f32_16x16x32_bf16 v[188:191], v[24:27], a[204:207], v[188:191]\n"
    "buffer_load_dwordx4 v2, s[36:39], s79 offen offset:1024 lds\n"
    "v_mfma_f32_16x16x32_bf16 v[208:211], v[28:31], a[192:195], v[208:211]\n"
    "ds_read_b64_tr_b16 v[24:25], v8 offset:57344\n"
    "v_mfma_f32_16x16x32_bf16 v[212:215], v[28:31], a[196:199], v[212:215]\n"
    "ds_read_b64_tr_b16 v[26:27], v8 offset:59392\n"
    "v_mfma_f32_16x16x32_bf16 v[216:219], v[28:31], a[200:203], v[216:219]\n"
    "v_mfma_f32_16x16x32_bf16 v[220:223], v[28:31], a[204:207], v[220:223]\n"
    "s_waitcnt vmcnt(17) lgkmcnt(2)\n"
    "v_mfma_f32_16x16x32_bf16 v[96:99], v[16:19], a[208:211], v[96:99]\n"
    "ds_read_b64_tr_b16 v[28:29], v9 offset:57344\n"
    "v_mfma_f32_16x16x32_bf16 v[100:103], v[16:19], a[212:215], v[100:103]\n"
    "ds_read_b64_tr_b16 v[30:31], v9 offset:59392\n"
    "v_mfma_f32_16x16x32_bf16 v[104:107], v[16:19], a[216:219], v[104:107]\n"
    "buffer_load_dwordx4 v3, s[36:39], s79 offen offset:2048 lds\n"
    "v_mfma_f32_16x16x32_bf16 v[108:111], v[16:19], a[220:223], v[108:111]\n"
    "v_mfma_f32_16x16x32_bf16 v[128:131], v[20:23], a[208:211], v[128:131]\n"
    "ds_read_b64_tr_b16 v[16:17], v6 offset:61440\n"
    "v_mfma_f32_16x16x32_bf16 v[132:135], v[20:23], a[212:215], v[132:135]\n"
    "ds_read_b64_tr_b16 v[18:19], v6 offset:63488\n"
    "v_mfma_f32_16x16x32_bf16 v[136:139], v[20:23], a[216:219], v[136:139]\n"
    "buffer_load_dwordx4 v4, s[36:39], s79 offen offset:3072 lds\n"
    "v_mfma_f32_16x16x32_bf16 v[140:143], v[20:23], a[220:223], v[140:143]\n"
    "s_waitcnt lgkmcnt(2)\n"
    "v_mfma_f32_16x16x32_bf16 v[160:163], v[24:27], a[208:211], v[160:163]\n"
    "ds_read_b64_tr_b16 v[20:21], v7 offset:61440\n"
    "v_mfma_f32_16x16x32_bf16 v[164:167], v[24:27], a[212:215], v[164:167]\n"
    "ds_read_b64_tr_b16 v[22:23], v7 offset:63488\n"
    "v_mfma_f32_16x16x32_bf16 v[168:171], v[24:27], a[216:219], v[168:171]\n"
    "s_add_u32 m0, s52, 32768\n"
    "s_add_u32 s79, s54, 1328128\n"
    "v_mfma_f32_16x16x32_bf16 v[172:175], v[24:27], a[220:223], v[172:175]\n"
    "buffer_load_dwordx4 v1, s[36:39], s79 offen lds\n"
    "v_mfma_f32_16x16x32_bf16 v[192:195], v[28:31], a[208:211], v[192:195]\n"
    "ds_read_b64_tr_b16 v[24:25], v8 offset:61440\n"
    "v_mfma_f32_16x16x32_bf16 v[196:199], v[28:31], a[212:215], v[196:199]\n"
    "ds_read_b64_tr_b16 v[26:27], v8 offset:63488\n"
    "v_mfma_f32_16x16x32_bf16 v[200:203], v[28:31], a[216:219], v[200:203]\n"
    "v_mfma_f32_16x16x32_bf16 v[204:207], v[28:31], a[220:223], v[204:207]\n"
    "buffer_load_dwordx4 v2, s[36:39], s79 offen offset:1024 lds\n"
    "s_waitcnt lgkmcnt(2)\n"
    "v_mfma_f32_16x16x32_bf16 v[112:115], v[16:19], a[208:211], v[112:115]\n"
    "ds_read_b64_tr_b16 v[28:29], v9 offset:61440\n"
    "v_mfma_f32_16x16x32_bf16 v[116:119], v[16:19], a[212:215], v[116:119]\n"
    "ds_read_b64_tr_b16 v[30:31], v9 offset:63488\n"
    "v_mfma_f32_16x16x32_bf16 v[120:123], v[16:19], a[216:219], v[120:123]\n"
    "v_mfma_f32_16x16x32_bf16 v[124:127], v[16:19], a[220:223], v[124:127]\n"
    "s_barrier\n"
    "v_mfma_f32_16x16x32_bf16 v[144:147], v[20:23], a[208:211], v[144:147]\n"
    "v_add_u32 v6, 0x10000, v6\n"
    "ds_read_b64_tr_b16 v[16:17], v6 offset:0\n"
    "buffer_load_dwordx4 v3, s[36:39], s79 offen offset:2048 lds\n"
    "v_mfma_f32_16x16x32_bf16 v[148:151], v[20:23], a[212:215], v[148:151]\n"
    "ds_read_b64_tr_b16 v[18:19], v6 offset:2048\n"
    "v_mfma_f32_16x16x32_bf16 v[152:155], v[20:23], a[216:219], v[152:155]\n"
    "v_mfma_f32_16x16x32_bf16 v[156:159], v[20:23], a[220:223], v[156:159]\n"
    "buffer_load_dwordx4 v4, s[36:39], s79 offen offset:3072 lds\n"
    "s_waitcnt lgkmcnt(2)\n"
    "v_mfma_f32_16x16x32_bf16 v[176:179], v[24:27], a[208:211], v[176:179]\n"
    "ds_read_b64_tr_b16 v[20:21], v6 offset:8192\n"
    "v_mfma_f32_16x16x32_bf16 v[180:183], v[24:27], a[212:215], v[180:183]\n"
    "ds_read_b64_tr_b16 v[22:23], v6 offset:10240\n"
    "v_mfma_f32_16x16x32_bf16 v[184:187], v[24:27], a[216:219], v[184:187]\n"
    "v_mfma_f32_16x16x32_bf16 v[188:191], v[24:27], a[220:223], v[188:191]\n"
    "s_add_u32 m0, s52, 49152\n"
    "s_add_u32 s79, s54, 1991680\n"
    "v_mfma_f32_16x16x32_bf16 v[208:211], v[28:31], a[208:211], v[208:211]\n"
    "ds_read_b64_tr_b16 v[24:25], v6 offset:4096\n"
    "buffer_load_dwordx4 v1, s[36:39], s79 offen lds\n"
    "v_mfma_f32_16x16x32_bf16 v[212:215], v[28:31], a[212:215], v[212:215]\n"
    "ds_read_b64_tr_b16 v[26:27], v6 offset:6144\n"
    "v_mfma_f32_16x16x32_bf16 v[216:219], v[28:31], a[216:219], v[216:219]\n"
    "v_mfma_f32_16x16x32_bf16 v[220:223], v[28:31], a[220:223], v[220:223]\n"
    "buffer_load_dwordx4 v2, s[36:39], s79 offen offset:1024 lds\n"
    "s_waitcnt vmcnt(21) lgkmcnt(2)\n"
    "v_mfma_f32_16x16x32_bf16 v[96:99], v[16:19], a[224:227], v[96:99]\n"
    "ds_read_b64_tr_b16 v[28:29], v6 offset:12288\n"
    "v_mfma_f32_16x16x32_bf16 v[100:103], v[16:19], a[228:231], v[100:103]\n"
    "ds_read_b64_tr_b16 v[30:31], v6 offset:14336\n"
    "v_mfma_f32_16x16x32_bf16 v[104:107], v[16:19], a[232:235], v[104:107]\n"
    "v_mfma_f32_16x16x32_bf16 v[108:111], v[16:19], a[236:239], v[108:111]\n"
    "buffer_load_dwordx4 v3, s[36:39], s79 offen offset:2048 lds\n"
    "s_waitcnt vmcnt(17)\n"
    "v_mfma_f32_16x16x32_bf16 v[96:99], v[20:23], a[240:243], v[96:99]\n"
    "v_add_u32 v7, 0x10000, v7\n"
    "ds_read_b64_tr_b16 v[16:17], v7 offset:0\n"
    "v_mfma_f32_16x16x32_bf16 v[100:103], v[20:23], a[244:247], v[100:103]\n"
    "ds_read_b64_tr_b16 v[18:19], v7 offset:2048\n"
    "v_mfma_f32_16x16x32_bf16 v[104:107], v[20:23], a[248:251], v[104:107]\n"
    "v_mfma_f32_16x16x32_bf16 v[108:111], v[20:23], a[252:255], v[108:111]\n"
    "buffer_load_dwordx4 v4, s[36:39], s79 offen offset:3072 lds\n"
    "s_waitcnt lgkmcnt(2)\n"
    "v_mfma_f32_16x16x32_bf16 v[112:115], v[24:27], a[224:227], v[112:115]\n"
    "ds_read_b64_tr_b16 v[20:21], v7 offset:8192\n"
    "v_mfma_f32_16x16x32_bf16 v[116:119], v[24:27], a[228:231], v[116:119]\n"
    "ds_read_b64_tr_b16 v[22:23], v7 offset:10240\n"
    "v_mfma_f32_16x16x32_bf16 v[120:123], v[24:27], a[232:235], v[120:123]\n"
    "v_mfma_f32_16x16x32_bf16 v[124:127], v[24:27], a[236:239], v[124:127]\n"
    "s_add_u32 m0, s53, 0\n"
    "s_add_u32 s79, s55, 0\n"
    "v_mfma_f32_16x16x32_bf16 v[112:115], v[28:31], a[240:243], v[112:115]\n"
    "ds_read_b64_tr_b16 v[24:25], v7 offset:4096\n"
    "buffer_load_dwordx4 v5, s[36:39], s79 offen lds\n"
    "v_mfma_f32_16x16x32_bf16 v[116:119], v[28:31], a[244:247], v[116:119]\n"
    "ds_read_b64_tr_b16 v[26:27], v7 offset:6144\n"
    "v_mul_f32 v96, v48, v96\n"
    "v_mul_f32 v97, v48, v97\n"
    "v_mul_f32 v98, v48, v98\n"
    "v_mfma_f32_16x16x32_bf16 v[120:123], v[28:31], a[248:251], v[120:123]\n"
    "v_mul_f32 v99, v48, v99\n"
    "v_exp_f32 v96, v96\n"
    "v_mul_f32 v100, v49, v100\n"
    "v_mfma_f32_16x16x32_bf16 v[124:127], v[28:31], a[252:255], v[124:127]\n"
    "v_exp_f32 v97, v97\n"
    "v_fma_f32 v96, v96, v52, v52\n"
    "s_waitcnt lgkmcnt(2)\n"
    "v_mfma_f32_16x16x32_bf16 v[128:131], v[16:19], a[224:227], v[128:131]\n"
    "ds_read_b64_tr_b16 v[28:29], v7 offset:12288\n"
    "v_exp_f32 v98, v98\n"
    "v_fma_f32 v97, v97, v52, v52\n"
    "v_mfma_f32_16x16x32_bf16 v[132:135], v[16:19], a[228:231], v[132:135]\n"
    "ds_read_b64_tr_b16 v[30:31], v7 offset:14336\n"
    "v_exp_f32 v99, v99\n"
    "v_fma_f32 v98, v98, v52, v52\n"
    "v_mfma_f32_16x16x32_bf16 v[136:139], v[16:19], a[232:235], v[136:139]\n"
    "v_fma_f32 v99, v99, v52, v52\n"
    "v_rcp_f32 v96, v96\n"
    "v_mul_f32 v101, v49, v101\n"
    "v_mfma_f32_16x16x32_bf16 v[140:143], v[16:19], a[236:239], v[140:143]\n"
    "v_rcp_f32 v97, v97\n"
    "v_rcp_f32 v98, v98\n"
    "v_mfma_f32_16x16x32_bf16 v[128:131], v[20:23], a[240:243], v[128:131]\n"
    "v_add_u32 v8, 0x10000, v8\n"
    "ds_read_b64_tr_b16 v[16:17], v8 offset:0\n"
    "v_rcp_f32 v99, v99\n"
    "v_mul_f32 v102, v49, v102\n"
    "v_mfma_f32_16x16x32_bf16 v[132:135], v[20:23], a[244:247], v[132:135]\n"
    "ds_read_b64_tr_b16 v[18:19], v8 offset:2048\n"
    "v_mul_f32 v103, v49, v103\n"
    "v_exp_f32 v100, v100\n"
    "v_mfma_f32_16x16x32_bf16 v[136:139], v[20:23], a[248:251], v[136:139]\n"
    "v_exp_f32 v101, v101\n"
    "v_exp_f32 v102, v102\n"
    "v_mfma_f32_16x16x32_bf16 v[140:143], v[20:23], a[252:255], v[140:143]\n"
    "s_add_u32 m0, s53, 4096\n"
    "v_exp_f32 v103, v103\n"
    "s_waitcnt lgkmcnt(2)\n"
    "v_mfma_f32_16x16x32_bf16 v[144:147], v[24:27], a[224:227], v[144:147]\n"
    "ds_read_b64_tr_b16 v[20:21], v8 offset:8192\n"
    "s_add_u32 s79, s55, 663552\n"
    "v_fma_f32 v100, v100, v53, v53\n"
    "v_fma_f32 v101, v101, v53, v53\n"
    "v_mfma_f32_16x16x32_bf16 v[148:151], v[24:27], a[228:231], v[148:151]\n"
    "ds_read_b64_tr_b16 v[22:23], v8 offset:10240\n"
    "v_fma_f32 v102, v102, v53, v53\n"
    "v_fma_f32 v103, v103, v53, v53\n"
    "v_mul_f32 v104, v50, v104\n"
    "v_mfma_f32_16x16x32_bf16 v[152:155], v[24:27], a[232:235], v[152:155]\n"
    "buffer_load_dwordx4 v5, s[36:39], s79 offen lds\n"
    "v_mul_f32 v105, v50, v105\n"
    "v_mfma_f32_16x16x32_bf16 v[156:159], v[24:27], a[236:239], v[156:159]\n"
    "v_rcp_f32 v100, v100\n"
    "v_rcp_f32 v101, v101\n"
    "v_mfma_f32_16x16x32_bf16 v[144:147], v[28:31], a[240:243], v[144:147]\n"
    "ds_read_b64_tr_b16 v[24:25], v8 offset:4096\n"
    "v_rcp_f32 v102, v102\n"
    "v_mul_f32 v106, v50, v106\n"
    "v_mfma_f32_16x16x32_bf16 v[148:151], v[28:31], a[244:247], v[148:151]\n"
    "ds_read_b64_tr_b16 v[26:27], v8 offset:6144\n"
    "v_rcp_f32 v103, v103\n"
    "v_mul_f32 v107, v50, v107\n"
    "v_mfma_f32_16x16x32_bf16 v[152:155], v[28:31], a[248:251], v[152:155]\n"
    "v_exp_f32 v104, v104\n"
    "v_exp_f32 v105, v105\n"
    "v_mfma_f32_16x16x32_bf16 v[156:159], v[28:31], a[252:255], v[156:159]\n"
    "v_exp_f32 v106, v106\n"
    "v_fma_f32 v104, v104, v54, v54\n"
    "s_waitcnt lgkmcnt(2)\n"
    "v_mfma_f32_16x16x32_bf16 v[160:163], v[16:19], a[224:227], v[160:163]\n"
    "ds_read_b64_tr_b16 v[28:29], v8 offset:12288\n"
    "v_exp_f32 v107, v107\n"
    "v_fma_f32 v105, v105, v54, v54\n"
    "v_mfma_f32_16x16x32_bf16 v[164:167], v[16:19], a[228:231], v[164:167]\n"
    "ds_read_b64_tr_b16 v[30:31], v8 offset:14336\n"
    "v_fma_f32 v106, v106, v54, v54\n"
    "v_fma_f32 v107, v107, v54, v54\n"
    "v_mul_f32 v108, v51, v108\n"
    "v_mfma_f32_16x16x32_bf16 v[168:171], v[16:19], a[232:235], v[168:171]\n"
    "v_rcp_f32 v104, v104\n"
    "v_rcp_f32 v105, v105\n"
    "v_mfma_f32_16x16x32_bf16 v[172:175], v[16:19], a[236:239], v[172:175]\n"
    "s_add_u32 m0, s53, 8192\n"
    "s_add_u32 s79, s55, 1327104\n"
    "v_rcp_f32 v106, v106\n"
    "v_mfma_f32_16x16x32_bf16 v[160:163], v[20:23], a[240:243], v[160:163]\n"
    "v_add_u32 v9, 0x10000, v9\n"
    "ds_read_b64_tr_b16 v[16:17], v9 offset:0\n"
    "buffer_load_dwordx4 v5, s[36:39], s79 offen lds\n"
    "v_mfma_f32_16x16x32_bf16 v[164:167], v[20:23], a[244:247], v[164:167]\n"
    "ds_read_b64_tr_b16 v[18:19], v9 offset:2048\n"
    "v_rcp_f32 v107, v107\n"
    "v_mul_f32 v109, v51, v109\n"
    "v_mfma_f32_16x16x32_bf16 v[168:171], v[20:23], a[248:251], v[168:171]\n"
    "v_mul_f32 v110, v51, v110\n"
    "v_mul_f32 v111, v51, v111\n"
    "v_exp_f32 v108, v108\n"
    "v_mfma_f32_16x16x32_bf16 v[172:175], v[20:23], a[252:255], v[172:175]\n"
    "v_exp_f32 v109, v109\n"
    "v_fma_f32 v108, v108, v55, v55\n"
    "s_waitcnt lgkmcnt(2)\n"
    "v_mfma_f32_16x16x32_bf16 v[176:179], v[24:27], a[224:227], v[176:179]\n"
    "ds_read_b64_tr_b16 v[20:21], v9 offset:8192\n"
    "v_exp_f32 v110, v110\n"
    "v_fma_f32 v109, v109, v55, v55\n"
    "v_mfma_f32_16x16x32_bf16 v[180:183], v[24:27], a[228:231], v[180:183]\n"
    "ds_read_b64_tr_b16 v[22:23], v9 offset:10240\n"
    "v_exp_f32 v111, v111\n"
    "v_fma_f32 v110, v110, v55, v55\n"
    "v_mfma_f32_16x16x32_bf16 v[184:187], v[24:27], a[232:235], v[184:187]\n"
    "v_fma_f32 v111, v111, v55, v55\n"
    "v_rcp_f32 v108, v108\n"
    "v_mul_f32 v112, v96, v112\n"
    "v_mfma_f32_16x16x32_bf16 v[188:191], v[24:27], a[236:239], v[188:191]\n"
    "v_rcp_f32 v109, v109\n"
    "v_rcp_f32 v110, v110\n"
    "v_mfma_f32_16x16x32_bf16 v[176:179], v[28:31], a[240:243], v[176:179]\n"
    "ds_read_b64_tr_b16 v[24:25], v9 offset:4096\n"
    "v_rcp_f32 v111, v111\n"
    "v_mul_f32 v113, v97, v113\n"
    "v_mfma_f32_16x16x32_bf16 v[180:183], v[28:31], a[244:247], v[180:183]\n"
    "ds_read_b64_tr_b16 v[26:27], v9 offset:6144\n"
    "v_mul_f32 v114, v98, v114\n"
    "v_mul_f32 v115, v99, v115\n"
    "v_cvt_pk_bf16_f32 v32, v112, v113\n"
    "v_mfma_f32_16x16x32_bf16 v[184:187], v[28:31], a[248:251], v[184:187]\n"
    "v_cvt_pk_bf16_f32 v33, v114, v115\n"
    "v_mul_f32 v116, v100, v116\n"
    "v_mul_f32 v117, v101, v117\n"
    "v_mul_f32 v118, v102, v118\n"
    "v_mfma_f32_16x16x32_bf16 v[188:191], v[28:31], a[252:255], v[188:191]\n"
    "s_waitcnt vmcnt(19)\n"
    "v_mul_f32 v119, v103, v119\n"
    "v_cvt_pk_bf16_f32 v36, v116, v117\n"
    "s_waitcnt lgkmcnt(2)\n"
    "v_mfma_f32_16x16x32_bf16 v[192:195], v[16:19], a[224:227], v[192:195]\n"
    "ds_read_b64_tr_b16 v[28:29], v9 offset:12288\n"
    "buffer_store_dword v61, v245, s[44:47], 0 offen offset:256 sc1\n"
    "v_cvt_pk_bf16_f32 v37, v118, v119\n"
    "v_mfma_f32_16x16x32_bf16 v[196:199], v[16:19], a[228:231], v[196:199]\n"
    "ds_read_b64_tr_b16 v[30:31], v9 offset:14336\n"
    "s_add_u32 m0, s53, 12288\n"
    "v_mul_f32 v120, v104, v120\n"
    "v_mul_f32 v121, v105, v121\n"
    "v_mfma_f32_16x16x32_bf16 v[200:203], v[16:19], a[232:235], v[200:203]\n"
    "s_add_u32 s79, s55, 1990656\n"
    "buffer_load_dwordx4 v5, s[36:39], s79 offen lds\n"
    "v_mfma_f32_16x16x32_bf16 v[204:207], v[16:19], a[236:239], v[204:207]\n"
    "v_mul_f32 v122, v106, v122\n"
    "v_mul_f32 v123, v107, v123\n"
    "s_waitcnt vmcnt(5)\n"
    "s_barrier\n"
    "v_mfma_f32_16x16x32_bf16 v[192:195], v[20:23], a[240:243], v[192:195]\n"
    "v_add_u32 v6, 0xfffe0000, v6\n"
    "ds_read_b64_tr_b16 v[16:17], v6 offset:0\n"
    "v_cvt_pk_bf16_f32 v40, v120, v121\n"
    "v_cvt_pk_bf16_f32 v41, v122, v123\n"
    "v_mul_f32 v124, v108, v124\n"
    "v_mfma_f32_16x16x32_bf16 v[196:199], v[20:23], a[244:247], v[196:199]\n"
    "ds_read_b64_tr_b16 v[18:19], v6 offset:2048\n"
    "v_mul_f32 v125, v109, v125\n"
    "v_mul_f32 v126, v110, v126\n"
    "v_mul_f32 v127, v111, v127\n"
    "v_mfma_f32_16x16x32_bf16 v[200:203], v[20:23], a[248:251], v[200:203]\n"
    "v_cvt_pk_bf16_f32 v44, v124, v125\n"
    "v_cvt_pk_bf16_f32 v45, v126, v127\n"
    "v_mul_f32 v128, v48, v128\n"
    "v_mul_f32 v129, v48, v129\n"
    "v_mfma_f32_16x16x32_bf16 v[204:207], v[20:23], a[252:255], v[204:207]\n"
    "v_mul_f32 v130, v48, v130\n"
    "v_mul_f32 v131, v48, v131\n"
    "s_waitcnt lgkmcnt(2)\n"
    "v_mfma_f32_16x16x32_bf16 v[208:211], v[24:27], a[224:227], v[208:211]\n"
    "ds_read_b64_tr_b16 v[20:21], v6 offset:8192\n"
    "v_exp_f32 v128, v128\n"
    "v_mul_f32 v132, v49, v132\n"
    "v_mfma_f32_16x16x32_bf16 v[212:215], v[24:27], a[228:231], v[212:215]\n"
    "ds_read_b64_tr_b16 v[22:23], v6 offset:10240\n"
    "v_exp_f32 v129, v129\n"
    "v_fma_f32 v128, v128, v52, v52\n"
    "v_mfma_f32_16x16x32_bf16 v[216:219], v[24:27], a[232:235], v[216:219]\n"
    "v_exp_f32 v130, v130\n"
    "v_exp_f32 v131, v131\n"
    "v_mfma_f32_16x16x32_bf16 v[220:223], v[24:27], a[236:239], v[220:223]\n"
    "v_fma_f32 v129, v129, v52, v52\n"
    "v_fma_f32 v130, v130, v52, v52\n"
    "v_fma_f32 v131, v131, v52, v52\n"
    "v_mul_f32 v133, v49, v133\n"
    "v_mfma_f32_16x16x32_bf16 v[208:211], v[28:31], a[240:243], v[208:211]\n"
    "ds_read_b64_tr_b16 v[24:25], v6 offset:16384\n"
    "s_add_u32 m0, s52, 65536\n"
    "v_rcp_f32 v128, v128\n"
    "v_mfma_f32_16x16x32_bf16 v[212:215], v[28:31], a[244:247], v[212:215]\n"
    "ds_read_b64_tr_b16 v[26:27], v6 offset:18432\n"
    "s_add_u32 s79, s54, 2655232\n"
    "v_rcp_f32 v129, v129\n"
    "v_mfma_f32_16x16x32_bf16 v[216:219], v[28:31], a[248:251], v[216:219]\n"
    "buffer_load_dwordx4 v1, s[36:39], s79 offen lds\n"
    "v_mul_f32 v134, v49, v134\n"
    "v_mfma_f32_16x16x32_bf16 v[220:223], v[28:31], a[252:255], v[220:223]\n"
    "v_rcp_f32 v130, v130\n"
    "v_mul_f32 v135, v49, v135\n"
    "s_waitcnt lgkmcnt(2)\n"
    "v_mfma_f32_16x16x32_bf16 v[224:227], v[16:19], a[0:3], 0\n"
    "ds_read_b64_tr_b16 v[28:29], v6 offset:24576\n"
    "v_rcp_f32 v131, v131\n"
    "v_mfma_f32_16x16x32_bf16 v[228:231], v[16:19], a[4:7], 0\n"
    "ds_read_b64_tr_b16 v[30:31], v6 offset:26624\n"
    "v_exp_f32 v132, v132\n"
    "v_mul_f32 v136, v50, v136\n"
    "v_mfma_f32_16x16x32_bf16 v[232:235], v[16:19], a[8:11], 0\n"
    "buffer_load_dwordx4 v2, s[36:39], s79 offen offset:1024 lds\n"
    "v_mfma_f32_16x16x32_bf16 v[236:239], v[16:19], a[12:15], 0\n"
    "v_exp_f32 v133, v133\n"
    "v_fma_f32 v132, v132, v53, v53\n"
    "v_mfma_f32_16x16x32_bf16 v[224:227], v[20:23], a[16:19], v[224:227]\n"
    "ds_read_b64_tr_b16 v[16:17], v6 offset:32768\n"
    "v_exp_f32 v134, v134\n"
    "v_mfma_f32_16x16x32_bf16 v[228:231], v[20:23], a[20:23], v[228:231]\n"
    "ds_read_b64_tr_b16 v[18:19], v6 offset:34816\n"
    "v_exp_f32 v135, v135\n"
    "v_mfma_f32_16x16x32_bf16 v[232:235], v[20:23], a[24:27], v[232:235]\n"
    "buffer_load_dwordx4 v3, s[36:39], s79 offen offset:2048 lds\n"
    "v_mfma_f32_16x16x32_bf16 v[236:239], v[20:23], a[28:31], v[236:239]\n"
    "v_fma_f32 v133, v133, v53, v53\n"
    "v_fma_f32 v134, v134, v53, v53\n"
    "s_waitcnt lgkmcnt(2)\n"
    "v_mfma_f32_16x16x32_bf16 v[224:227], v[24:27], a[32:35], v[224:227]\n"
    "ds_read_b64_tr_b16 v[20:21], v6 offset:40960\n"
    "v_fma_f32 v135, v135, v53, v53\n"
    "v_mul_f32 v137, v50, v137\n"
    "v_mfma_f32_16x16x32_bf16 v[228:231], v[24:27], a[36:39], v[228:231]\n"
    "ds_read_b64_tr_b16 v[22:23], v6 offset:43008\n"
    "v_rcp_f32 v132, v132\n"
    "v_mfma_f32_16x16x32_bf16 v[232:235], v[24:27], a[40:43], v[232:235]\n"
    "buffer_load_dwordx4 v4, s[36:39], s79 offen offset:3072 lds\n"
    "v_mfma_f32_16x16x32_bf16 v[236:239], v[24:27], a[44:47], v[236:239]\n"
    "v_rcp_f32 v133, v133\n"
    "v_mul_f32 v138, v50, v138\n"
    "v_mfma_f32_16x16x32_bf16 v[224:227], v[28:31], a[48:51], v[224:227]\n"
    "ds_read_b64_tr_b16 v[24:25], v6 offset:49152\n"
    "s_add_u32 m0, s52, 81920\n"
    "v_mul_f32 v139, v50, v139\n"
    "v_mfma_f32_16x16x32_bf16 v[228:231], v[28:31], a[52:55], v[228:231]\n"
    "ds_read_b64_tr_b16 v[26:27], v6 offset:51200\n"
    "s_add_u32 s79, s54, 3318784\n"
    "v_mfma_f32_16x16x32_bf16 v[232:235], v[28:31], a[56:59], v[232:235]\n"
    "buffer_load_dwordx4 v1, s[36:39], s79 offen lds\n"
    "v_mfma_f32_16x16x32_bf16 v[236:239], v[28:31], a[60:63], v[236:239]\n"
    "v_rcp_f32 v134, v134\n"
    "s_waitcnt lgkmcnt(2)\n"
    "v_mfma_f32_16x16x32_bf16 v[224:227], v[16:19], a[64:67], v[224:227]\n"
    "ds_read_b64_tr_b16 v[28:29], v6 offset:57344\n"
    "v_rcp_f32 v135, v135\n"
    "v_mfma_f32_16x16x32_bf16 v[228:231], v[16:19], a[68:71], v[228:231]\n"
    "ds_read_b64_tr_b16 v[30:31], v6 offset:59392\n"
    "v_exp_f32 v136, v136\n"
    "v_mfma_f32_16x16x32_bf16 v[232:235], v[16:19], a[72:75], v[232:235]\n"
    "buffer_load_dwordx4 v2, s[36:39], s79 offen offset:1024 lds\n"
    "v_mfma_f32_16x16x32_bf16 v[236:239], v[16:19], a[76:79], v[236:239]\n"
    "v_exp_f32 v137, v137\n"
    "v_fma_f32 v136, v136, v54, v54\n"
    "v_mfma_f32_16x16x32_bf16 v[224:227], v[20:23], a[80:83], v[224:227]\n"
    "ds_read_b64_tr_b16 v[16:17], v6 offset:4096\n"
    "v_exp_f32 v138, v138\n"
    "v_mfma_f32_16x16x32_bf16 v[228:231], v[20:23], a[84:87], v[228:231]\n"
    "ds_read_b64_tr_b16 v[18:19], v6 offset:6144\n"
    "v_exp_f32 v139, v139\n"
    "v_mfma_f32_16x16x32_bf16 v[232:235], v[20:23], a[88:91], v[232:235]\n"
    "buffer_load_dwordx4 v3, s[36:39], s79 offen offset:2048 lds\n"
    "v_mfma_f32_16x16x32_bf16 v[236:239], v[20:23], a[92:95], v[236:239]\n"
    "v_fma_f32 v137, v137, v54, v54\n"
    "v_fma_f32 v138, v138, v54, v54\n"
    "s_waitcnt lgkmcnt(2)\n"
    "v_mfma_f32_16x16x32_bf16 v[224:227], v[24:27], a[96:99], v[224:227]\n"
    "ds_read_b64_tr_b16 v[20:21], v6 offset:12288\n"
    "v_fma_f32 v139, v139, v54, v54\n"
    "v_mul_f32 v140, v51, v140\n"
    "v_mfma_f32_16x16x32_bf16 v[228:231], v[24:27], a[100:103], v[228:231]\n"
    "ds_read_b64_tr_b16 v[22:23], v6 offset:14336\n"
    "v_rcp_f32 v136, v136\n"
    "v_mfma_f32_16x16x32_bf16 v[232:235], v[24:27], a[104:107], v[232:235]\n"
    "buffer_load_dwordx4 v4, s[36:39], s79 offen offset:3072 lds\n"
    "v_mfma_f32_16x16x32_bf16 v[236:239], v[24:27], a[108:111], v[236:239]\n"
    "v_rcp_f32 v137, v137\n"
    "v_mul_f32 v141, v51, v141\n"
    "v_mfma_f32_16x16x32_bf16 v[224:227], v[28:31], a[112:115], v[224:227]\n"
    "ds_read_b64_tr_b16 v[24:25], v6 offset:20480\n"
    "s_add_u32 m0, s52, 98304\n"
    "v_mul_f32 v142, v51, v142\n"
    "v_mfma_f32_16x16x32_bf16 v[228:231], v[28:31], a[116:119], v[228:231]\n"
    "ds_read_b64_tr_b16 v[26:27], v6 offset:22528\n"
    "s_add_u32 s79, s54, 3982336\n"
    "v_mul_f32 v143, v51, v143\n"
    "v_mfma_f32_16x16x32_bf16 v[232:235], v[28:31], a[120:123], v[232:235]\n"
    "buffer_load_dwordx4 v1, s[36:39], s79 offen lds\n"
    "v_mfma_f32_16x16x32_bf16 v[236:239], v[28:31], a[124:127], v[236:239]\n"
    "v_rcp_f32 v138, v138\n"
    "s_waitcnt lgkmcnt(2)\n"
    "v_mfma_f32_16x16x32_bf16 v[240:243], v[16:19], a[0:3], 0\n"
    "ds_read_b64_tr_b16 v[28:29], v6 offset:28672\n"
    "v_rcp_f32 v139, v139\n"
    "v_mfma_f32_16x16x32_bf16 v[244:247], v[16:19], a[4:7], 0\n"
    "ds_read_b64_tr_b16 v[30:31], v6 offset:30720\n"
    "v_exp_f32 v140, v140\n"
    "v_mfma_f32_16x16x32_bf16 v[248:251], v[16:19], a[8:11], 0\n"
    "buffer_load_dwordx4 v2, s[36:39], s79 offen offset:1024 lds\n"
    "v_mfma_f32_16x16x32_bf16 v[252:255], v[16:19], a[12:15], 0\n"
    "v_exp_f32 v141, v141\n"
    "v_fma_f32 v140, v140, v55, v55\n"
    "v_mfma_f32_16x16x32_bf16 v[240:243], v[20:23], a[16:19], v[240:243]\n"
    "ds_read_b64_tr_b16 v[16:17], v6 offset:36864\n"
    "v_exp_f32 v142, v142\n"
    "v_mfma_f32_16x16x32_bf16 v[244:247], v[20:23], a[20:23], v[244:247]\n"
    "ds_read_b64_tr_b16 v[18:19], v6 offset:38912\n"
    "v_exp_f32 v143, v143\n"
    "v_mfma_f32_16x16x32_bf16 v[248:251], v[20:23], a[24:27], v[248:251]\n"
    "buffer_load_dwordx4 v3, s[36:39], s79 offen offset:2048 lds\n"
    "v_mfma_f32_16x16x32_bf16 v[252:255], v[20:23], a[28:31], v[252:255]\n"
    "v_fma_f32 v141, v141, v55, v55\n"
    "v_fma_f32 v142, v142, v55, v55\n"
    "s_waitcnt lgkmcnt(2)\n"
    "v_mfma_f32_16x16x32_bf16 v[240:243], v[24:27], a[32:35], v[240:243]\n"
    "ds_read_b64_tr_b16 v[20:21], v6 offset:45056\n"
    "v_fma_f32 v143, v143, v55, v55\n"
    "v_mul_f32 v144, v128, v144\n"
    "v_mfma_f32_16x16x32_bf16 v[244:247], v[24:27], a[36:39], v[244:247]\n"
    "ds_read_b64_tr_b16 v[22:23], v6 offset:47104\n"
    "v_rcp_f32 v140, v140\n"
    "v_mfma_f32_16x16x32_bf16 v[248:251], v[24:27], a[40:43], v[248:251]\n"
    "buffer_load_dwordx4 v4, s[36:39], s79 offen offset:3072 lds\n"
    "v_mfma_f32_16x16x32_bf16 v[252:255], v[24:27], a[44:47], v[252:255]\n"
    "v_rcp_f32 v141, v141\n"
    "v_mul_f32 v145, v129, v145\n"
    "v_mfma_f32_16x16x32_bf16 v[240:243], v[28:31], a[48:51], v[240:243]\n"
    "ds_read_b64_tr_b16 v[24:25], v6 offset:53248\n"
    "s_add_u32 m0, s52, 114688\n"
    "v_mul_f32 v146, v130, v146\n"
    "v_mfma_f32_16x16x32_bf16 v[244:247], v[28:31], a[52:55], v[244:247]\n"
    "ds_read_b64_tr_b16 v[26:27], v6 offset:55296\n"
    "s_add_u32 s79, s54, 4645888\n"
    "v_mul_f32 v147, v131, v147\n"
    "v_mfma_f32_16x16x32_bf16 v[248:251], v[28:31], a[56:59], v[248:251]\n"
    "buffer_load_dwordx4 v1, s[36:39], s79 offen lds\n"
    "v_mfma_f32_16x16x32_bf16 v[252:255], v[28:31], a[60:63], v[252:255]\n"
    "v_rcp_f32 v142, v142\n"
    "s_waitcnt lgkmcnt(2)\n"
    "v_mfma_f32_16x16x32_bf16 v[240:243], v[16:19], a[64:67], v[240:243]\n"
    "ds_read_b64_tr_b16 v[28:29], v6 offset:61440\n"
    "v_rcp_f32 v143, v143\n"
    "v_mfma_f32_16x16x32_bf16 v[244:247], v[16:19], a[68:71], v[244:247]\n"
    "ds_read_b64_tr_b16 v[30:31], v6 offset:63488\n"
    "v_cvt_pk_bf16_f32 v34, v144, v145\n"
    "v_cvt_pk_bf16_f32 v35, v146, v147\n"
    "v_mfma_f32_16x16x32_bf16 v[248:251], v[16:19], a[72:75], v[248:251]\n"
    "buffer_load_dwordx4 v2, s[36:39], s79 offen offset:1024 lds\n"
    "v_mfma_f32_16x16x32_bf16 v[252:255], v[16:19], a[76:79], v[252:255]\n"
    "s_add_u32 s35, s57, 0\n"
    "s_barrier\n"
    "v_mfma_f32_16x16x32_bf16 v[240:243], v[20:23], a[80:83], v[240:243]\n"
    "v_add_u32 v7, 0xfffe0000, v7\n"
    "ds_read_b64_tr_b16 v[16:17], v7 offset:0\n"
    "buffer_store_dwordx4 v[32:35], v0, s[44:47], s35 offen offset:0 sc1\n"
    "v_mfma_f32_16x16x32_bf16 v[244:247], v[20:23], a[84:87], v[244:247]\n"
    "ds_read_b64_tr_b16 v[18:19], v7 offset:2048\n"
    "v_mul_f32 v148, v132, v148\n"
    "v_mul_f32 v149, v133, v149\n"
    "v_mfma_f32_16x16x32_bf16 v[248:251], v[20:23], a[88:91], v[248:251]\n"
    "v_mul_f32 v150, v134, v150\n"
    "v_mul_f32 v151, v135, v151\n"
    "v_cvt_pk_bf16_f32 v38, v148, v149\n"
    "v_mfma_f32_16x16x32_bf16 v[252:255], v[20:23], a[92:95], v[252:255]\n"
    "v_cvt_pk_bf16_f32 v39, v150, v151\n"
    "v_mul_f32 v152, v136, v152\n"
    "s_waitcnt lgkmcnt(2)\n"
    "v_mfma_f32_16x16x32_bf16 v[240:243], v[24:27], a[96:99], v[240:243]\n"
    "ds_read_b64_tr_b16 v[20:21], v7 offset:8192\n"
    "v_mul_f32 v153, v137, v153\n"
    "v_mul_f32 v154, v138, v154\n"
    "v_mfma_f32_16x16x32_bf16 v[244:247], v[24:27], a[100:103], v[244:247]\n"
    "ds_read_b64_tr_b16 v[22:23], v7 offset:10240\n"
    "v_mul_f32 v155, v139, v155\n"
    "v_cvt_pk_bf16_f32 v42, v152, v153\n"
    "v_mfma_f32_16x16x32_bf16 v[248:251], v[24:27], a[104:107], v[248:251]\n"
    "buffer_store_dwordx4 v[36:39], v0, s[44:47], s35 offen offset:1024 sc1\n"
    "v_cvt_pk_bf16_f32 v43, v154, v155\n"
    "v_mfma_f32_16x16x32_bf16 v[252:255], v[24:27], a[108:111], v[252:255]\n"
    "s_add_u32 s76, s57, 294912\n"
    "v_mul_f32 v156, v140, v156\n"
    "v_mul_f32 v157, v141, v157\n"
    "v_mfma_f32_16x16x32_bf16 v[240:243], v[28:31], a[112:115], v[240:243]\n"
    "ds_read_b64_tr_b16 v[24:25], v7 offset:16384\n"
    "v_mul_f32 v158, v142, v158\n"
    "v_mul_f32 v159, v143, v159\n"
    "v_mfma_f32_16x16x32_bf16 v[244:247], v[28:31], a[116:119], v[244:247]\n"
    "ds_read_b64_tr_b16 v[26:27], v7 offset:18432\n"
    "v_cvt_pk_bf16_f32 v46, v156, v157\n"
    "v_cvt_pk_bf16_f32 v47, v158, v159\n"
    "v_mfma_f32_16x16x32_bf16 v[248:251], v[28:31], a[120:123], v[248:251]\n"
    "v_mul_f32 v160, v48, v160\n"
    "v_mul_f32 v161, v48, v161\n"
    "v_mul_f32 v162, v48, v162\n"
    "v_mfma_f32_16x16x32_bf16 v[252:255], v[28:31], a[124:127], v[252:255]\n"
    "v_mul_f32 v163, v48, v163\n"
    "s_waitcnt lgkmcnt(2)\n"
    "v_mfma_f32_16x16x32_bf16 v[96:99], v[16:19], a[0:3], 0\n"
    "ds_read_b64_tr_b16 v[28:29], v7 offset:24576\n"
    "buffer_store_dwordx4 v[40:43], v0, s[44:47], s76 offen offset:0 sc1\n"
    "v_mfma_f32_16x16x32_bf16 v[100:103], v[16:19], a[4:7], 0\n"
    "ds_read_b64_tr_b16 v[30:31], v7 offset:26624\n"
    "v_exp_f32 v160, v160\n"
    "v_mfma_f32_16x16x32_bf16 v[104:107], v[16:19], a[8:11], 0\n"
    "v_exp_f32 v161, v161\n"
    "v_fma_f32 v160, v160, v52, v52\n"
    "v_mfma_f32_16x16x32_bf16 v[108:111], v[16:19], a[12:15], 0\n"
    "v_exp_f32 v162, v162\n"
    "v_fma_f32 v161, v161, v52, v52\n"
    "v_mfma_f32_16x16x32_bf16 v[96:99], v[20:23], a[16:19], v[96:99]\n"
    "ds_read_b64_tr_b16 v[16:17], v7 offset:32768\n"
    "v_exp_f32 v163, v163\n"
    "v_mfma_f32_16x16x32_bf16 v[100:103], v[20:23], a[20:23], v[100:103]\n"
    "ds_read_b64_tr_b16 v[18:19], v7 offset:34816\n"
    "v_fma_f32 v162, v162, v52, v52\n"
    "v_fma_f32 v163, v163, v52, v52\n"
    "v_mfma_f32_16x16x32_bf16 v[104:107], v[20:23], a[24:27], v[104:107]\n"
    "buffer_store_dwordx4 v[44:47], v0, s[44:47], s76 offen offset:1024 sc1\n"
    "v_mfma_f32_16x16x32_bf16 v[108:111], v[20:23], a[28:31], v[108:111]\n"
    "v_rcp_f32 v160, v160\n"
    "s_waitcnt lgkmcnt(2)\n"
    "v_mfma_f32_16x16x32_bf16 v[96:99], v[24:27], a[32:35], v[96:99]\n"
    "ds_read_b64_tr_b16 v[20:21], v7 offset:40960\n"
    "v_rcp_f32 v161, v161\n"
    "v_mfma_f32_16x16x32_bf16 v[100:103], v[24:27], a[36:39], v[100:103]\n"
    "ds_read_b64_tr_b16 v[22:23], v7 offset:43008\n"
    "v_rcp_f32 v162, v162\n"
    "v_mfma_f32_16x16x32_bf16 v[104:107], v[24:27], a[40:43], v[104:107]\n"
    "v_rcp_f32 v163, v163\n"
    "v_mul_f32 v164, v49, v164\n"
    "v_mfma_f32_16x16x32_bf16 v[108:111], v[24:27], a[44:47], v[108:111]\n"
    "v_mul_f32 v165, v49, v165\n"
    "v_mul_f32 v166, v49, v166\n"
    "v_mul_f32 v167, v49, v167\n"
    "v_mfma_f32_16x16x32_bf16 v[96:99], v[28:31], a[48:51], v[96:99]\n"
    "ds_read_b64_tr_b16 v[24:25], v7 offset:49152\n"
    "v_exp_f32 v164, v164\n"
    "v_mfma_f32_16x16x32_bf16 v[100:103], v[28:31], a[52:55], v[100:103]\n"
    "ds_read_b64_tr_b16 v[26:27], v7 offset:51200\n"
    "v_exp_f32 v165, v165\n"
    "v_mfma_f32_16x16x32_bf16 v[104:107], v[28:31], a[56:59], v[104:107]\n"
    "v_exp_f32 v166, v166\n"
    "v_fma_f32 v164, v164, v53, v53\n"
    "v_mfma_f32_16x16x32_bf16 v[108:111], v[28:31], a[60:63], v[108:111]\n"
    "v_exp_f32 v167, v167\n"
    "s_waitcnt lgkmcnt(2)\n"
    "v_mfma_f32_16x16x32_bf16 v[96:99], v[16:19], a[64:67], v[96:99]\n"
    "ds_read_b64_tr_b16 v[28:29], v7 offset:57344\n"
    "v_fma_f32 v165, v165, v53, v53\n"
    "v_fma_f32 v166, v166, v53, v53\n"
    "v_mfma_f32_16x16x32_bf16 v[100:103], v[16:19], a[68:71], v[100:103]\n"
    "ds_read_b64_tr_b16 v[30:31], v7 offset:59392\n"
    "v_fma_f32 v167, v167, v53, v53\n"
    "v_mul_f32 v168, v50, v168\n"
    "v_mfma_f32_16x16x32_bf16 v[104:107], v[16:19], a[72:75], v[104:107]\n"
    "v_rcp_f32 v164, v164\n"
    "v_mul_f32 v169, v50, v169\n"
    "v_mfma_f32_16x16x32_bf16 v[108:111], v[16:19], a[76:79], v[108:111]\n"
    "v_rcp_f32 v165, v165\n"
    "v_mul_f32 v170, v50, v170\n"
    "v_mfma_f32_16x16x32_bf16 v[96:99], v[20:23], a[80:83], v[96:99]\n"
    "ds_read_b64_tr_b16 v[16:17], v7 offset:4096\n"
    "s_cmp_eq_u32 s71, 0\n"
    "s_cselect_b32 s77, 0, 0x80000000\n"
    "v_mfma_f32_16x16x32_bf16 v[100:103], v[20:23], a[84:87], v[100:103]\n"
    "ds_read_b64_tr_b16 v[18:19], v7 offset:6144\n"
    "v_or_b32 v62, s77, v59\n"
    "v_mul_f32 v171, v50, v171\n"
    "v_mfma_f32_16x16x32_bf16 v[104:107], v[20:23], a[88:91], v[104:107]\n"
    "buffer_load_dword v63, v62, s[40:43], 0 offen\n"
    "v_mfma_f32_16x16x32_bf16 v[108:111], v[20:23], a[92:95], v[108:111]\n"
    "v_rcp_f32 v166, v166\n"
    "s_waitcnt lgkmcnt(2)\n"
    "v_mfma_f32_16x16x32_bf16 v[96:99], v[24:27], a[96:99], v[96:99]\n"
    "ds_read_b64_tr_b16 v[20:21], v7 offset:12288\n"
    "v_rcp_f32 v167, v167\n"
    "v_mfma_f32_16x16x32_bf16 v[100:103], v[24:27], a[100:103], v[100:103]\n"
    "ds_read_b64_tr_b16 v[22:23], v7 offset:14336\n"
    "v_exp_f32 v168, v168\n"
    "v_mfma_f32_16x16x32_bf16 v[104:107], v[24:27], a[104:107], v[104:107]\n"
    "buffer_load_dwordx4 v3, s[36:39], s79 offen offset:2048 lds\n"
    "v_mfma_f32_16x16x32_bf16 v[108:111], v[24:27], a[108:111], v[108:111]\n"
    "v_exp_f32 v169, v169\n"
    "v_fma_f32 v168, v168, v54, v54\n"
    "v_mfma_f32_16x16x32_bf16 v[96:99], v[28:31], a[112:115], v[96:99]\n"
    "ds_read_b64_tr_b16 v[24:25], v7 offset:20480\n"
    "v_exp_f32 v170, v170\n"
    "v_mfma_f32_16x16x32_bf16 v[100:103], v[28:31], a[116:119], v[100:103]\n"
    "ds_read_b64_tr_b16 v[26:27], v7 offset:22528\n"
    "v_exp_f32 v171, v171\n"
    "v_mfma_f32_16x16x32_bf16 v[104:107], v[28:31], a[120:123], v[104:107]\n"
    "buffer_load_dwordx4 v4, s[36:39], s79 offen offset:3072 lds\n"
    "v_mfma_f32_16x16x32_bf16 v[108:111], v[28:31], a[124:127], v[108:111]\n"
    "v_fma_f32 v169, v169, v54, v54\n"
    "v_fma_f32 v170, v170, v54, v54\n"
    "s_waitcnt lgkmcnt(2)\n"
    "v_mfma_f32_16x16x32_bf16 v[112:115], v[16:19], a[0:3], 0\n"
    "ds_read_b64_tr_b16 v[28:29], v7 offset:28672\n"
    "s_add_u32 m0, s52, 131072\n"
    "v_fma_f32 v171, v171, v54, v54\n"
    "v_mfma_f32_16x16x32_bf16 v[116:119], v[16:19], a[4:7], 0\n"
    "ds_read_b64_tr_b16 v[30:31], v7 offset:30720\n"
    "s_add_u32 s79, s54, 590848\n"
    "v_mul_f32 v172, v51, v172\n"
    "v_mfma_f32_16x16x32_bf16 v[120:123], v[16:19], a[8:11], 0\n"
    "s_add_u32 s79, s79, s73\n"
    "v_rcp_f32 v168, v168\n"
    "v_mfma_f32_16x16x32_bf16 v[124:127], v[16:19], a[12:15], 0\n"
    "buffer_load_dwordx4 v1, s[36:39], s79 offen lds\n"
    "v_mfma_f32_16x16x32_bf16 v[112:115], v[20:23], a[16:19], v[112:115]\n"
    "ds_read_b64_tr_b16 v[16:17], v7 offset:36864\n"
    "s_cmp_eq_u32 s71, 1\n"
    "s_cselect_b32 s77, 0, 0x80000000\n"
    "v_mfma_f32_16x16x32_bf16 v[116:119], v[20:23], a[20:23], v[116:119]\n"
    "ds_read_b64_tr_b16 v[18:19], v7 offset:38912\n"
    "v_or_b32 v62, s77, v59\n"
    "v_mul_f32 v173, v51, v173\n"
    "v_mfma_f32_16x16x32_bf16 v[120:123], v[20:23], a[24:27], v[120:123]\n"
    "buffer_load_dword v63, v62, s[40:43], 0 offen\n"
    "v_mul_f32 v174, v51, v174\n"
    "v_mfma_f32_16x16x32_bf16 v[124:127], v[20:23], a[28:31], v[124:127]\n"
    "v_rcp_f32 v169, v169\n"
    "s_waitcnt lgkmcnt(2)\n"
    "v_mfma_f32_16x16x32_bf16 v[112:115], v[24:27], a[32:35], v[112:115]\n"
    "ds_read_b64_tr_b16 v[20:21], v7 offset:45056\n"
    "s_add_u32 s79, s79, 589824\n"
    "v_mul_f32 v175, v51, v175\n"
    "v_mfma_f32_16x16x32_bf16 v[116:119], v[24:27], a[36:39], v[116:119]\n"
    "ds_read_b64_tr_b16 v[22:23], v7 offset:47104\n"
    "v_rcp_f32 v170, v170\n"
    "v_mfma_f32_16x16x32_bf16 v[120:123], v[24:27], a[40:43], v[120:123]\n"
    "buffer_load_dwordx4 v2, s[36:39], s79 offen offset:1024 lds\n"
    "v_mfma_f32_16x16x32_bf16 v[124:127], v[24:27], a[44:47], v[124:127]\n"
    "v_rcp_f32 v171, v171\n"
    "v_mfma_f32_16x16x32_bf16 v[112:115], v[28:31], a[48:51], v[112:115]\n"
    "ds_read_b64_tr_b16 v[24:25], v7 offset:53248\n"
    "s_add_u32 s79, s79, 589824\n"
    "v_mul_f32 v176, v160, v176\n"
    "v_mfma_f32_16x16x32_bf16 v[116:119], v[28:31], a[52:55], v[116:119]\n"
    "ds_read_b64_tr_b16 v[26:27], v7 offset:55296\n"
    "v_exp_f32 v172, v172\n"
    "v_mfma_f32_16x16x32_bf16 v[120:123], v[28:31], a[56:59], v[120:123]\n"
    "buffer_load_dwordx4 v3, s[36:39], s79 offen offset:2048 lds\n"
    "v_mfma_f32_16x16x32_bf16 v[124:127], v[28:31], a[60:63], v[124:127]\n"
    "v_exp_f32 v173, v173\n"
    "s_waitcnt lgkmcnt(2)\n"
    "v_mfma_f32_16x16x32_bf16 v[112:115], v[16:19], a[64:67], v[112:115]\n"
    "ds_read_b64_tr_b16 v[28:29], v7 offset:61440\n"
    "s_add_u32 s79, s79, 589824\n"
    "v_fma_f32 v172, v172, v55, v55\n"
    "v_mfma_f32_16x16x32_bf16 v[116:119], v[16:19], a[68:71], v[116:119]\n"
    "ds_read_b64_tr_b16 v[30:31], v7 offset:63488\n"
    "v_exp_f32 v174, v174\n"
    "v_mfma_f32_16x16x32_bf16 v[120:123], v[16:19], a[72:75], v[120:123]\n"
    "buffer_load_dwordx4 v4, s[36:39], s79 offen offset:3072 lds\n"
    "v_mfma_f32_16x16x32_bf16 v[124:127], v[16:19], a[76:79], v[124:127]\n"
    "v_fma_f32 v173, v173, v55, v55\n"
    "s_barrier\n"
    "v_mfma_f32_16x16x32_bf16 v[112:115], v[20:23], a[80:83], v[112:115]\n"
    "v_add_u32 v8, 0xfffe0000, v8\n"
    "ds_read_b64_tr_b16 v[16:17], v8 offset:0\n"
    "s_cmp_eq_u32 s71, 2\n"
    "s_cselect_b32 s77, 0, 0x80000000\n"
    "v_mfma_f32_16x16x32_bf16 v[116:119], v[20:23], a[84:87], v[116:119]\n"
    "ds_read_b64_tr_b16 v[18:19], v8 offset:2048\n"
    "v_or_b32 v62, s77, v59\n"
    "v_fma_f32 v174, v174, v55, v55\n"
    "v_mfma_f32_16x16x32_bf16 v[120:123], v[20:23], a[88:91], v[120:123]\n"
    "buffer_load_dword v63, v62, s[40:43], 0 offen\n"
    "v_mul_f32 v177, v161, v177\n"
    "v_mfma_f32_16x16x32_bf16 v[124:127], v[20:23], a[92:95], v[124:127]\n"
    "v_exp_f32 v175, v175\n"
    "s_waitcnt lgkmcnt(2)\n"
    "v_mfma_f32_16x16x32_bf16 v[112:115], v[24:27], a[96:99], v[112:115]\n"
    "ds_read_b64_tr_b16 v[20:21], v8 offset:8192\n"
    "v_fma_f32 v175, v175, v55, v55\n"
    "v_mul_f32 v178, v162, v178\n"
    "v_mfma_f32_16x16x32_bf16 v[116:119], v[24:27], a[100:103], v[116:119]\n"
    "ds_read_b64_tr_b16 v[22:23], v8 offset:10240\n"
    "v_rcp_f32 v172, v172\n"
    "v_mfma_f32_16x16x32_bf16 v[120:123], v[24:27], a[104:107], v[120:123]\n"
    "v_rcp_f32 v173, v173\n"
    "v_mul_f32 v179, v163, v179\n"
    "v_mfma_f32_16x16x32_bf16 v[124:127], v[24:27], a[108:111], v[124:127]\n"
    "v_rcp_f32 v174, v174\n"
    "v_cvt_pk_bf16_f32 v32, v176, v177\n"
    "v_mfma_f32_16x16x32_bf16 v[112:115], v[28:31], a[112:115], v[112:115]\n"
    "ds_read_b64_tr_b16 v[24:25], v8 offset:16384\n"
    "v_rcp_f32 v175, v175\n"
    "v_mfma_f32_16x16x32_bf16 v[116:119], v[28:31], a[116:119], v[116:119]\n"
    "ds_read_b64_tr_b16 v[26:27], v8 offset:18432\n"
    "v_cvt_pk_bf16_f32 v33, v178, v179\n"
    "v_mul_f32 v180, v164, v180\n"
    "v_mfma_f32_16x16x32_bf16 v[120:123], v[28:31], a[120:123], v[120:123]\n"
    "v_mul_f32 v181, v165, v181\n"
    "v_mul_f32 v182, v166, v182\n"
    "v_mul_f32 v183, v167, v183\n"
    "v_mfma_f32_16x16x32_bf16 v[124:127], v[28:31], a[124:127], v[124:127]\n"
    "v_cvt_pk_bf16_f32 v36, v180, v181\n"
    "v_cvt_pk_bf16_f32 v37, v182, v183\n"
    "s_waitcnt lgkmcnt(2)\n"
    "v_mfma_f32_16x16x32_bf16 v[128:131], v[16:19], a[0:3], 0\n"
    "ds_read_b64_tr_b16 v[28:29], v8 offset:24576\n"
    "v_mul_f32 v184, v168, v184\n"
    "v_mul_f32 v185, v169, v185\n"
    "v_mfma_f32_16x16x32_bf16 v[132:135], v[16:19], a[4:7], 0\n"
    "ds_read_b64_tr_b16 v[30:31], v8 offset:26624\n"
    "v_mul_f32 v186, v170, v186\n"
    "v_mul_f32 v187, v171, v187\n"
    "v_mfma_f32_16x16x32_bf16 v[136:139], v[16:19], a[8:11], 0\n"
    "v_cvt_pk_bf16_f32 v40, v184, v185\n"
    "v_cvt_pk_bf16_f32 v41, v186, v187\n"
    "v_mul_f32 v188, v172, v188\n"
    "v_mfma_f32_16x16x32_bf16 v[140:143], v[16:19], a[12:15], 0\n"
    "v_mul_f32 v189, v173, v189\n"
    "v_mul_f32 v190, v174, v190\n"
    "v_mul_f32 v191, v175, v191\n"
    "v_mfma_f32_16x16x32_bf16 v[128:131], v[20:23], a[16:19], v[128:131]\n"
    "ds_read_b64_tr_b16 v[16:17], v8 offset:32768\n"
    "s_cmp_eq_u32 s71, 3\n"
    "s_cselect_b32 s77, 0, 0x80000000\n"
    "v_mfma_f32_16x16x32_bf16 v[132:135], v[20:23], a[20:23], v[132:135]\n"
    "ds_read_b64_tr_b16 v[18:19], v8 offset:34816\n"
    "v_or_b32 v62, s77, v59\n"
    "v_cvt_pk_bf16_f32 v44, v188, v189\n"
    "v_mfma_f32_16x16x32_bf16 v[136:139], v[20:23], a[24:27], v[136:139]\n"
    "buffer_load_dword v63, v62, s[40:43], 0 offen\n"
    "v_cvt_pk_bf16_f32 v45, v190, v191\n"
    "v_mfma_f32_16x16x32_bf16 v[140:143], v[20:23], a[28:31], v[140:143]\n"
    "v_mul_f32 v192, v48, v192\n"
    "v_mul_f32 v193, v48, v193\n"
    "s_waitcnt lgkmcnt(2)\n"
    "v_mfma_f32_16x16x32_bf16 v[128:131], v[24:27], a[32:35], v[128:131]\n"
    "ds_read_b64_tr_b16 v[20:21], v8 offset:40960\n"
    "v_mul_f32 v194, v48, v194\n"
    "v_mul_f32 v195, v48, v195\n"
    "v_mfma_f32_16x16x32_bf16 v[132:135], v[24:27], a[36:39], v[132:135]\n"
    "ds_read_b64_tr_b16 v[22:23], v8 offset:43008\n"
    "v_exp_f32 v192, v192\n"
    "v_mfma_f32_16x16x32_bf16 v[136:139], v[24:27], a[40:43], v[136:139]\n"
    "v_exp_f32 v193, v193\n"
    "v_fma_f32 v192, v192, v52, v52\n"
    "v_mfma_f32_16x16x32_bf16 v[140:143], v[24:27], a[44:47], v[140:143]\n"
    "v_exp_f32 v194, v194\n"
    "v_fma_f32 v193, v193, v52, v52\n"
    "v_mfma_f32_16x16x32_bf16 v[128:131], v[28:31], a[48:51], v[128:131]\n"
    "ds_read_b64_tr_b16 v[24:25], v8 offset:49152\n"
    "v_exp_f32 v195, v195\n"
    "v_mfma_f32_16x16x32_bf16 v[132:135], v[28:31], a[52:55], v[132:135]\n"
    "ds_read_b64_tr_b16 v[26:27], v8 offset:51200\n"
    "v_fma_f32 v194, v194, v52, v52\n"
    "v_fma_f32 v195, v195, v52, v52\n"
    "v_mfma_f32_16x16x32_bf16 v[136:139], v[28:31], a[56:59], v[136:139]\n"
    "v_rcp_f32 v192, v192\n"
    "v_mul_f32 v196, v49, v196\n"
    "v_mfma_f32_16x16x32_bf16 v[140:143], v[28:31], a[60:63], v[140:143]\n"
    "v_rcp_f32 v193, v193\n"
    "s_waitcnt lgkmcnt(2)\n"
    "v_mfma_f32_16x16x32_bf16 v[128:131], v[16:19], a[64:67], v[128:131]\n"
    "ds_read_b64_tr_b16 v[28:29], v8 offset:57344\n"
    "v_rcp_f32 v194, v194\n"
    "v_mfma_f32_16x16x32_bf16 v[132:135], v[16:19], a[68:71], v[132:135]\n"
    "ds_read_b64_tr_b16 v[30:31], v8 offset:59392\n"
    "v_rcp_f32 v195, v195\n"
    "v_mfma_f32_16x16x32_bf16 v[136:139], v[16:19], a[72:75], v[136:139]\n"
    "v_mul_f32 v197, v49, v197\n"
    "v_mul_f32 v198, v49, v198\n"
    "v_mul_f32 v199, v49, v199\n"
    "v_mfma_f32_16x16x32_bf16 v[140:143], v[16:19], a[76:79], v[140:143]\n"
    "v_exp_f32 v196, v196\n"
    "v_mul_f32 v200, v50, v200\n"
    "v_mfma_f32_16x16x32_bf16 v[128:131], v[20:23], a[80:83], v[128:131]\n"
    "ds_read_b64_tr_b16 v[16:17], v8 offset:4096\n"
    "s_cmp_eq_u32 s71, 4\n"
    "s_cselect_b32 s77, 0, 0x80000000\n"
    "v_mfma_f32_16x16x32_bf16 v[132:135], v[20:23], a[84:87], v[132:135]\n"
    "ds_read_b64_tr_b16 v[18:19], v8 offset:6144\n"
    "v_or_b32 v62, s77, v59\n"
    "v_fma_f32 v196, v196, v53, v53\n"
    "v_mfma_f32_16x16x32_bf16 v[136:139], v[20:23], a[88:91], v[136:139]\n"
    "buffer_load_dword v63, v62, s[40:43], 0 offen\n"
    "v_mul_f32 v201, v50, v201\n"
    "v_mfma_f32_16x16x32_bf16 v[140:143], v[20:23], a[92:95], v[140:143]\n"
    "v_exp_f32 v197, v197\n"
    "s_waitcnt lgkmcnt(2)\n"
    "v_mfma_f32_16x16x32_bf16 v[128:131], v[24:27], a[96:99], v[128:131]\n"
    "ds_read_b64_tr_b16 v[20:21], v8 offset:12288\n"
    "v_exp_f32 v198, v198\n"
    "v_mfma_f32_16x16x32_bf16 v[132:135], v[24:27], a[100:103], v[132:135]\n"
    "ds_read_b64_tr_b16 v[22:23], v8 offset:14336\n"
    "v_exp_f32 v199, v199\n"
    "v_mfma_f32_16x16x32_bf16 v[136:139], v[24:27], a[104:107], v[136:139]\n"
    "v_fma_f32 v197, v197, v53, v53\n"
    "v_fma_f32 v198, v198, v53, v53\n"
    "v_fma_f32 v199, v199, v53, v53\n"
    "v_mfma_f32_16x16x32_bf16 v[140:143], v[24:27], a[108:111], v[140:143]\n"
    "v_rcp_f32 v196, v196\n"
    "v_mul_f32 v202, v50, v202\n"
    "v_mfma_f32_16x16x32_bf16 v[128:131], v[28:31], a[112:115], v[128:131]\n"
    "ds_read_b64_tr_b16 v[24:25], v8 offset:20480\n"
    "v_rcp_f32 v197, v197\n"
    "v_mfma_f32_16x16x32_bf16 v[132:135], v[28:31], a[116:119], v[132:135]\n"
    "ds_read_b64_tr_b16 v[26:27], v8 offset:22528\n"
    "v_rcp_f32 v198, v198\n"
    "v_mfma_f32_16x16x32_bf16 v[136:139], v[28:31], a[120:123], v[136:139]\n"
    "v_rcp_f32 v199, v199\n"
    "v_mul_f32 v203, v50, v203\n"
    "v_mfma_f32_16x16x32_bf16 v[140:143], v[28:31], a[124:127], v[140:143]\n"
    "v_exp_f32 v200, v200\n"
    "s_waitcnt lgkmcnt(2)\n"
    "v_mfma_f32_16x16x32_bf16 v[144:147], v[16:19], a[0:3], 0\n"
    "ds_read_b64_tr_b16 v[28:29], v8 offset:28672\n"
    "v_exp_f32 v201, v201\n"
    "v_mfma_f32_16x16x32_bf16 v[148:151], v[16:19], a[4:7], 0\n"
    "ds_read_b64_tr_b16 v[30:31], v8 offset:30720\n"
    "v_exp_f32 v202, v202\n"
    "v_mfma_f32_16x16x32_bf16 v[152:155], v[16:19], a[8:11], 0\n"
    "v_exp_f32 v203, v203\n"
    "v_fma_f32 v200, v200, v54, v54\n"
    "v_mfma_f32_16x16x32_bf16 v[156:159], v[16:19], a[12:15], 0\n"
    "v_fma_f32 v201, v201, v54, v54\n"
    "s_waitcnt vmcnt(11)\n"
    "s_barrier\n"
    "buffer_store_dword v61, v12, s[44:47], 0 offen offset:0 sc1\n"
    "v_mfma_f32_16x16x32_bf16 v[144:147], v[20:23], a[16:19], v[144:147]\n"
    "ds_read_b64_tr_b16 v[16:17], v8 offset:36864\n"
    "s_cmp_eq_u32 s71, 5\n"
    "s_cselect_b32 s77, 0, 0x80000000\n"
    "v_mfma_f32_16x16x32_bf16 v[148:151], v[20:23], a[20:23], v[148:151]\n"
    "ds_read_b64_tr_b16 v[18:19], v8 offset:38912\n"
    "v_or_b32 v62, s77, v59\n"
    "v_fma_f32 v202, v202, v54, v54\n"
    "v_mfma_f32_16x16x32_bf16 v[152:155], v[20:23], a[24:27], v[152:155]\n"
    "buffer_load_dword v63, v62, s[40:43], 0 offen\n"
    "v_fma_f32 v203, v203, v54, v54\n"
    "v_mfma_f32_16x16x32_bf16 v[156:159], v[20:23], a[28:31], v[156:159]\n"
    "v_rcp_f32 v200, v200\n"
    "s_waitcnt lgkmcnt(2)\n"
    "v_mfma_f32_16x16x32_bf16 v[144:147], v[24:27], a[32:35], v[144:147]\n"
    "ds_read_b64_tr_b16 v[20:21], v8 offset:45056\n"
    "v_rcp_f32 v201, v201\n"
    "v_mfma_f32_16x16x32_bf16 v[148:151], v[24:27], a[36:39], v[148:151]\n"
    "ds_read_b64_tr_b16 v[22:23], v8 offset:47104\n"
    "v_rcp_f32 v202, v202\n"
    "v_mfma_f32_16x16x32_bf16 v[152:155], v[24:27], a[40:43], v[152:155]\n"
    "v_rcp_f32 v203, v203\n"
    "v_mul_f32 v204, v51, v204\n"
    "v_mfma_f32_16x16x32_bf16 v[156:159], v[24:27], a[44:47], v[156:159]\n"
    "v_mul_f32 v205, v51, v205\n"
    "v_mul_f32 v206, v51, v206\n"
    "v_mul_f32 v207, v51, v207\n"
    "v_mfma_f32_16x16x32_bf16 v[144:147], v[28:31], a[48:51], v[144:147]\n"
    "ds_read_b64_tr_b16 v[24:25], v8 offset:53248\n"
    "v_exp_f32 v204, v204\n"
    "v_mfma_f32_16x16x32_bf16 v[148:151], v[28:31], a[52:55], v[148:151]\n"
    "ds_read_b64_tr_b16 v[26:27], v8 offset:55296\n"
    "v_exp_f32 v205, v205\n"
    "v_mfma_f32_16x16x32_bf16 v[152:155], v[28:31], a[56:59], v[152:155]\n"
    "v_exp_f32 v206, v206\n"
    "v_fma_f32 v204, v204, v55, v55\n"
    "v_mfma_f32_16x16x32_bf16 v[156:159], v[28:31], a[60:63], v[156:159]\n"
    "v_exp_f32 v207, v207\n"
    "s_waitcnt lgkmcnt(2)\n"
    "v_mfma_f32_16x16x32_bf16 v[144:147], v[16:19], a[64:67], v[144:147]\n"
    "ds_read_b64_tr_b16 v[28:29], v8 offset:61440\n"
    "v_fma_f32 v205, v205, v55, v55\n"
    "v_fma_f32 v206, v206, v55, v55\n"
    "v_mfma_f32_16x16x32_bf16 v[148:151], v[16:19], a[68:71], v[148:151]\n"
    "ds_read_b64_tr_b16 v[30:31], v8 offset:63488\n"
    "v_fma_f32 v207, v207, v55, v55\n"
    "v_mul_f32 v208, v192, v208\n"
    "v_mfma_f32_16x16x32_bf16 v[152:155], v[16:19], a[72:75], v[152:155]\n"
    "v_rcp_f32 v204, v204\n"
    "v_mul_f32 v209, v193, v209\n"
    "v_mfma_f32_16x16x32_bf16 v[156:159], v[16:19], a[76:79], v[156:159]\n"
    "s_barrier\n"
    "v_mfma_f32_16x16x32_bf16 v[144:147], v[20:23], a[80:83], v[144:147]\n"
    "v_add_u32 v9, 0xfffe0000, v9\n"
    "ds_read_b64_tr_b16 v[16:17], v9 offset:0\n"
    "s_cmp_eq_u32 s71, 6\n"
    "s_cselect_b32 s77, 0, 0x80000000\n"
    "v_mfma_f32_16x16x32_bf16 v[148:151], v[20:23], a[84:87], v[148:151]\n"
    "ds_read_b64_tr_b16 v[18:19], v9 offset:2048\n"
    "v_or_b32 v62, s77, v59\n"
    "v_mfma_f32_16x16x32_bf16 v[152:155], v[20:23], a[88:91], v[152:155]\n"
    "buffer_load_dword v63, v62, s[40:43], 0 offen\n"
    "v_mfma_f32_16x16x32_bf16 v[156:159], v[20:23], a[92:95], v[156:159]\n"
    "v_mul_f32 v210, v194, v210\n"
    "s_waitcnt lgkmcnt(2)\n"
    "v_mfma_f32_16x16x32_bf16 v[144:147], v[24:27], a[96:99], v[144:147]\n"
    "ds_read_b64_tr_b16 v[20:21], v9 offset:8192\n"
    "v_mul_f32 v211, v195, v211\n"
    "v_mfma_f32_16x16x32_bf16 v[148:151], v[24:27], a[100:103], v[148:151]\n"
    "ds_read_b64_tr_b16 v[22:23], v9 offset:10240\n"
    "v_cvt_pk_bf16_f32 v34, v208, v209\n"
    "v_mfma_f32_16x16x32_bf16 v[152:155], v[24:27], a[104:107], v[152:155]\n"
    "v_rcp_f32 v205, v205\n"
    "v_mfma_f32_16x16x32_bf16 v[156:159], v[24:27], a[108:111], v[156:159]\n"
    "v_rcp_f32 v206, v206\n"
    "v_mfma_f32_16x16x32_bf16 v[144:147], v[28:31], a[112:115], v[144:147]\n"
    "ds_read_b64_tr_b16 v[24:25], v9 offset:16384\n"
    "v_cvt_pk_bf16_f32 v35, v210, v211\n"
    "v_mfma_f32_16x16x32_bf16 v[148:151], v[28:31], a[116:119], v[148:151]\n"
    "ds_read_b64_tr_b16 v[26:27], v9 offset:18432\n"
    "s_add_u32 s35, s57, 16384\n"
    "v_mfma_f32_16x16x32_bf16 v[152:155], v[28:31], a[120:123], v[152:155]\n"
    "v_rcp_f32 v207, v207\n"
    "v_mfma_f32_16x16x32_bf16 v[156:159], v[28:31], a[124:127], v[156:159]\n"
    "v_mul_f32 v212, v196, v212\n"
    "s_waitcnt lgkmcnt(2)\n"
    "v_mfma_f32_16x16x32_bf16 v[160:163], v[16:19], a[0:3], 0\n"
    "ds_read_b64_tr_b16 v[28:29], v9 offset:24576\n"
    "v_mul_f32 v213, v197, v213\n"
    "v_mfma_f32_16x16x32_bf16 v[164:167], v[16:19], a[4:7], 0\n"
    "ds_read_b64_tr_b16 v[30:31], v9 offset:26624\n"
    "v_mul_f32 v214, v198, v214\n"
    "v_mfma_f32_16x16x32_bf16 v[168:171], v[16:19], a[8:11], 0\n"
    "buffer_store_dwordx4 v[32:35], v0, s[44:47], s35 offen offset:0 sc1\n"
    "v_mfma_f32_16x16x32_bf16 v[172:175], v[16:19], a[12:15], 0\n"
    "v_mul_f32 v215, v199, v215\n"
    "v_cvt_pk_bf16_f32 v38, v212, v213\n"
    "v_mfma_f32_16x16x32_bf16 v[160:163], v[20:23], a[16:19], v[160:163]\n"
    "ds_read_b64_tr_b16 v[16:17], v9 offset:32768\n"
    "s_cmp_eq_u32 s71, 7\n"
    "s_cselect_b32 s77, 0, 0x80000000\n"
    "v_mfma_f32_16x16x32_bf16 v[164:167], v[20:23], a[20:23], v[164:167]\n"
    "ds_read_b64_tr_b16 v[18:19], v9 offset:34816\n"
    "v_or_b32 v62, s77, v59\n"
    "v_mfma_f32_16x16x32_bf16 v[168:171], v[20:23], a[24:27], v[168:171]\n"
    "buffer_load_dword v63, v62, s[40:43], 0 offen\n"
    "v_mfma_f32_16x16x32_bf16 v[172:175], v[20:23], a[28:31], v[172:175]\n"
    "v_cvt_pk_bf16_f32 v39, v214, v215\n"
    "s_waitcnt lgkmcnt(2)\n"
    "v_mfma_f32_16x16x32_bf16 v[160:163], v[24:27], a[32:35], v[160:163]\n"
    "ds_read_b64_tr_b16 v[20:21], v9 offset:40960\n"
    "v_mul_f32 v216, v200, v216\n"
    "v_mfma_f32_16x16x32_bf16 v[164:167], v[24:27], a[36:39], v[164:167]\n"
    "ds_read_b64_tr_b16 v[22:23], v9 offset:43008\n"
    "v_mul_f32 v217, v201, v217\n"
    "v_mfma_f32_16x16x32_bf16 v[168:171], v[24:27], a[40:43], v[168:171]\n"
    "buffer_store_dwordx4 v[36:39], v0, s[44:47], s35 offen offset:1024 sc1\n"
    "v_mfma_f32_16x16x32_bf16 v[172:175], v[24:27], a[44:47], v[172:175]\n"
    "v_mul_f32 v218, v202, v218\n"
    "v_mul_f32 v219, v203, v219\n"
    "v_mfma_f32_16x16x32_bf16 v[160:163], v[28:31], a[48:51], v[160:163]\n"
    "ds_read_b64_tr_b16 v[24:25], v9 offset:49152\n"
    "v_cvt_pk_bf16_f32 v42, v216, v217\n"
    "v_mfma_f32_16x16x32_bf16 v[164:167], v[28:31], a[52:55], v[164:167]\n"
    "ds_read_b64_tr_b16 v[26:27], v9 offset:51200\n"
    "v_cvt_pk_bf16_f32 v43, v218, v219\n"
    "v_mfma_f32_16x16x32_bf16 v[168:171], v[28:31], a[56:59], v[168:171]\n"
    "s_add_u32 s76, s57, 311296\n"
    "v_mul_f32 v220, v204, v220\n"
    "v_mfma_f32_16x16x32_bf16 v[172:175], v[28:31], a[60:63], v[172:175]\n"
    "v_mul_f32 v221, v205, v221\n"
    "s_waitcnt lgkmcnt(2)\n"
    "v_mfma_f32_16x16x32_bf16 v[160:163], v[16:19], a[64:67], v[160:163]\n"
    "ds_read_b64_tr_b16 v[28:29], v9 offset:57344\n"
    "v_mul_f32 v222, v206, v222\n"
    "v_mfma_f32_16x16x32_bf16 v[164:167], v[16:19], a[68:71], v[164:167]\n"
    "ds_read_b64_tr_b16 v[30:31], v9 offset:59392\n"
    "v_mul_f32 v223, v207, v223\n"
    "v_mfma_f32_16x16x32_bf16 v[168:171], v[16:19], a[72:75], v[168:171]\n"
    "buffer_store_dwordx4 v[40:43], v0, s[44:47], s76 offen offset:0 sc1\n"
    "v_mfma_f32_16x16x32_bf16 v[172:175], v[16:19], a[76:79], v[172:175]\n"
    "v_cvt_pk_bf16_f32 v46, v220, v221\n"
    "v_cvt_pk_bf16_f32 v47, v222, v223\n"
    "v_mfma_f32_16x16x32_bf16 v[160:163], v[20:23], a[80:83], v[160:163]\n"
    "ds_read_b64_tr_b16 v[16:17], v9 offset:4096\n"
    "v_mfma_f32_16x16x32_bf16 v[164:167], v[20:23], a[84:87], v[164:167]\n"
    "ds_read_b64_tr_b16 v[18:19], v9 offset:6144\n"
    "v_mfma_f32_16x16x32_bf16 v[168:171], v[20:23], a[88:91], v[168:171]\n"
    "v_mfma_f32_16x16x32_bf16 v[172:175], v[20:23], a[92:95], v[172:175]\n"
    "s_waitcnt lgkmcnt(2)\n"
    "v_mfma_f32_16x16x32_bf16 v[160:163], v[24:27], a[96:99], v[160:163]\n"
    "ds_read_b64_tr_b16 v[20:21], v9 offset:12288\n"
    "v_mfma_f32_16x16x32_bf16 v[164:167], v[24:27], a[100:103], v[164:167]\n"
    "ds_read_b64_tr_b16 v[22:23], v9 offset:14336\n"
    "v_mfma_f32_16x16x32_bf16 v[168:171], v[24:27], a[104:107], v[168:171]\n"
    "buffer_store_dwordx4 v[44:47], v0, s[44:47], s76 offen offset:1024 sc1\n"
    "v_mfma_f32_16x16x32_bf16 v[172:175], v[24:27], a[108:111], v[172:175]\n"
    "v_mfma_f32_16x16x32_bf16 v[160:163], v[28:31], a[112:115], v[160:163]\n"
    "ds_read_b64_tr_b16 v[24:25], v9 offset:20480\n"
    "v_mfma_f32_16x16x32_bf16 v[164:167], v[28:31], a[116:119], v[164:167]\n"
    "ds_read_b64_tr_b16 v[26:27], v9 offset:22528\n"
    "v_mfma_f32_16x16x32_bf16 v[168:171], v[28:31], a[120:123], v[168:171]\n"
    "v_mfma_f32_16x16x32_bf16 v[172:175], v[28:31], a[124:127], v[172:175]\n"
    "s_waitcnt lgkmcnt(2)\n"
    "v_mfma_f32_16x16x32_bf16 v[176:179], v[16:19], a[0:3], 0\n"
    "ds_read_b64_tr_b16 v[28:29], v9 offset:28672\n"
    "v_mfma_f32_16x16x32_bf16 v[180:183], v[16:19], a[4:7], 0\n"
    "ds_read_b64_tr_b16 v[30:31], v9 offset:30720\n"
    "v_mfma_f32_16x16x32_bf16 v[184:187], v[16:19], a[8:11], 0\n"
    "v_mfma_f32_16x16x32_bf16 v[188:191], v[16:19], a[12:15], 0\n"
    "v_mfma_f32_16x16x32_bf16 v[176:179], v[20:23], a[16:19], v[176:179]\n"
    "ds_read_b64_tr_b16 v[16:17], v9 offset:36864\n"
    "v_mfma_f32_16x16x32_bf16 v[180:183], v[20:23], a[20:23], v[180:183]\n"
    "ds_read_b64_tr_b16 v[18:19], v9 offset:38912\n"
    "v_mfma_f32_16x16x32_bf16 v[184:187], v[20:23], a[24:27], v[184:187]\n"
    "v_mfma_f32_16x16x32_bf16 v[188:191], v[20:23], a[28:31], v[188:191]\n"
    "s_waitcnt lgkmcnt(2)\n"
    "v_mfma_f32_16x16x32_bf16 v[176:179], v[24:27], a[32:35], v[176:179]\n"
    "ds_read_b64_tr_b16 v[20:21], v9 offset:45056\n"
    "v_mfma_f32_16x16x32_bf16 v[180:183], v[24:27], a[36:39], v[180:183]\n"
    "ds_read_b64_tr_b16 v[22:23], v9 offset:47104\n"
    "v_mfma_f32_16x16x32_bf16 v[184:187], v[24:27], a[40:43], v[184:187]\n"
    "v_mfma_f32_16x16x32_bf16 v[188:191], v[24:27], a[44:47], v[188:191]\n"
    "v_mfma_f32_16x16x32_bf16 v[176:179], v[28:31], a[48:51], v[176:179]\n"
    "ds_read_b64_tr_b16 v[24:25], v9 offset:53248\n"
    "v_mfma_f32_16x16x32_bf16 v[180:183], v[28:31], a[52:55], v[180:183]\n"
    "ds_read_b64_tr_b16 v[26:27], v9 offset:55296\n"
    "v_mfma_f32_16x16x32_bf16 v[184:187], v[28:31], a[56:59], v[184:187]\n"
    "v_mfma_f32_16x16x32_bf16 v[188:191], v[28:31], a[60:63], v[188:191]\n"
    "s_waitcnt lgkmcnt(2)\n"
    "v_mfma_f32_16x16x32_bf16 v[176:179], v[16:19], a[64:67], v[176:179]\n"
    "ds_read_b64_tr_b16 v[28:29], v9 offset:61440\n"
    "v_mfma_f32_16x16x32_bf16 v[180:183], v[16:19], a[68:71], v[180:183]\n"
    "ds_read_b64_tr_b16 v[30:31], v9 offset:63488\n"
    "v_mfma_f32_16x16x32_bf16 v[184:187], v[16:19], a[72:75], v[184:187]\n"
    "v_mfma_f32_16x16x32_bf16 v[188:191], v[16:19], a[76:79], v[188:191]\n"
    "s_barrier\n"
    "v_mfma_f32_16x16x32_bf16 v[176:179], v[20:23], a[80:83], v[176:179]\n"
    "ds_read_b64_tr_b16 v[16:17], v10 offset:0\n"
    "s_add_u32 m0, s52, 0\n"
    "v_mfma_f32_16x16x32_bf16 v[180:183], v[20:23], a[84:87], v[180:183]\n"
    "ds_read_b64_tr_b16 v[18:19], v10 offset:512\n"
    "s_add_u32 s79, s59, 0\n"
    "v_mfma_f32_16x16x32_bf16 v[184:187], v[20:23], a[88:91], v[184:187]\n"
    "v_add_u32 v2, 8192, v14\n"
    "v_add_u32 v3, 16384, v14\n"
    "v_mfma_f32_16x16x32_bf16 v[188:191], v[20:23], a[92:95], v[188:191]\n"
    "v_add_u32 v4, 24576, v14\n"
    "s_waitcnt lgkmcnt(2)\n"
    "v_mfma_f32_16x16x32_bf16 v[176:179], v[24:27], a[96:99], v[176:179]\n"
    "ds_read_b64_tr_b16 v[20:21], v10 offset:1024\n"
    "buffer_load_dwordx4 v14, s[40:43], s79 offen lds\n"
    "v_mfma_f32_16x16x32_bf16 v[180:183], v[24:27], a[100:103], v[180:183]\n"
    "ds_read_b64_tr_b16 v[22:23], v10 offset:1536\n"
    "v_mfma_f32_16x16x32_bf16 v[184:187], v[24:27], a[104:107], v[184:187]\n"
    "buffer_load_dwordx4 v2, s[40:43], s79 offen offset:1024 lds\n"
    "v_mfma_f32_16x16x32_bf16 v[188:191], v[24:27], a[108:111], v[188:191]\n"
    "buffer_load_dwordx4 v3, s[40:43], s79 offen offset:2048 lds\n"
    "v_mfma_f32_16x16x32_bf16 v[176:179], v[28:31], a[112:115], v[176:179]\n"
    "ds_read_b64_tr_b16 v[24:25], v10 offset:2048\n"
    "buffer_load_dwordx4 v4, s[40:43], s79 offen offset:3072 lds\n"
    "v_mfma_f32_16x16x32_bf16 v[180:183], v[28:31], a[116:119], v[180:183]\n"
    "ds_read_b64_tr_b16 v[26:27], v10 offset:2560\n"
    "v_mfma_f32_16x16x32_bf16 v[184:187], v[28:31], a[120:123], v[184:187]\n"
    "v_mfma_f32_16x16x32_bf16 v[188:191], v[28:31], a[124:127], v[188:191]\n"
    "s_add_u32 m0, s52, 16384\n"
    "s_waitcnt lgkmcnt(2)\n"
    "v_mfma_f32_16x16x32_bf16 v[192:195], v[16:19], a[0:3], 0\n"
    "ds_read_b64_tr_b16 v[28:29], v10 offset:3072\n"
    "s_add_u32 s79, s59, 147456\n"
    "v_mfma_f32_16x16x32_bf16 v[196:199], v[16:19], a[4:7], 0\n"
    "ds_read_b64_tr_b16 v[30:31], v10 offset:3584\n"
    "v_mfma_f32_16x16x32_bf16 v[200:203], v[16:19], a[8:11], 0\n"
    "buffer_load_dwordx4 v14, s[40:43], s79 offen lds\n"
    "v_mfma_f32_16x16x32_bf16 v[204:207], v[16:19], a[12:15], 0\n"
    "buffer_load_dwordx4 v2, s[40:43], s79 offen offset:1024 lds\n"
    "v_mfma_f32_16x16x32_bf16 v[208:211], v[20:23], a[0:3], 0\n"
    "ds_read_b64_tr_b16 v[16:17], v10 offset:4096\n"
    "v_mfma_f32_16x16x32_bf16 v[212:215], v[20:23], a[4:7], 0\n"
    "ds_read_b64_tr_b16 v[18:19], v10 offset:4608\n"
    "v_mfma_f32_16x16x32_bf16 v[216:219], v[20:23], a[8:11], 0\n"
    "v_mfma_f32_16x16x32_bf16 v[220:223], v[20:23], a[12:15], 0\n"
    "buffer_load_dwordx4 v3, s[40:43], s79 offen offset:2048 lds\n"
    "s_waitcnt lgkmcnt(2)\n"
    "v_mfma_f32_16x16x32_bf16 v[192:195], v[24:27], a[16:19], v[192:195]\n"
    "ds_read_b64_tr_b16 v[20:21], v10 offset:5120\n"
    "v_mfma_f32_16x16x32_bf16 v[196:199], v[24:27], a[20:23], v[196:199]\n"
    "ds_read_b64_tr_b16 v[22:23], v10 offset:5632\n"
    "v_mfma_f32_16x16x32_bf16 v[200:203], v[24:27], a[24:27], v[200:203]\n"
    "v_mfma_f32_16x16x32_bf16 v[204:207], v[24:27], a[28:31], v[204:207]\n"
    "buffer_load_dwordx4 v4, s[40:43], s79 offen offset:3072 lds\n"
    "v_mfma_f32_16x16x32_bf16 v[208:211], v[28:31], a[16:19], v[208:211]\n"
    "ds_read_b64_tr_b16 v[24:25], v10 offset:6144\n"
    "v_mfma_f32_16x16x32_bf16 v[212:215], v[28:31], a[20:23], v[212:215]\n"
    "ds_read_b64_tr_b16 v[26:27], v10 offset:6656\n"
    "v_mfma_f32_16x16x32_bf16 v[216:219], v[28:31], a[24:27], v[216:219]\n"
    "v_mfma_f32_16x16x32_bf16 v[220:223], v[28:31], a[28:31], v[220:223]\n"
    "s_waitcnt lgkmcnt(2)\n"
    "v_mfma_f32_16x16x32_bf16 v[192:195], v[16:19], a[32:35], v[192:195]\n"
    "ds_read_b64_tr_b16 v[28:29], v10 offset:7168\n"
    "v_mfma_f32_16x16x32_bf16 v[196:199], v[16:19], a[36:39], v[196:199]\n"
    "ds_read_b64_tr_b16 v[30:31], v10 offset:7680\n"
    "v_mfma_f32_16x16x32_bf16 v[200:203], v[16:19], a[40:43], v[200:203]\n"
    "v_mfma_f32_16x16x32_bf16 v[204:207], v[16:19], a[44:47], v[204:207]\n"
    "v_mfma_f32_16x16x32_bf16 v[208:211], v[20:23], a[32:35], v[208:211]\n"
    "ds_read_b64_tr_b16 v[16:17], v10 offset:8192\n"
    "v_mfma_f32_16x16x32_bf16 v[212:215], v[20:23], a[36:39], v[212:215]\n"
    "ds_read_b64_tr_b16 v[18:19], v10 offset:8704\n"
    "v_mfma_f32_16x16x32_bf16 v[216:219], v[20:23], a[40:43], v[216:219]\n"
    "v_mfma_f32_16x16x32_bf16 v[220:223], v[20:23], a[44:47], v[220:223]\n"
    "s_waitcnt lgkmcnt(2)\n"
    "v_mfma_f32_16x16x32_bf16 v[192:195], v[24:27], a[48:51], v[192:195]\n"
    "ds_read_b64_tr_b16 v[20:21], v10 offset:9216\n"
    "v_mfma_f32_16x16x32_bf16 v[196:199], v[24:27], a[52:55], v[196:199]\n"
    "ds_read_b64_tr_b16 v[22:23], v10 offset:9728\n"
    "v_mfma_f32_16x16x32_bf16 v[200:203], v[24:27], a[56:59], v[200:203]\n"
    "v_mfma_f32_16x16x32_bf16 v[204:207], v[24:27], a[60:63], v[204:207]\n"
    "v_mfma_f32_16x16x32_bf16 v[208:211], v[28:31], a[48:51], v[208:211]\n"
    "ds_read_b64_tr_b16 v[24:25], v10 offset:10240\n"
    "v_mfma_f32_16x16x32_bf16 v[212:215], v[28:31], a[52:55], v[212:215]\n"
    "ds_read_b64_tr_b16 v[26:27], v10 offset:10752\n"
    "v_mfma_f32_16x16x32_bf16 v[216:219], v[28:31], a[56:59], v[216:219]\n"
    "v_mfma_f32_16x16x32_bf16 v[220:223], v[28:31], a[60:63], v[220:223]\n"
    "s_waitcnt lgkmcnt(2)\n"
    "v_mfma_f32_16x16x32_bf16 v[192:195], v[16:19], a[64:67], v[192:195]\n"
    "ds_read_b64_tr_b16 v[28:29], v10 offset:11264\n"
    "v_mfma_f32_16x16x32_bf16 v[196:199], v[16:19], a[68:71], v[196:199]\n"
    "ds_read_b64_tr_b16 v[30:31], v10 offset:11776\n"
    "v_mfma_f32_16x16x32_bf16 v[200:203], v[16:19], a[72:75], v[200:203]\n"
    "v_mfma_f32_16x16x32_bf16 v[204:207], v[16:19], a[76:79], v[204:207]\n"
    "v_mfma_f32_16x16x32_bf16 v[208:211], v[20:23], a[64:67], v[208:211]\n"
    "ds_read_b64_tr_b16 v[16:17], v10 offset:12288\n"
    "v_mfma_f32_16x16x32_bf16 v[212:215], v[20:23], a[68:71], v[212:215]\n"
    "ds_read_b64_tr_b16 v[18:19], v10 offset:12800\n"
    "v_mfma_f32_16x16x32_bf16 v[216:219], v[20:23], a[72:75], v[216:219]\n"
    "v_mfma_f32_16x16x32_bf16 v[220:223], v[20:23], a[76:79], v[220:223]\n"
    "s_waitcnt lgkmcnt(2)\n"
    "v_mfma_f32_16x16x32_bf16 v[192:195], v[24:27], a[80:83], v[192:195]\n"
    "ds_read_b64_tr_b16 v[20:21], v10 offset:13312\n"
    "v_mfma_f32_16x16x32_bf16 v[196:199], v[24:27], a[84:87], v[196:199]\n"
    "ds_read_b64_tr_b16 v[22:23], v10 offset:13824\n"
    "v_mfma_f32_16x16x32_bf16 v[200:203], v[24:27], a[88:91], v[200:203]\n"
    "v_mfma_f32_16x16x32_bf16 v[204:207], v[24:27], a[92:95], v[204:207]\n"
    "v_mfma_f32_16x16x32_bf16 v[208:211], v[28:31], a[80:83], v[208:211]\n"
    "ds_read_b64_tr_b16 v[24:25], v10 offset:14336\n"
    "v_mfma_f32_16x16x32_bf16 v[212:215], v[28:31], a[84:87], v[212:215]\n"
    "ds_read_b64_tr_b16 v[26:27], v10 offset:14848\n"
    "v_mfma_f32_16x16x32_bf16 v[216:219], v[28:31], a[88:91], v[216:219]\n"
    "v_mfma_f32_16x16x32_bf16 v[220:223], v[28:31], a[92:95], v[220:223]\n"
    "s_waitcnt lgkmcnt(2)\n"
    "v_mfma_f32_16x16x32_bf16 v[192:195], v[16:19], a[96:99], v[192:195]\n"
    "ds_read_b64_tr_b16 v[28:29], v10 offset:15360\n"
    "v_mfma_f32_16x16x32_bf16 v[196:199], v[16:19], a[100:103], v[196:199]\n"
    "ds_read_b64_tr_b16 v[30:31], v10 offset:15872\n"
    "v_mfma_f32_16x16x32_bf16 v[200:203], v[16:19], a[104:107], v[200:203]\n"
    "v_mfma_f32_16x16x32_bf16 v[204:207], v[16:19], a[108:111], v[204:207]\n"
    "s_waitcnt vmcnt(19)\n"
    "s_barrier\n"
    "v_mfma_f32_16x16x32_bf16 v[208:211], v[20:23], a[96:99], v[208:211]\n"
    "v_add_u32 v6, 0x10000, v6\n"
    "ds_read_b64_tr_b16 v[16:17], v6 offset:0\n"
    "s_add_u32 m0, s53, 0\n"
    "v_mfma_f32_16x16x32_bf16 v[212:215], v[20:23], a[100:103], v[212:215]\n"
    "ds_read_b64_tr_b16 v[18:19], v6 offset:2048\n"
    "s_add_u32 s79, s55, 2654208\n"
    "v_mfma_f32_16x16x32_bf16 v[216:219], v[20:23], a[104:107], v[216:219]\n"
    "buffer_load_dwordx4 v5, s[36:39], s79 offen lds\n"
    "v_mfma_f32_16x16x32_bf16 v[220:223], v[20:23], a[108:111], v[220:223]\n"
    "s_waitcnt lgkmcnt(2)\n"
    "v_mfma_f32_16x16x32_bf16 v[192:195], v[24:27], a[112:115], v[192:195]\n"
    "ds_read_b64_tr_b16 v[20:21], v6 offset:8192\n"
    "v_mfma_f32_16x16x32_bf16 v[196:199], v[24:27], a[116:119], v[196:199]\n"
    "ds_read_b64_tr_b16 v[22:23], v6 offset:10240\n"
    "v_mfma_f32_16x16x32_bf16 v[200:203], v[24:27], a[120:123], v[200:203]\n"
    "v_mfma_f32_16x16x32_bf16 v[204:207], v[24:27], a[124:127], v[204:207]\n"
    "buffer_load_dword v60, v11, s[44:47], 0 offen offset:0 sc1\n"
    "v_mfma_f32_16x16x32_bf16 v[208:211], v[28:31], a[112:115], v[208:211]\n"
    "ds_read_b64_tr_b16 v[24:25], v6 offset:16384\n"
    "s_add_u32 m0, s53, 4096\n"
    "v_mfma_f32_16x16x32_bf16 v[212:215], v[28:31], a[116:119], v[212:215]\n"
    "ds_read_b64_tr_b16 v[26:27], v6 offset:18432\n"
    "s_add_u32 s79, s55, 3317760\n"
    "v_mfma_f32_16x16x32_bf16 v[216:219], v[28:31], a[120:123], v[216:219]\n"
    "buffer_load_dwordx4 v5, s[36:39], s79 offen lds\n"
    "v_mfma_f32_16x16x32_bf16 v[220:223], v[28:31], a[124:127], v[220:223]\n"
    "s_waitcnt lgkmcnt(2)\n"
    "v_mfma_f32_16x16x32_bf16 v[224:227], v[16:19], a[128:131], v[224:227]\n"
    "ds_read_b64_tr_b16 v[28:29], v6 offset:24576\n"
    "v_mfma_f32_16x16x32_bf16 v[228:231], v[16:19], a[132:135], v[228:231]\n"
    "ds_read_b64_tr_b16 v[30:31], v6 offset:26624\n"
    "v_mfma_f32_16x16x32_bf16 v[232:235], v[16:19], a[136:139], v[232:235]\n"
    "v_mfma_f32_16x16x32_bf16 v[236:239], v[16:19], a[140:143], v[236:239]\n"
    "v_mfma_f32_16x16x32_bf16 v[224:227], v[20:23], a[144:147], v[224:227]\n"
    "ds_read_b64_tr_b16 v[16:17], v6 offset:32768\n"
    "v_mfma_f32_16x16x32_bf16 v[228:231], v[20:23], a[148:151], v[228:231]\n"
    "ds_read_b64_tr_b16 v[18:19], v6 offset:34816\n"
    "v_mfma_f32_16x16x32_bf16 v[232:235], v[20:23], a[152:155], v[232:235]\n"
    "v_mfma_f32_16x16x32_bf16 v[236:239], v[20:23], a[156:159], v[236:239]\n"
    "s_waitcnt lgkmcnt(2)\n"
    "v_mfma_f32_16x16x32_bf16 v[224:227], v[24:27], a[160:163], v[224:227]\n"
    "ds_read_b64_tr_b16 v[20:21], v6 offset:40960\n"
    "s_add_u32 m0, s53, 8192\n"
    "v_mfma_f32_16x16x32_bf16 v[228:231], v[24:27], a[164:167], v[228:231]\n"
    "ds_read_b64_tr_b16 v[22:23], v6 offset:43008\n"
    "s_add_u32 s79, s55, 3981312\n"
    "v_mfma_f32_16x16x32_bf16 v[232:235], v[24:27], a[168:171], v[232:235]\n"
    "buffer_load_dwordx4 v5, s[36:39], s79 offen lds\n"
    "v_mfma_f32_16x16x32_bf16 v[236:239], v[24:27], a[172:175], v[236:239]\n"
    "v_mfma_f32_16x16x32_bf16 v[224:227], v[28:31], a[176:179], v[224:227]\n"
    "ds_read_b64_tr_b16 v[24:25], v6 offset:49152\n"
    "v_mfma_f32_16x16x32_bf16 v[228:231], v[28:31], a[180:183], v[228:231]\n"
    "ds_read_b64_tr_b16 v[26:27], v6 offset:51200\n"
    "v_mfma_f32_16x16x32_bf16 v[232:235], v[28:31], a[184:187], v[232:235]\n"
    "v_mfma_f32_16x16x32_bf16 v[236:239], v[28:31], a[188:191], v[236:239]\n"
    "s_waitcnt lgkmcnt(2)\n"
    "v_mfma_f32_16x16x32_bf16 v[224:227], v[16:19], a[192:195], v[224:227]\n"
    "ds_read_b64_tr_b16 v[28:29], v6 offset:57344\n"
    "v_mfma_f32_16x16x32_bf16 v[228:231], v[16:19], a[196:199], v[228:231]\n"
    "ds_read_b64_tr_b16 v[30:31], v6 offset:59392\n"
    "v_mfma_f32_16x16x32_bf16 v[232:235], v[16:19], a[200:203], v[232:235]\n"
    "v_mfma_f32_16x16x32_bf16 v[236:239], v[16:19], a[204:207], v[236:239]\n"
    "v_mfma_f32_16x16x32_bf16 v[224:227], v[20:23], a[208:211], v[224:227]\n"
    "v_add_u32 v6, 0x10000, v6\n"
    "ds_read_b64_tr_b16 v[16:17], v6 offset:0\n"
    "s_add_u32 m0, s52, 32768\n"
    "v_mfma_f32_16x16x32_bf16 v[228:231], v[20:23], a[212:215], v[228:231]\n"
    "ds_read_b64_tr_b16 v[18:19], v6 offset:2048\n"
    "s_add_u32 s79, s59, 294912\n"
    "v_mfma_f32_16x16x32_bf16 v[232:235], v[20:23], a[216:219], v[232:235]\n"
    "buffer_load_dwordx4 v14, s[40:43], s79 offen lds\n"
    "v_mfma_f32_16x16x32_bf16 v[236:239], v[20:23], a[220:223], v[236:239]\n"
    "s_waitcnt lgkmcnt(2)\n"
    "v_mfma_f32_16x16x32_bf16 v[224:227], v[24:27], a[224:227], v[224:227]\n"
    "ds_read_b64_tr_b16 v[20:21], v6 offset:8192\n"
    "v_mfma_f32_16x16x32_bf16 v[228:231], v[24:27], a[228:231], v[228:231]\n"
    "ds_read_b64_tr_b16 v[22:23], v6 offset:10240\n"
    "v_mfma_f32_16x16x32_bf16 v[232:235], v[24:27], a[232:235], v[232:235]\n"
    "buffer_load_dwordx4 v2, s[40:43], s79 offen offset:1024 lds\n"
    "v_mfma_f32_16x16x32_bf16 v[236:239], v[24:27], a[236:239], v[236:239]\n"
    "v_mfma_f32_16x16x32_bf16 v[224:227], v[28:31], a[240:243], v[224:227]\n"
    "v_add_u32 v6, 0xffff0000, v6\n"
    "ds_read_b64_tr_b16 v[24:25], v6 offset:4096\n"
    "v_mfma_f32_16x16x32_bf16 v[228:231], v[28:31], a[244:247], v[228:231]\n"
    "ds_read_b64_tr_b16 v[26:27], v6 offset:6144\n"
    "v_mfma_f32_16x16x32_bf16 v[232:235], v[28:31], a[248:251], v[232:235]\n"
    "buffer_load_dwordx4 v3, s[40:43], s79 offen offset:2048 lds\n"
    "v_mfma_f32_16x16x32_bf16 v[236:239], v[28:31], a[252:255], v[236:239]\n"
    "s_waitcnt vmcnt(15)\n"
    "s_barrier\n"
    "buffer_store_dword v61, v12, s[44:47], 0 offen offset:256 sc1\n"
    "s_waitcnt lgkmcnt(2)\n"
    "v_mfma_f32_16x16x32_bf16 v[224:227], v[16:19], v[64:67], v[224:227]\n"
    "ds_read_b64_tr_b16 v[28:29], v6 offset:12288\n"
    "v_mfma_f32_16x16x32_bf16 v[228:231], v[16:19], v[68:71], v[228:231]\n"
    "ds_read_b64_tr_b16 v[30:31], v6 offset:14336\n"
    "v_mfma_f32_16x16x32_bf16 v[232:235], v[16:19], v[72:75], v[232:235]\n"
    "buffer_load_dwordx4 v4, s[40:43], s79 offen offset:3072 lds\n"
    "v_mfma_f32_16x16x32_bf16 v[236:239], v[16:19], v[76:79], v[236:239]\n"
    "v_mfma_f32_16x16x32_bf16 v[224:227], v[20:23], v[80:83], v[224:227]\n"
    "ds_read_b64_tr_b16 v[16:17], v6 offset:20480\n"
    "s_add_u32 m0, s53, 12288\n"
    "v_mfma_f32_16x16x32_bf16 v[228:231], v[20:23], v[84:87], v[228:231]\n"
    "ds_read_b64_tr_b16 v[18:19], v6 offset:22528\n"
    "s_add_u32 s79, s55, 4644864\n"
    "v_mfma_f32_16x16x32_bf16 v[232:235], v[20:23], v[88:91], v[232:235]\n"
    "buffer_load_dwordx4 v5, s[36:39], s79 offen lds\n"
    "v_mfma_f32_16x16x32_bf16 v[236:239], v[20:23], v[92:95], v[236:239]\n"
    "s_waitcnt lgkmcnt(2)\n"
    "v_mfma_f32_16x16x32_bf16 v[240:243], v[24:27], a[128:131], v[240:243]\n"
    "ds_read_b64_tr_b16 v[20:21], v6 offset:28672\n"
    "v_mfma_f32_16x16x32_bf16 v[244:247], v[24:27], a[132:135], v[244:247]\n"
    "ds_read_b64_tr_b16 v[22:23], v6 offset:30720\n"
    "v_mfma_f32_16x16x32_bf16 v[248:251], v[24:27], a[136:139], v[248:251]\n"
    "v_mfma_f32_16x16x32_bf16 v[252:255], v[24:27], a[140:143], v[252:255]\n"
    "v_mfma_f32_16x16x32_bf16 v[240:243], v[28:31], a[144:147], v[240:243]\n"
    "ds_read_b64_tr_b16 v[24:25], v6 offset:36864\n"
    "v_mul_f32 v224, v48, v224\n"
    "v_mul_f32 v225, v48, v225\n"
    "v_mfma_f32_16x16x32_bf16 v[244:247], v[28:31], a[148:151], v[244:247]\n"
    "ds_read_b64_tr_b16 v[26:27], v6 offset:38912\n"
    "v_mul_f32 v226, v48, v226\n"
    "v_mul_f32 v227, v48, v227\n"
    "v_mfma_f32_16x16x32_bf16 v[248:251], v[28:31], a[152:155], v[248:251]\n"
    "v_exp_f32 v224, v224\n"
    "v_mul_f32 v228, v49, v228\n"
    "v_mfma_f32_16x16x32_bf16 v[252:255], v[28:31], a[156:159], v[252:255]\n"
    "v_exp_f32 v225, v225\n"
    "s_waitcnt lgkmcnt(2)\n"
    "v_mfma_f32_16x16x32_bf16 v[240:243], v[16:19], a[160:163], v[240:243]\n"
    "ds_read_b64_tr_b16 v[28:29], v6 offset:45056\n"
    "s_add_u32 m0, s52, 49152\n"
    "v_fma_f32 v224, v224, v52, v52\n"
    "v_mfma_f32_16x16x32_bf16 v[244:247], v[16:19], a[164:167], v[244:247]\n"
    "ds_read_b64_tr_b16 v[30:31], v6 offset:47104\n"
    "s_add_u32 s79, s59, 442368\n"
    "v_fma_f32 v225, v225, v52, v52\n"
    "v_mfma_f32_16x16x32_bf16 v[248:251], v[16:19], a[168:171], v[248:251]\n"
    "buffer_load_dwordx4 v14, s[40:43], s79 offen lds\n"
    "v_mfma_f32_16x16x32_bf16 v[252:255], v[16:19], a[172:175], v[252:255]\n"
    "v_mul_f32 v229, v49, v229\n"
    "s_waitcnt vmcnt(9)\n"
    "v_cmp_ne_u32 vcc, s93, v60\n"
    "s_cbranch_vccnz LS0_k\n"
    "LB0_k:\n"
    "s_barrier\n"
    "v_mfma_f32_16x16x32_bf16 v[240:243], v[20:23], a[176:179], v[240:243]\n"
    "ds_read_b64_tr_b16 v[16:17], v6 offset:53248\n"
    "s_add_u32 s33, s63, 0\n"
    "v_mul_f32 v230, v49, v230\n"
    "v_mfma_f32_16x16x32_bf16 v[244:247], v[20:23], a[180:183], v[244:247]\n"
    "ds_read_b64_tr_b16 v[18:19], v6 offset:55296\n"
    "v_exp_f32 v226, v226\n"
    "v_mfma_f32_16x16x32_bf16 v[248:251], v[20:23], a[184:187], v[248:251]\n"
    "buffer_load_dwordx4 a[0:3], v0, s[44:47], s33 offen offset:0 sc1\n"
    "v_fma_f32 v226, v226, v52, v52\n"
    "v_mfma_f32_16x16x32_bf16 v[252:255], v[20:23], a[188:191], v[252:255]\n"
    "v_exp_f32 v227, v227\n"
    "s_waitcnt lgkmcnt(2)\n"
    "v_mfma_f32_16x16x32_bf16 v[240:243], v[24:27], a[192:195], v[240:243]\n"
    "ds_read_b64_tr_b16 v[20:21], v6 offset:61440\n"
    "v_fma_f32 v227, v227, v52, v52\n"
    "v_mul_f32 v231, v49, v231\n"
    "v_mfma_f32_16x16x32_bf16 v[244:247], v[24:27], a[196:199], v[244:247]\n"
    "ds_read_b64_tr_b16 v[22:23], v6 offset:63488\n"
    "v_rcp_f32 v224, v224\n"
    "v_mfma_f32_16x16x32_bf16 v[248:251], v[24:27], a[200:203], v[248:251]\n"
    "buffer_load_dwordx4 a[4:7], v0, s[44:47], s33 offen offset:1024 sc1\n"
    "v_mfma_f32_16x16x32_bf16 v[252:255], v[24:27], a[204:207], v[252:255]\n"
    "v_rcp_f32 v225, v225\n"
    "v_mfma_f32_16x16x32_bf16 v[240:243], v[28:31], a[208:211], v[240:243]\n"
    "v_add_u32 v6, 0x10000, v6\n"
    "ds_read_b64_tr_b16 v[24:25], v6 offset:4096\n"
    "v_rcp_f32 v226, v226\n"
    "v_mfma_f32_16x16x32_bf16 v[244:247], v[28:31], a[212:215], v[244:247]\n"
    "ds_read_b64_tr_b16 v[26:27], v6 offset:6144\n"
    "v_rcp_f32 v227, v227\n"
    "v_mfma_f32_16x16x32_bf16 v[248:251], v[28:31], a[216:219], v[248:251]\n"
    "buffer_load_dwordx4 a[8:11], v0, s[44:47], s33 offen offset:2048 sc1\n"
    "v_mul_f32 v232, v50, v232\n"
    "v_mfma_f32_16x16x32_bf16 v[252:255], v[28:31], a[220:223], v[252:255]\n"
    "v_exp_f32 v228, v228\n"
    "s_waitcnt lgkmcnt(2)\n"
    "v_mfma_f32_16x16x32_bf16 v[240:243], v[16:19], a[224:227], v[240:243]\n"
    "ds_read_b64_tr_b16 v[28:29], v6 offset:12288\n"
    "v_exp_f32 v229, v229\n"
    "v_mfma_f32_16x16x32_bf16 v[244:247], v[16:19], a[228:231], v[244:247]\n"
    "ds_read_b64_tr_b16 v[30:31], v6 offset:14336\n"
    "v_exp_f32 v230, v230\n"
    "v_mfma_f32_16x16x32_bf16 v[248:251], v[16:19], a[232:235], v[248:251]\n"
    "buffer_load_dwordx4 a[12:15], v0, s[44:47], s33 offen offset:3072 sc1\n"
    "v_fma_f32 v228, v228, v53, v53\n"
    "v_mfma_f32_16x16x32_bf16 v[252:255], v[16:19], a[236:239], v[252:255]\n"
    "v_exp_f32 v231, v231\n"
    "v_fma_f32 v229, v229, v53, v53\n"
    "v_mfma_f32_16x16x32_bf16 v[240:243], v[20:23], a[240:243], v[240:243]\n"
    "ds_read_b64_tr_b16 v[16:17], v10 offset:0\n"
    "v_fma_f32 v230, v230, v53, v53\n"
    "v_fma_f32 v231, v231, v53, v53\n"
    "v_mfma_f32_16x16x32_bf16 v[244:247], v[20:23], a[244:247], v[244:247]\n"
    "ds_read_b64_tr_b16 v[18:19], v10 offset:512\n"
    "v_rcp_f32 v228, v228\n"
    "v_mfma_f32_16x16x32_bf16 v[248:251], v[20:23], a[248:251], v[248:251]\n"
    "buffer_load_dwordx4 v2, s[40:43], s79 offen offset:1024 lds\n"
    "v_mfma_f32_16x16x32_bf16 v[252:255], v[20:23], a[252:255], v[252:255]\n"
    "v_rcp_f32 v229, v229\n"
    "s_waitcnt lgkmcnt(2)\n"
    "v_mfma_f32_16x16x32_bf16 v[240:243], v[24:27], v[64:67], v[240:243]\n"
    "ds_read_b64_tr_b16 v[20:21], v10 offset:1024\n"
    "v_rcp_f32 v230, v230\n"
    "v_mfma_f32_16x16x32_bf16 v[244:247], v[24:27], v[68:71], v[244:247]\n"
    "ds_read_b64_tr_b16 v[22:23], v10 offset:1536\n"
    "v_rcp_f32 v231, v231\n"
    "v_mfma_f32_16x16x32_bf16 v[248:251], v[24:27], v[72:75], v[248:251]\n"
    "buffer_load_dwordx4 v3, s[40:43], s79 offen offset:2048 lds\n"
    "v_mfma_f32_16x16x32_bf16 v[252:255], v[24:27], v[76:79], v[252:255]\n"
    "v_mul_f32 v233, v50, v233\n"
    "v_mul_f32 v234, v50, v234\n"
    "v_mul_f32 v235, v50, v235\n"
    "v_mfma_f32_16x16x32_bf16 v[240:243], v[28:31], v[80:83], v[240:243]\n"
    "ds_read_b64_tr_b16 v[24:25], v10 offset:2048\n"
    "v_exp_f32 v232, v232\n"
    "v_mfma_f32_16x16x32_bf16 v[244:247], v[28:31], v[84:87], v[244:247]\n"
    "ds_read_b64_tr_b16 v[26:27], v10 offset:2560\n"
    "v_exp_f32 v233, v233\n"
    "v_mfma_f32_16x16x32_bf16 v[248:251], v[28:31], v[88:91], v[248:251]\n"
    "buffer_load_dwordx4 v4, s[40:43], s79 offen offset:3072 lds\n"
    "v_mfma_f32_16x16x32_bf16 v[252:255], v[28:31], v[92:95], v[252:255]\n"
    "v_exp_f32 v234, v234\n"
    "s_waitcnt lgkmcnt(2)\n"
    "v_mfma_f32_16x16x32_bf16 v[192:195], v[16:19], a[128:131], v[192:195]\n"
    "ds_read_b64_tr_b16 v[28:29], v10 offset:3072\n"
    "s_add_u32 s33, s63, 4096\n"
    "v_fma_f32 v232, v232, v54, v54\n"
    "v_mfma_f32_16x16x32_bf16 v[196:199], v[16:19], a[132:135], v[196:199]\n"
    "ds_read_b64_tr_b16 v[30:31], v10 offset:3584\n"
    "v_exp_f32 v235, v235\n"
    "v_mfma_f32_16x16x32_bf16 v[200:203], v[16:19], a[136:139], v[200:203]\n"
    "buffer_load_dwordx4 a[16:19], v0, s[44:47], s33 offen offset:0 sc1\n"
    "v_fma_f32 v233, v233, v54, v54\n"
    "v_mfma_f32_16x16x32_bf16 v[204:207], v[16:19], a[140:143], v[204:207]\n"
    "v_fma_f32 v234, v234, v54, v54\n"
    "v_fma_f32 v235, v235, v54, v54\n"
    "v_mul_f32 v236, v51, v236\n"
    "v_mfma_f32_16x16x32_bf16 v[208:211], v[20:23], a[128:131], v[208:211]\n"
    "v_add_u32 v7, 0x10000, v7\n"
    "ds_read_b64_tr_b16 v[16:17], v7 offset:0\n"
    "v_rcp_f32 v232, v232\n"
    "v_mfma_f32_16x16x32_bf16 v[212:215], v[20:23], a[132:135], v[212:215]\n"
    "ds_read_b64_tr_b16 v[18:19], v7 offset:2048\n"
    "v_rcp_f32 v233, v233\n"
    "v_mfma_f32_16x16x32_bf16 v[216:219], v[20:23], a[136:139], v[216:219]\n"
    "buffer_load_dwordx4 a[20:23], v0, s[44:47], s33 offen offset:1024 sc1\n"
    "v_mul_f32 v237, v51, v237\n"
    "v_mfma_f32_16x16x32_bf16 v[220:223], v[20:23], a[140:143], v[220:223]\n"
    "v_rcp_f32 v234, v234\n"
    "s_waitcnt lgkmcnt(2)\n"
    "v_mfma_f32_16x16x32_bf16 v[192:195], v[24:27], a[144:147], v[192:195]\n"
    "ds_read_b64_tr_b16 v[20:21], v7 offset:8192\n"
    "v_rcp_f32 v235, v235\n"
    "v_mfma_f32_16x16x32_bf16 v[196:199], v[24:27], a[148:151], v[196:199]\n"
    "ds_read_b64_tr_b16 v[22:23], v7 offset:10240\n"
    "v_mul_f32 v238, v51, v238\n"
    "v_mul_f32 v239, v51, v239\n"
    "v_mfma_f32_16x16x32_bf16 v[200:203], v[24:27], a[152:155], v[200:203]\n"
    "buffer_load_dwordx4 a[24:27], v0, s[44:47], s33 offen offset:2048 sc1\n"
    "v_mul_f32 v240, v224, v240\n"
    "v_mfma_f32_16x16x32_bf16 v[204:207], v[24:27], a[156:159], v[204:207]\n"
    "v_exp_f32 v236, v236\n"
    "v_mul_f32 v241, v225, v241\n"
    "v_mfma_f32_16x16x32_bf16 v[208:211], v[28:31], a[144:147], v[208:211]\n"
    "ds_read_b64_tr_b16 v[24:25], v7 offset:16384\n"
    "v_exp_f32 v237, v237\n"
    "v_mfma_f32_16x16x32_bf16 v[212:215], v[28:31], a[148:151], v[212:215]\n"
    "ds_read_b64_tr_b16 v[26:27], v7 offset:18432\n"
    "v_exp_f32 v238, v238\n"
    "v_mfma_f32_16x16x32_bf16 v[216:219], v[28:31], a[152:155], v[216:219]\n"
    "buffer_load_dwordx4 a[28:31], v0, s[44:47], s33 offen offset:3072 sc1\n"
    "v_fma_f32 v236, v236, v55, v55\n"
    "v_mfma_f32_16x16x32_bf16 v[220:223], v[28:31], a[156:159], v[220:223]\n"
    "v_exp_f32 v239, v239\n"
    "s_waitcnt lgkmcnt(2)\n"
    "v_mfma_f32_16x16x32_bf16 v[96:99], v[16:19], a[128:131], v[96:99]\n"
    "ds_read_b64_tr_b16 v[28:29], v7 offset:24576\n"
    "s_add_u32 s33, s63, 8192\n"
    "v_fma_f32 v237, v237, v55, v55\n"
    "v_mfma_f32_16x16x32_bf16 v[100:103], v[16:19], a[132:135], v[100:103]\n"
    "ds_read_b64_tr_b16 v[30:31], v7 offset:26624\n"
    "v_fma_f32 v238, v238, v55, v55\n"
    "v_fma_f32 v239, v239, v55, v55\n"
    "v_mfma_f32_16x16x32_bf16 v[104:107], v[16:19], a[136:139], v[104:107]\n"
    "buffer_load_dwordx4 a[32:35], v0, s[44:47], s33 offen offset:0 sc1\n"
    "v_mul_f32 v242, v226, v242\n"
    "v_mfma_f32_16x16x32_bf16 v[108:111], v[16:19], a[140:143], v[108:111]\n"
    "v_rcp_f32 v236, v236\n"
    "v_mul_f32 v243, v227, v243\n"
    "v_mfma_f32_16x16x32_bf16 v[96:99], v[20:23], a[144:147], v[96:99]\n"
    "ds_read_b64_tr_b16 v[16:17], v7 offset:32768\n"
    "v_rcp_f32 v237, v237\n"
    "v_mfma_f32_16x16x32_bf16 v[100:103], v[20:23], a[148:151], v[100:103]\n"
    "ds_read_b64_tr_b16 v[18:19], v7 offset:34816\n"
    "v_rcp_f32 v238, v238\n"
    "v_mfma_f32_16x16x32_bf16 v[104:107], v[20:23], a[152:155], v[104:107]\n"
    "buffer_load_dwordx4 a[36:39], v0, s[44:47], s33 offen offset:1024 sc1\n"
    "v_cvt_pk_bf16_f32 v32, v240, v241\n"
    "v_mfma_f32_16x16x32_bf16 v[108:111], v[20:23], a[156:159], v[108:111]\n"
    "v_rcp_f32 v239, v239\n"
    "s_waitcnt lgkmcnt(2)\n"
    "v_mfma_f32_16x16x32_bf16 v[96:99], v[24:27], a[160:163], v[96:99]\n"
    "ds_read_b64_tr_b16 v[20:21], v7 offset:40960\n"
    "v_cvt_pk_bf16_f32 v33, v242, v243\n"
    "v_mul_f32 v244, v228, v244\n"
    "v_mfma_f32_16x16x32_bf16 v[100:103], v[24:27], a[164:167], v[100:103]\n"
    "ds_read_b64_tr_b16 v[22:23], v7 offset:43008\n"
    "v_mul_f32 v245, v229, v245\n"
    "v_mul_f32 v246, v230, v246\n"
    "v_mfma_f32_16x16x32_bf16 v[104:107], v[24:27], a[168:171], v[104:107]\n"
    "buffer_load_dwordx4 a[40:43], v0, s[44:47], s33 offen offset:2048 sc1\n"
    "v_mul_f32 v247, v231, v247\n"
    "v_mfma_f32_16x16x32_bf16 v[108:111], v[24:27], a[172:175], v[108:111]\n"
    "v_cvt_pk_bf16_f32 v36, v244, v245\n"
    "v_cvt_pk_bf16_f32 v37, v246, v247\n"
    "v_mul_f32 v248, v232, v248\n"
    "v_mfma_f32_16x16x32_bf16 v[96:99], v[28:31], a[176:179], v[96:99]\n"
    "ds_read_b64_tr_b16 v[24:25], v7 offset:49152\n"
    "v_mul_f32 v249, v233, v249\n"
    "v_mul_f32 v250, v234, v250\n"
    "v_mfma_f32_16x16x32_bf16 v[100:103], v[28:31], a[180:183], v[100:103]\n"
    "ds_read_b64_tr_b16 v[26:27], v7 offset:51200\n"
    "v_mul_f32 v251, v235, v251\n"
    "v_cvt_pk_bf16_f32 v40, v248, v249\n"
    "v_mfma_f32_16x16x32_bf16 v[104:107], v[28:31], a[184:187], v[104:107]\n"
    "buffer_load_dwordx4 a[44:47], v0, s[44:47], s33 offen offset:3072 sc1\n"
    "v_cvt_pk_bf16_f32 v41, v250, v251\n"
    "v_mfma_f32_16x16x32_bf16 v[108:111], v[28:31], a[188:191], v[108:111]\n"
    "v_mul_f32 v252, v236, v252\n"
    "v_mul_f32 v253, v237, v253\n"
    "s_waitcnt lgkmcnt(2)\n"
    "v_mfma_f32_16x16x32_bf16 v[96:99], v[16:19], a[192:195], v[96:99]\n"
    "ds_read_b64_tr_b16 v[28:29], v7 offset:57344\n"
    "s_add_u32 s33, s63, 12288\n"
    "v_mul_f32 v254, v238, v254\n"
    "v_mfma_f32_16x16x32_bf16 v[100:103], v[16:19], a[196:199], v[100:103]\n"
    "ds_read_b64_tr_b16 v[30:31], v7 offset:59392\n"
    "v_mul_f32 v255, v239, v255\n"
    "v_cvt_pk_bf16_f32 v44, v252, v253\n"
    "v_mfma_f32_16x16x32_bf16 v[104:107], v[16:19], a[200:203], v[104:107]\n"
    "buffer_load_dwordx4 a[48:51], v0, s[44:47], s33 offen offset:0 sc1\n"
    "v_cvt_pk_bf16_f32 v45, v254, v255\n"
    "v_mfma_f32_16x16x32_bf16 v[108:111], v[16:19], a[204:207], v[108:111]\n"
    "s_waitcnt vmcnt(24)\n"
    "s_barrier\n"
    "v_mfma_f32_16x16x32_bf16 v[96:99], v[20:23], a[208:211], v[96:99]\n"
    "v_add_u32 v7, 0x10000, v7\n"
    "ds_read_b64_tr_b16 v[16:17], v7 offset:0\n"
    "s_add_u32 m0, s53, 0\n"
    "v_mfma_f32_16x16x32_bf16 v[100:103], v[20:23], a[212:215], v[100:103]\n"
    "ds_read_b64_tr_b16 v[18:19], v7 offset:2048\n"
    "s_add_u32 s79, s55, 589824\n"
    "v_mfma_f32_16x16x32_bf16 v[104:107], v[20:23], a[216:219], v[104:107]\n"
    "s_add_u32 s79, s79, s73\n"
    "v_lshrrev_b32 v62, 8, v0\n"
    "v_mfma_f32_16x16x32_bf16 v[108:111], v[20:23], a[220:223], v[108:111]\n"
    "v_mul_u32_u24 v62, 0x90000, v62\n"
    "s_waitcnt lgkmcnt(2)\n"
    "v_mfma_f32_16x16x32_bf16 v[96:99], v[24:27], a[224:227], v[96:99]\n"
    "ds_read_b64_tr_b16 v[20:21], v7 offset:8192\n"
    "v_add_u32 v5, v5, v62\n"
    "v_mfma_f32_16x16x32_bf16 v[100:103], v[24:27], a[228:231], v[100:103]\n"
    "ds_read_b64_tr_b16 v[22:23], v7 offset:10240\n"
    "v_mfma_f32_16x16x32_bf16 v[104:107], v[24:27], a[232:235], v[104:107]\n"
    "buffer_load_dwordx4 v5, s[36:39], s79 offen lds\n"
    "v_mfma_f32_16x16x32_bf16 v[108:111], v[24:27], a[236:239], v[108:111]\n"
    "v_mfma_f32_16x16x32_bf16 v[96:99], v[28:31], a[240:243], v[96:99]\n"
    "v_add_u32 v7, 0xffff0000, v7\n"
    "ds_read_b64_tr_b16 v[24:25], v7 offset:4096\n"
    "v_mfma_f32_16x16x32_bf16 v[100:103], v[28:31], a[244:247], v[100:103]\n"
    "ds_read_b64_tr_b16 v[26:27], v7 offset:6144\n"
    "v_mfma_f32_16x16x32_bf16 v[104:107], v[28:31], a[248:251], v[104:107]\n"
    "v_mfma_f32_16x16x32_bf16 v[108:111], v[28:31], a[252:255], v[108:111]\n"
    "s_waitcnt lgkmcnt(2)\n"
    "v_mfma_f32_16x16x32_bf16 v[96:99], v[16:19], v[64:67], v[96:99]\n"
    "ds_read_b64_tr_b16 v[28:29], v7 offset:12288\n"
    "v_mfma_f32_16x16x32_bf16 v[100:103], v[16:19], v[68:71], v[100:103]\n"
    "ds_read_b64_tr_b16 v[30:31], v7 offset:14336\n"
    "v_mfma_f32_16x16x32_bf16 v[104:107], v[16:19], v[72:75], v[104:107]\n"
    "buffer_load_dword v60, v11, s[44:47], 0 offen offset:256 sc1\n"
    "v_mfma_f32_16x16x32_bf16 v[108:111], v[16:19], v[76:79], v[108:111]\n"
    "buffer_load_dwordx4 a[52:55], v0, s[44:47], s33 offen offset:1024 sc1\n"
    "v_mfma_f32_16x16x32_bf16 v[96:99], v[20:23], v[80:83], v[96:99]\n"
    "ds_read_b64_tr_b16 v[16:17], v7 offset:20480\n"
    "v_mfma_f32_16x16x32_bf16 v[100:103], v[20:23], v[84:87], v[100:103]\n"
    "ds_read_b64_tr_b16 v[18:19], v7 offset:22528\n"
    "v_mfma_f32_16x16x32_bf16 v[104:107], v[20:23], v[88:91], v[104:107]\n"
    "buffer_load_dwordx4 a[56:59], v0, s[44:47], s33 offen offset:2048 sc1\n"
    "v_mfma_f32_16x16x32_bf16 v[108:111], v[20:23], v[92:95], v[108:111]\n"
    "s_waitcnt lgkmcnt(2)\n"
    "v_mfma_f32_16x16x32_bf16 v[112:115], v[24:27], a[128:131], v[112:115]\n"
    "ds_read_b64_tr_b16 v[20:21], v7 offset:28672\n"
    "v_mfma_f32_16x16x32_bf16 v[116:119], v[24:27], a[132:135], v[116:119]\n"
    "ds_read_b64_tr_b16 v[22:23], v7 offset:30720\n"
    "v_mfma_f32_16x16x32_bf16 v[120:123], v[24:27], a[136:139], v[120:123]\n"
    "buffer_load_dwordx4 a[60:63], v0, s[44:47], s33 offen offset:3072 sc1\n"
    "v_mfma_f32_16x16x32_bf16 v[124:127], v[24:27], a[140:143], v[124:127]\n"
    "v_mfma_f32_16x16x32_bf16 v[112:115], v[28:31], a[144:147], v[112:115]\n"
    "ds_read_b64_tr_b16 v[24:25], v7 offset:36864\n"
    "v_mul_f32 v96, v48, v96\n"
    "v_mul_f32 v97, v48, v97\n"
    "v_mfma_f32_16x16x32_bf16 v[116:119], v[28:31], a[148:151], v[116:119]\n"
    "ds_read_b64_tr_b16 v[26:27], v7 offset:38912\n"
    "v_mul_f32 v98, v48, v98\n"
    "v_mul_f32 v99, v48, v99\n"
    "v_mfma_f32_16x16x32_bf16 v[120:123], v[28:31], a[152:155], v[120:123]\n"
    "v_exp_f32 v96, v96\n"
    "v_mul_f32 v100, v49, v100\n"
    "v_mfma_f32_16x16x32_bf16 v[124:127], v[28:31], a[156:159], v[124:127]\n"
    "v_exp_f32 v97, v97\n"
    "s_waitcnt lgkmcnt(2)\n"
    "v_mfma_f32_16x16x32_bf16 v[112:115], v[16:19], a[160:163], v[112:115]\n"
    "ds_read_b64_tr_b16 v[28:29], v7 offset:45056\n"
    "v_exp_f32 v98, v98\n"
    "v_mfma_f32_16x16x32_bf16 v[116:119], v[16:19], a[164:167], v[116:119]\n"
    "ds_read_b64_tr_b16 v[30:31], v7 offset:47104\n"
    "v_exp_f32 v99, v99\n"
    "v_mfma_f32_16x16x32_bf16 v[120:123], v[16:19], a[168:171], v[120:123]\n"
    "v_fma_f32 v96, v96, v52, v52\n"
    "v_fma_f32 v97, v97, v52, v52\n"
    "v_fma_f32 v98, v98, v52, v52\n"
    "v_mfma_f32_16x16x32_bf16 v[124:127], v[16:19], a[172:175], v[124:127]\n"
    "v_fma_f32 v99, v99, v52, v52\n"
    "v_rcp_f32 v96, v96\n"
    "v_mfma_f32_16x16x32_bf16 v[112:115], v[20:23], a[176:179], v[112:115]\n"
    "ds_read_b64_tr_b16 v[16:17], v7 offset:53248\n"
    "v_rcp_f32 v97, v97\n"
    "v_mfma_f32_16x16x32_bf16 v[116:119], v[20:23], a[180:183], v[116:119]\n"
    "ds_read_b64_tr_b16 v[18:19], v7 offset:55296\n"
    "v_rcp_f32 v98, v98\n"
    "v_mfma_f32_16x16x32_bf16 v[120:123], v[20:23], a[184:187], v[120:123]\n"
    "v_rcp_f32 v99, v99\n"
    "v_mul_f32 v101, v49, v101\n"
    "v_mfma_f32_16x16x32_bf16 v[124:127], v[20:23], a[188:191], v[124:127]\n"
    "v_mul_f32 v102, v49, v102\n"
    "v_mul_f32 v103, v49, v103\n"
    "s_waitcnt lgkmcnt(2)\n"
    "v_mfma_f32_16x16x32_bf16 v[112:115], v[24:27], a[192:195], v[112:115]\n"
    "ds_read_b64_tr_b16 v[20:21], v7 offset:61440\n"
    "v_exp_f32 v100, v100\n"
    "v_mfma_f32_16x16x32_bf16 v[116:119], v[24:27], a[196:199], v[116:119]\n"
    "ds_read_b64_tr_b16 v[22:23], v7 offset:63488\n"
    "v_exp_f32 v101, v101\n"
    "v_mfma_f32_16x16x32_bf16 v[120:123], v[24:27], a[200:203], v[120:123]\n"
    "v_exp_f32 v102, v102\n"
    "v_fma_f32 v100, v100, v53, v53\n"
    "v_mfma_f32_16x16x32_bf16 v[124:127], v[24:27], a[204:207], v[124:127]\n"
    "v_exp_f32 v103, v103\n"
    "v_fma_f32 v101, v101, v53, v53\n"
    "v_mfma_f32_16x16x32_bf16 v[112:115], v[28:31], a[208:211], v[112:115]\n"
    "v_add_u32 v7, 0x10000, v7\n"
    "ds_read_b64_tr_b16 v[24:25], v7 offset:4096\n"
    "v_fma_f32 v102, v102, v53, v53\n"
    "v_fma_f32 v103, v103, v53, v53\n"
    "v_mfma_f32_16x16x32_bf16 v[116:119], v[28:31], a[212:215], v[116:119]\n"
    "ds_read_b64_tr_b16 v[26:27], v7 offset:6144\n"
    "v_rcp_f32 v100, v100\n"
    "v_mfma_f32_16x16x32_bf16 v[120:123], v[28:31], a[216:219], v[120:123]\n"
    "v_rcp_f32 v101, v101\n"
    "v_mul_f32 v104, v50, v104\n"
    "v_mfma_f32_16x16x32_bf16 v[124:127], v[28:31], a[220:223], v[124:127]\n"
    "v_rcp_f32 v102, v102\n"
    "s_waitcnt lgkmcnt(2)\n"
    "v_mfma_f32_16x16x32_bf16 v[112:115], v[16:19], a[224:227], v[112:115]\n"
    "ds_read_b64_tr_b16 v[28:29], v7 offset:12288\n"
    "v_rcp_f32 v103, v103\n"
    "v_mfma_f32_16x16x32_bf16 v[116:119], v[16:19], a[228:231], v[116:119]\n"
    "ds_read_b64_tr_b16 v[30:31], v7 offset:14336\n"
    "v_mul_f32 v105, v50, v105\n"
    "v_mul_f32 v106, v50, v106\n"
    "v_mfma_f32_16x16x32_bf16 v[120:123], v[16:19], a[232:235], v[120:123]\n"
    "v_mul_f32 v107, v50, v107\n"
    "v_exp_f32 v104, v104\n"
    "v_mfma_f32_16x16x32_bf16 v[124:127], v[16:19], a[236:239], v[124:127]\n"
    "v_exp_f32 v105, v105\n"
    "v_fma_f32 v104, v104, v54, v54\n"
    "v_mfma_f32_16x16x32_bf16 v[112:115], v[20:23], a[240:243], v[112:115]\n"
    "ds_read_b64_tr_b16 v[16:17], v10 offset:4096\n"
    "v_exp_f32 v106, v106\n"
    "v_mfma_f32_16x16x32_bf16 v[116:119], v[20:23], a[244:247], v[116:119]\n"
    "ds_read_b64_tr_b16 v[18:19], v10 offset:4608\n"
    "v_exp_f32 v107, v107\n"
    "v_mfma_f32_16x16x32_bf16 v[120:123], v[20:23], a[248:251], v[120:123]\n"
    "v_fma_f32 v105, v105, v54, v54\n"
    "v_fma_f32 v106, v106, v54, v54\n"
    "v_fma_f32 v107, v107, v54, v54\n"
    "v_mfma_f32_16x16x32_bf16 v[124:127], v[20:23], a[252:255], v[124:127]\n"
    "v_rcp_f32 v104, v104\n"
    "s_waitcnt lgkmcnt(2)\n"
    "v_mfma_f32_16x16x32_bf16 v[112:115], v[24:27], v[64:67], v[112:115]\n"
    "ds_read_b64_tr_b16 v[20:21], v10 offset:5120\n"
    "v_rcp_f32 v105, v105\n"
    "v_mfma_f32_16x16x32_bf16 v[116:119], v[24:27], v[68:71], v[116:119]\n"
    "ds_read_b64_tr_b16 v[22:23], v10 offset:5632\n"
    "v_rcp_f32 v106, v106\n"
    "v_mfma_f32_16x16x32_bf16 v[120:123], v[24:27], v[72:75], v[120:123]\n"
    "v_rcp_f32 v107, v107\n"
    "v_mul_f32 v108, v51, v108\n"
    "v_mfma_f32_16x16x32_bf16 v[124:127], v[24:27], v[76:79], v[124:127]\n"
    "v_mul_f32 v109, v51, v109\n"
    "v_mul_f32 v110, v51, v110\n"
    "v_mul_f32 v111, v51, v111\n"
    "v_mfma_f32_16x16x32_bf16 v[112:115], v[28:31], v[80:83], v[112:115]\n"
    "ds_read_b64_tr_b16 v[24:25], v10 offset:6144\n"
    "v_exp_f32 v108, v108\n"
    "v_mfma_f32_16x16x32_bf16 v[116:119], v[28:31], v[84:87], v[116:119]\n"
    "ds_read_b64_tr_b16 v[26:27], v10 offset:6656\n"
    "v_exp_f32 v109, v109\n"
    "v_mfma_f32_16x16x32_bf16 v[120:123], v[28:31], v[88:91], v[120:123]\n"
    "v_exp_f32 v110, v110\n"
    "v_fma_f32 v108, v108, v55, v55\n"
    "v_mfma_f32_16x16x32_bf16 v[124:127], v[28:31], v[92:95], v[124:127]\n"
    "v_exp_f32 v111, v111\n"
    "s_waitcnt lgkmcnt(2)\n"
    "v_mfma_f32_16x16x32_bf16 v[192:195], v[16:19], a[160:163], v[192:195]\n"
    "ds_read_b64_tr_b16 v[28:29], v10 offset:7168\n"
    "v_fma_f32 v109, v109, v55, v55\n"
    "v_fma_f32 v110, v110, v55, v55\n"
    "v_mfma_f32_16x16x32_bf16 v[196:199], v[16:19], a[164:167], v[196:199]\n"
    "ds_read_b64_tr_b16 v[30:31], v10 offset:7680\n"
    "v_fma_f32 v111, v111, v55, v55\n"
    "v_mfma_f32_16x16x32_bf16 v[200:203], v[16:19], a[168:171], v[200:203]\n"
    "v_rcp_f32 v108, v108\n"
    "v_mfma_f32_16x16x32_bf16 v[204:207], v[16:19], a[172:175], v[204:207]\n"
    "s_waitcnt vmcnt(3)\n"
    "v_cmp_ne_u32 vcc, s93, v60\n"
    "s_cbranch_vccnz LS1_k\n"
    "LB1_k:\n"
    "s_barrier\n"
    "v_mfma_f32_16x16x32_bf16 v[208:211], v[20:23], a[160:163], v[208:211]\n"
    "v_add_u32 v8, 0x10000, v8\n"
    "ds_read_b64_tr_b16 v[16:17], v8 offset:0\n"
    "s_add_u32 s33, s63, 16384\n"
    "v_mul_f32 v112, v96, v112\n"
    "v_mfma_f32_16x16x32_bf16 v[212:215], v[20:23], a[164:167], v[212:215]\n"
    "ds_read_b64_tr_b16 v[18:19], v8 offset:2048\n"
    "v_rcp_f32 v109, v109\n"
    "v_mfma_f32_16x16x32_bf16 v[216:219], v[20:23], a[168:171], v[216:219]\n"
    "buffer_load_dwordx4 a[96:99], v0, s[44:47], s33 offen offset:0 sc1\n"
    "v_mul_f32 v113, v97, v113\n"
    "v_mfma_f32_16x16x32_bf16 v[220:223], v[20:23], a[172:175], v[220:223]\n"
    "buffer_load_dwordx4 a[100:103], v0, s[44:47], s33 offen offset:1024 sc1\n"
    "s_waitcnt lgkmcnt(2)\n"
    "v_mfma_f32_16x16x32_bf16 v[192:195], v[24:27], a[176:179], v[192:195]\n"
    "ds_read_b64_tr_b16 v[20:21], v8 offset:8192\n"
    "buffer_load_dwordx4 a[104:107], v0, s[44:47], s33 offen offset:2048 sc1\n"
    "v_mfma_f32_16x16x32_bf16 v[196:199], v[24:27], a[180:183], v[196:199]\n"
    "ds_read_b64_tr_b16 v[22:23], v8 offset:10240\n"
    "v_rcp_f32 v110, v110\n"
    "v_mfma_f32_16x16x32_bf16 v[200:203], v[24:27], a[184:187], v[200:203]\n"
    "v_rcp_f32 v111, v111\n"
    "v_mul_f32 v114, v98, v114\n"
    "v_mfma_f32_16x16x32_bf16 v[204:207], v[24:27], a[188:191], v[204:207]\n"
    "v_mul_f32 v115, v99, v115\n"
    "v_cvt_pk_bf16_f32 v34, v112, v113\n"
    "s_add_u32 s35, s57, 32768\n"
    "v_mfma_f32_16x16x32_bf16 v[208:211], v[28:31], a[176:179], v[208:211]\n"
    "ds_read_b64_tr_b16 v[24:25], v8 offset:16384\n"
    "v_cvt_pk_bf16_f32 v35, v114, v115\n"
    "v_mul_f32 v116, v100, v116\n"
    "v_mfma_f32_16x16x32_bf16 v[212:215], v[28:31], a[180:183], v[212:215]\n"
    "ds_read_b64_tr_b16 v[26:27], v8 offset:18432\n"
    "buffer_store_dwordx4 v[32:35], v0, s[44:47], s35 offen offset:0 sc1\n"
    "v_mfma_f32_16x16x32_bf16 v[216:219], v[28:31], a[184:187], v[216:219]\n"
    "v_mul_f32 v117, v101, v117\n"
    "v_mul_f32 v118, v102, v118\n"
    "v_mul_f32 v119, v103, v119\n"
    "v_mfma_f32_16x16x32_bf16 v[220:223], v[28:31], a[188:191], v[220:223]\n"
    "v_cvt_pk_bf16_f32 v38, v116, v117\n"
    "v_cvt_pk_bf16_f32 v39, v118, v119\n"
    "s_waitcnt lgkmcnt(2)\n"
    "v_mfma_f32_16x16x32_bf16 v[128:131], v[16:19], a[128:131], v[128:131]\n"
    "ds_read_b64_tr_b16 v[28:29], v8 offset:24576\n"
    "v_mul_f32 v120, v104, v120\n"
    "v_mul_f32 v121, v105, v121\n"
    "v_mfma_f32_16x16x32_bf16 v[132:135], v[16:19], a[132:135], v[132:135]\n"
    "ds_read_b64_tr_b16 v[30:31], v8 offset:26624\n"
    "v_mul_f32 v122, v106, v122\n"
    "v_mul_f32 v123, v107, v123\n"
    "v_mfma_f32_16x16x32_bf16 v[136:139], v[16:19], a[136:139], v[136:139]\n"
    "v_cvt_pk_bf16_f32 v42, v120, v121\n"
    "v_cvt_pk_bf16_f32 v43, v122, v123\n"
    "s_add_u32 s76, s57, 327680\n"
    "v_mfma_f32_16x16x32_bf16 v[140:143], v[16:19], a[140:143], v[140:143]\n"
    "buffer_store_dwordx4 v[36:39], v0, s[44:47], s35 offen offset:1024 sc1\n"
    "v_mul_f32 v124, v108, v124\n"
    "v_mfma_f32_16x16x32_bf16 v[128:131], v[20:23], a[144:147], v[128:131]\n"
    "ds_read_b64_tr_b16 v[16:17], v8 offset:32768\n"
    "v_mul_f32 v125, v109, v125\n"
    "v_mul_f32 v126, v110, v126\n"
    "v_mfma_f32_16x16x32_bf16 v[132:135], v[20:23], a[148:151], v[132:135]\n"
    "ds_read_b64_tr_b16 v[18:19], v8 offset:34816\n"
    "v_mul_f32 v127, v111, v127\n"
    "v_cvt_pk_bf16_f32 v46, v124, v125\n"
    "v_mfma_f32_16x16x32_bf16 v[136:139], v[20:23], a[152:155], v[136:139]\n"
    "v_cvt_pk_bf16_f32 v47, v126, v127\n"
    "v_mfma_f32_16x16x32_bf16 v[140:143], v[20:23], a[156:159], v[140:143]\n"
    "s_waitcnt lgkmcnt(2)\n"
    "v_mfma_f32_16x16x32_bf16 v[128:131], v[24:27], a[160:163], v[128:131]\n"
    "ds_read_b64_tr_b16 v[20:21], v8 offset:40960\n"
    "v_mfma_f32_16x16x32_bf16 v[132:135], v[24:27], a[164:167], v[132:135]\n"
    "ds_read_b64_tr_b16 v[22:23], v8 offset:43008\n"
    "buffer_store_dwordx4 v[40:43], v0, s[44:47], s76 offen offset:0 sc1\n"
    "v_mfma_f32_16x16x32_bf16 v[136:139], v[24:27], a[168:171], v[136:139]\n"
    "v_mfma_f32_16x16x32_bf16 v[140:143], v[24:27], a[172:175], v[140:143]\n"
    "v_mfma_f32_16x16x32_bf16 v[128:131], v[28:31], a[176:179], v[128:131]\n"
    "ds_read_b64_tr_b16 v[24:25], v8 offset:49152\n"
    "v_mfma_f32_16x16x32_bf16 v[132:135], v[28:31], a[180:183], v[132:135]\n"
    "ds_read_b64_tr_b16 v[26:27], v8 offset:51200\n"
    "v_mfma_f32_16x16x32_bf16 v[136:139], v[28:31], a[184:187], v[136:139]\n"
    "v_mfma_f32_16x16x32_bf16 v[140:143], v[28:31], a[188:191], v[140:143]\n"
    "buffer_store_dwordx4 v[44:47], v0, s[44:47], s76 offen offset:1024 sc1\n"
    "s_waitcnt lgkmcnt(2)\n"
    "v_mfma_f32_16x16x32_bf16 v[128:131], v[16:19], a[192:195], v[128:131]\n"
    "ds_read_b64_tr_b16 v[28:29], v8 offset:57344\n"
    "v_mfma_f32_16x16x32_bf16 v[132:135], v[16:19], a[196:199], v[132:135]\n"
    "ds_read_b64_tr_b16 v[30:31], v8 offset:59392\n"
    "v_mfma_f32_16x16x32_bf16 v[136:139], v[16:19], a[200:203], v[136:139]\n"
    "v_mfma_f32_16x16x32_bf16 v[140:143], v[16:19], a[204:207], v[140:143]\n"
    "v_mfma_f32_16x16x32_bf16 v[128:131], v[20:23], a[208:211], v[128:131]\n"
    "v_add_u32 v8, 0x10000, v8\n"
    "ds_read_b64_tr_b16 v[16:17], v8 offset:0\n"
    "v_mfma_f32_16x16x32_bf16 v[132:135], v[20:23], a[212:215], v[132:135]\n"
    "ds_read_b64_tr_b16 v[18:19], v8 offset:2048\n"
    "v_mfma_f32_16x16x32_bf16 v[136:139], v[20:23], a[216:219], v[136:139]\n"
    "v_mfma_f32_16x16x32_bf16 v[140:143], v[20:23], a[220:223], v[140:143]\n"
    "s_waitcnt lgkmcnt(2)\n"
    "v_mfma_f32_16x16x32_bf16 v[128:131], v[24:27], a[224:227], v[128:131]\n"
    "ds_read_b64_tr_b16 v[20:21], v8 offset:8192\n"
    "v_mfma_f32_16x16x32_bf16 v[132:135], v[24:27], a[228:231], v[132:135]\n"
    "ds_read_b64_tr_b16 v[22:23], v8 offset:10240\n"
    "v_mfma_f32_16x16x32_bf16 v[136:139], v[24:27], a[232:235], v[136:139]\n"
    "v_mfma_f32_16x16x32_bf16 v[140:143], v[24:27], a[236:239], v[140:143]\n"
    "buffer_load_dwordx4 a[108:111], v0, s[44:47], s33 offen offset:3072 sc1\n"
    "v_mfma_f32_16x16x32_bf16 v[128:131], v[28:31], a[240:243], v[128:131]\n"
    "v_add_u32 v8, 0xffff0000, v8\n"
    "ds_read_b64_tr_b16 v[24:25], v8 offset:4096\n"
    "v_mfma_f32_16x16x32_bf16 v[132:135], v[28:31], a[244:247], v[132:135]\n"
    "ds_read_b64_tr_b16 v[26:27], v8 offset:6144\n"
    "v_mfma_f32_16x16x32_bf16 v[136:139], v[28:31], a[248:251], v[136:139]\n"
    "v_mfma_f32_16x16x32_bf16 v[140:143], v[28:31], a[252:255], v[140:143]\n"
    "s_add_u32 s33, s63, 20480\n"
    "s_waitcnt lgkmcnt(2)\n"
    "v_mfma_f32_16x16x32_bf16 v[128:131], v[16:19], v[64:67], v[128:131]\n"
    "ds_read_b64_tr_b16 v[28:29], v8 offset:12288\n"
    "buffer_load_dwordx4 a[112:115], v0, s[44:47], s33 offen offset:0 sc1\n"
    "v_mfma_f32_16x16x32_bf16 v[132:135], v[16:19], v[68:71], v[132:135]\n"
    "ds_read_b64_tr_b16 v[30:31], v8 offset:14336\n"
    "v_mfma_f32_16x16x32_bf16 v[136:139], v[16:19], v[72:75], v[136:139]\n"
    "v_mfma_f32_16x16x32_bf16 v[140:143], v[16:19], v[76:79], v[140:143]\n"
    "buffer_load_dwordx4 a[116:119], v0, s[44:47], s33 offen offset:1024 sc1\n"
    "v_mfma_f32_16x16x32_bf16 v[128:131], v[20:23], v[80:83], v[128:131]\n"
    "ds_read_b64_tr_b16 v[16:17], v8 offset:20480\n"
    "v_mfma_f32_16x16x32_bf16 v[132:135], v[20:23], v[84:87], v[132:135]\n"
    "ds_read_b64_tr_b16 v[18:19], v8 offset:22528\n"
    "v_mfma_f32_16x16x32_bf16 v[136:139], v[20:23], v[88:91], v[136:139]\n"
    "v_mfma_f32_16x16x32_bf16 v[140:143], v[20:23], v[92:95], v[140:143]\n"
    "buffer_load_dwordx4 a[120:123], v0, s[44:47], s33 offen offset:2048 sc1\n"
    "s_waitcnt lgkmcnt(2)\n"
    "v_mfma_f32_16x16x32_bf16 v[144:147], v[24:27], a[128:131], v[144:147]\n"
    "ds_read_b64_tr_b16 v[20:21], v8 offset:28672\n"
    "v_mfma_f32_16x16x32_bf16 v[148:151], v[24:27], a[132:135], v[148:151]\n"
    "ds_read_b64_tr_b16 v[22:23], v8 offset:30720\n"
    "v_mfma_f32_16x16x32_bf16 v[152:155], v[24:27], a[136:139], v[152:155]\n"
    "v_mfma_f32_16x16x32_bf16 v[156:159], v[24:27], a[140:143], v[156:159]\n"
    "buffer_load_dwordx4 a[124:127], v0, s[44:47], s33 offen offset:3072 sc1\n"
    "v_mfma_f32_16x16x32_bf16 v[144:147], v[28:31], a[144:147], v[144:147]\n"
    "ds_read_b64_tr_b16 v[24:25], v8 offset:36864\n"
    "v_mul_f32 v128, v48, v128\n"
    "v_mul_f32 v129, v48, v129\n"
    "v_mfma_f32_16x16x32_bf16 v[148:151], v[28:31], a[148:151], v[148:151]\n"
    "ds_read_b64_tr_b16 v[26:27], v8 offset:38912\n"
    "v_mul_f32 v130, v48, v130\n"
    "v_mul_f32 v131, v48, v131\n"
    "v_mfma_f32_16x16x32_bf16 v[152:155], v[28:31], a[152:155], v[152:155]\n"
    "v_exp_f32 v128, v128\n"
    "v_mul_f32 v132, v49, v132\n"
    "v_mfma_f32_16x16x32_bf16 v[156:159], v[28:31], a[156:159], v[156:159]\n"
    "v_exp_f32 v129, v129\n"
    "s_waitcnt lgkmcnt(2)\n"
    "v_mfma_f32_16x16x32_bf16 v[144:147], v[16:19], a[160:163], v[144:147]\n"
    "ds_read_b64_tr_b16 v[28:29], v8 offset:45056\n"
    "v_exp_f32 v130, v130\n"
    "v_mfma_f32_16x16x32_bf16 v[148:151], v[16:19], a[164:167], v[148:151]\n"
    "ds_read_b64_tr_b16 v[30:31], v8 offset:47104\n"
    "v_exp_f32 v131, v131\n"
    "v_mfma_f32_16x16x32_bf16 v[152:155], v[16:19], a[168:171], v[152:155]\n"
    "v_fma_f32 v128, v128, v52, v52\n"
    "v_fma_f32 v129, v129, v52, v52\n"
    "v_fma_f32 v130, v130, v52, v52\n"
    "v_mfma_f32_16x16x32_bf16 v[156:159], v[16:19], a[172:175], v[156:159]\n"
    "v_fma_f32 v131, v131, v52, v52\n"
    "s_barrier\n"
    "v_mfma_f32_16x16x32_bf16 v[144:147], v[20:23], a[176:179], v[144:147]\n"
    "ds_read_b64_tr_b16 v[16:17], v8 offset:53248\n"
    "s_add_u32 m0, s53, 4096\n"
    "v_mul_f32 v133, v49, v133\n"
    "v_mfma_f32_16x16x32_bf16 v[148:151], v[20:23], a[180:183], v[148:151]\n"
    "ds_read_b64_tr_b16 v[18:19], v8 offset:55296\n"
    "s_add_u32 s79, s61, 0\n"
    "v_mul_f32 v134, v49, v134\n"
    "v_mfma_f32_16x16x32_bf16 v[152:155], v[20:23], a[184:187], v[152:155]\n"
    "buffer_load_dwordx4 v15, s[40:43], s79 offen lds\n"
    "v_mfma_f32_16x16x32_bf16 v[156:159], v[20:23], a[188:191], v[156:159]\n"
    "v_rcp_f32 v128, v128\n"
    "s_waitcnt lgkmcnt(2)\n"
    "v_mfma_f32_16x16x32_bf16 v[144:147], v[24:27], a[192:195], v[144:147]\n"
    "ds_read_b64_tr_b16 v[20:21], v8 offset:61440\n"
    "v_rcp_f32 v129, v129\n"
    "v_mfma_f32_16x16x32_bf16 v[148:151], v[24:27], a[196:199], v[148:151]\n"
    "ds_read_b64_tr_b16 v[22:23], v8 offset:63488\n"
    "v_rcp_f32 v130, v130\n"
    "v_mfma_f32_16x16x32_bf16 v[152:155], v[24:27], a[200:203], v[152:155]\n"
    "v_rcp_f32 v131, v131\n"
    "v_mul_f32 v135, v49, v135\n"
    "v_mfma_f32_16x16x32_bf16 v[156:159], v[24:27], a[204:207], v[156:159]\n"
    "v_lshrrev_b32 v63, 2, v0\n"
    "v_mul_f32 v136, v50, v136\n"
    "v_mfma_f32_16x16x32_bf16 v[144:147], v[28:31], a[208:211], v[144:147]\n"
    "v_add_u32 v8, 0x10000, v8\n"
    "ds_read_b64_tr_b16 v[24:25], v8 offset:4096\n"
    "v_and_b32 v63, 0x7c, v63\n"
    "v_mul_f32 v137, v50, v137\n"
    "v_mfma_f32_16x16x32_bf16 v[148:151], v[28:31], a[212:215], v[148:151]\n"
    "ds_read_b64_tr_b16 v[26:27], v8 offset:6144\n"
    "v_add_u32 v63, s32, v63\n"
    "v_mfma_f32_16x16x32_bf16 v[152:155], v[28:31], a[216:219], v[152:155]\n"
    "buffer_load_dword v60, v63, s[44:47], 0 offen offset:256 sc1\n"
    "v_mfma_f32_16x16x32_bf16 v[156:159], v[28:31], a[220:223], v[156:159]\n"
    "v_exp_f32 v132, v132\n"
    "s_waitcnt lgkmcnt(2)\n"
    "v_mfma_f32_16x16x32_bf16 v[144:147], v[16:19], a[224:227], v[144:147]\n"
    "ds_read_b64_tr_b16 v[28:29], v8 offset:12288\n"
    "v_exp_f32 v133, v133\n"
    "v_mfma_f32_16x16x32_bf16 v[148:151], v[16:19], a[228:231], v[148:151]\n"
    "ds_read_b64_tr_b16 v[30:31], v8 offset:14336\n"
    "v_exp_f32 v134, v134\n"
    "v_mfma_f32_16x16x32_bf16 v[152:155], v[16:19], a[232:235], v[152:155]\n"
    "v_exp_f32 v135, v135\n"
    "v_fma_f32 v132, v132, v53, v53\n"
    "v_mfma_f32_16x16x32_bf16 v[156:159], v[16:19], a[236:239], v[156:159]\n"
    "v_fma_f32 v133, v133, v53, v53\n"
    "v_fma_f32 v134, v134, v53, v53\n"
    "v_fma_f32 v135, v135, v53, v53\n"
    "v_mfma_f32_16x16x32_bf16 v[144:147], v[20:23], a[240:243], v[144:147]\n"
    "ds_read_b64_tr_b16 v[16:17], v10 offset:8192\n"
    "v_rcp_f32 v132, v132\n"
    "v_mfma_f32_16x16x32_bf16 v[148:151], v[20:23], a[244:247], v[148:151]\n"
    "ds_read_b64_tr_b16 v[18:19], v10 offset:8704\n"
    "v_rcp_f32 v133, v133\n"
    "v_mfma_f32_16x16x32_bf16 v[152:155], v[20:23], a[248:251], v[152:155]\n"
    "v_rcp_f32 v134, v134\n"
    "v_mul_f32 v138, v50, v138\n"
    "v_mfma_f32_16x16x32_bf16 v[156:159], v[20:23], a[252:255], v[156:159]\n"
    "v_rcp_f32 v135, v135\n"
    "s_waitcnt lgkmcnt(2)\n"
    "v_mfma_f32_16x16x32_bf16 v[144:147], v[24:27], v[64:67], v[144:147]\n"
    "ds_read_b64_tr_b16 v[20:21], v10 offset:9216\n"
    "v_mul_f32 v139, v50, v139\n"
    "v_mul_f32 v140, v51, v140\n"
    "v_mfma_f32_16x16x32_bf16 v[148:151], v[24:27], v[68:71], v[148:151]\n"
    "ds_read_b64_tr_b16 v[22:23], v10 offset:9728\n"
    "v_exp_f32 v136, v136\n"
    "v_mfma_f32_16x16x32_bf16 v[152:155], v[24:27], v[72:75], v[152:155]\n"
    "v_exp_f32 v137, v137\n"
    "v_fma_f32 v136, v136, v54, v54\n"
    "v_mfma_f32_16x16x32_bf16 v[156:159], v[24:27], v[76:79], v[156:159]\n"
    "v_exp_f32 v138, v138\n"
    "v_fma_f32 v137, v137, v54, v54\n"
    "v_mfma_f32_16x16x32_bf16 v[144:147], v[28:31], v[80:83], v[144:147]\n"
    "ds_read_b64_tr_b16 v[24:25], v10 offset:10240\n"
    "v_exp_f32 v139, v139\n"
    "v_mfma_f32_16x16x32_bf16 v[148:151], v[28:31], v[84:87], v[148:151]\n"
    "ds_read_b64_tr_b16 v[26:27], v10 offset:10752\n"
    "v_fma_f32 v138, v138, v54, v54\n"
    "v_fma_f32 v139, v139, v54, v54\n"
    "v_mfma_f32_16x16x32_bf16 v[152:155], v[28:31], v[88:91], v[152:155]\n"
    "v_rcp_f32 v136, v136\n"
    "v_mul_f32 v141, v51, v141\n"
    "v_mfma_f32_16x16x32_bf16 v[156:159], v[28:31], v[92:95], v[156:159]\n"
    "v_rcp_f32 v137, v137\n"
    "s_waitcnt lgkmcnt(2)\n"
    "v_mfma_f32_16x16x32_bf16 v[192:195], v[16:19], a[192:195], v[192:195]\n"
    "ds_read_b64_tr_b16 v[28:29], v10 offset:11264\n"
    "v_rcp_f32 v138, v138\n"
    "v_mfma_f32_16x16x32_bf16 v[196:199], v[16:19], a[196:199], v[196:199]\n"
    "ds_read_b64_tr_b16 v[30:31], v10 offset:11776\n"
    "v_rcp_f32 v139, v139\n"
    "v_mfma_f32_16x16x32_bf16 v[200:203], v[16:19], a[200:203], v[200:203]\n"
    "v_mul_f32 v142, v51, v142\n"
    "v_mul_f32 v143, v51, v143\n"
    "v_mfma_f32_16x16x32_bf16 v[204:207], v[16:19], a[204:207], v[204:207]\n"
    "v_exp_f32 v140, v140\n"
    "v_mfma_f32_16x16x32_bf16 v[208:211], v[20:23], a[192:195], v[208:211]\n"
    "v_add_u32 v9, 0x10000, v9\n"
    "ds_read_b64_tr_b16 v[16:17], v9 offset:0\n"
    "v_exp_f32 v141, v141\n"
    "v_mfma_f32_16x16x32_bf16 v[212:215], v[20:23], a[196:199], v[212:215]\n"
    "ds_read_b64_tr_b16 v[18:19], v9 offset:2048\n"
    "v_exp_f32 v142, v142\n"
    "v_mfma_f32_16x16x32_bf16 v[216:219], v[20:23], a[200:203], v[216:219]\n"
    "v_exp_f32 v143, v143\n"
    "v_fma_f32 v140, v140, v55, v55\n"
    "v_mfma_f32_16x16x32_bf16 v[220:223], v[20:23], a[204:207], v[220:223]\n"
    "v_fma_f32 v141, v141, v55, v55\n"
    "v_fma_f32 v142, v142, v55, v55\n"
    "s_waitcnt lgkmcnt(2)\n"
    "v_mfma_f32_16x16x32_bf16 v[192:195], v[24:27], a[208:211], v[192:195]\n"
    "ds_read_b64_tr_b16 v[20:21], v9 offset:8192\n"
    "v_fma_f32 v143, v143, v55, v55\n"
    "v_mul_f32 v144, v128, v144\n"
    "v_mfma_f32_16x16x32_bf16 v[196:199], v[24:27], a[212:215], v[196:199]\n"
    "ds_read_b64_tr_b16 v[22:23], v9 offset:10240\n"
    "v_rcp_f32 v140, v140\n"
    "v_mfma_f32_16x16x32_bf16 v[200:203], v[24:27], a[216:219], v[200:203]\n"
    "v_rcp_f32 v141, v141\n"
    "v_mul_f32 v145, v129, v145\n"
    "v_mfma_f32_16x16x32_bf16 v[204:207], v[24:27], a[220:223], v[204:207]\n"
    "v_rcp_f32 v142, v142\n"
    "v_mul_f32 v146, v130, v146\n"
    "v_mfma_f32_16x16x32_bf16 v[208:211], v[28:31], a[208:211], v[208:211]\n"
    "ds_read_b64_tr_b16 v[24:25], v9 offset:16384\n"
    "v_rcp_f32 v143, v143\n"
    "v_mfma_f32_16x16x32_bf16 v[212:215], v[28:31], a[212:215], v[212:215]\n"
    "ds_read_b64_tr_b16 v[26:27], v9 offset:18432\n"
    "v_mul_f32 v147, v131, v147\n"
    "v_cvt_pk_bf16_f32 v32, v144, v145\n"
    "v_mfma_f32_16x16x32_bf16 v[216:219], v[28:31], a[216:219], v[216:219]\n"
    "v_cvt_pk_bf16_f32 v33, v146, v147\n"
    "v_mul_f32 v148, v132, v148\n"
    "v_mul_f32 v149, v133, v149\n"
    "v_mfma_f32_16x16x32_bf16 v[220:223], v[28:31], a[220:223], v[220:223]\n"
    "v_mul_f32 v150, v134, v150\n"
    "v_mul_f32 v151, v135, v151\n"
    "s_waitcnt lgkmcnt(2)\n"
    "v_mfma_f32_16x16x32_bf16 v[160:163], v[16:19], a[128:131], v[160:163]\n"
    "ds_read_b64_tr_b16 v[28:29], v9 offset:24576\n"
    "v_cvt_pk_bf16_f32 v36, v148, v149\n"
    "v_cvt_pk_bf16_f32 v37, v150, v151\n"
    "v_mfma_f32_16x16x32_bf16 v[164:167], v[16:19], a[132:135], v[164:167]\n"
    "ds_read_b64_tr_b16 v[30:31], v9 offset:26624\n"
    "v_mul_f32 v152, v136, v152\n"
    "v_mul_f32 v153, v137, v153\n"
    "v_mfma_f32_16x16x32_bf16 v[168:171], v[16:19], a[136:139], v[168:171]\n"
    "v_mul_f32 v154, v138, v154\n"
    "v_mul_f32 v155, v139, v155\n"
    "v_cvt_pk_bf16_f32 v40, v152, v153\n"
    "v_mfma_f32_16x16x32_bf16 v[172:175], v[16:19], a[140:143], v[172:175]\n"
    "v_cvt_pk_bf16_f32 v41, v154, v155\n"
    "v_mul_f32 v156, v140, v156\n"
    "v_mul_f32 v157, v141, v157\n"
    "v_mfma_f32_16x16x32_bf16 v[160:163], v[20:23], a[144:147], v[160:163]\n"
    "ds_read_b64_tr_b16 v[16:17], v9 offset:32768\n"
    "v_mul_f32 v158, v142, v158\n"
    "v_mul_f32 v159, v143, v159\n"
    "v_mfma_f32_16x16x32_bf16 v[164:167], v[20:23], a[148:151], v[164:167]\n"
    "ds_read_b64_tr_b16 v[18:19], v9 offset:34816\n"
    "v_cvt_pk_bf16_f32 v44, v156, v157\n"
    "v_cvt_pk_bf16_f32 v45, v158, v159\n"
    "v_mfma_f32_16x16x32_bf16 v[168:171], v[20:23], a[152:155], v[168:171]\n"
    "v_mfma_f32_16x16x32_bf16 v[172:175], v[20:23], a[156:159], v[172:175]\n"
    "s_waitcnt lgkmcnt(2)\n"
    "v_mfma_f32_16x16x32_bf16 v[160:163], v[24:27], a[160:163], v[160:163]\n"
    "ds_read_b64_tr_b16 v[20:21], v9 offset:40960\n"
    "v_mfma_f32_16x16x32_bf16 v[164:167], v[24:27], a[164:167], v[164:167]\n"
    "ds_read_b64_tr_b16 v[22:23], v9 offset:43008\n"
    "v_mfma_f32_16x16x32_bf16 v[168:171], v[24:27], a[168:171], v[168:171]\n"
    "v_mfma_f32_16x16x32_bf16 v[172:175], v[24:27], a[172:175], v[172:175]\n"
    "v_mfma_f32_16x16x32_bf16 v[160:163], v[28:31], a[176:179], v[160:163]\n"
    "ds_read_b64_tr_b16 v[24:25], v9 offset:49152\n"
    "v_mfma_f32_16x16x32_bf16 v[164:167], v[28:31], a[180:183], v[164:167]\n"
    "ds_read_b64_tr_b16 v[26:27], v9 offset:51200\n"
    "v_mfma_f32_16x16x32_bf16 v[168:171], v[28:31], a[184:187], v[168:171]\n"
    "v_mfma_f32_16x16x32_bf16 v[172:175], v[28:31], a[188:191], v[172:175]\n"
    "s_waitcnt lgkmcnt(2)\n"
    "v_mfma_f32_16x16x32_bf16 v[160:163], v[16:19], a[192:195], v[160:163]\n"
    "ds_read_b64_tr_b16 v[28:29], v9 offset:57344\n"
    "v_mfma_f32_16x16x32_bf16 v[164:167], v[16:19], a[196:199], v[164:167]\n"
    "ds_read_b64_tr_b16 v[30:31], v9 offset:59392\n"
    "v_mfma_f32_16x16x32_bf16 v[168:171], v[16:19], a[200:203], v[168:171]\n"
    "v_mfma_f32_16x16x32_bf16 v[172:175], v[16:19], a[204:207], v[172:175]\n"
    "s_waitcnt vmcnt(0)\n"
    "v_cmp_ne_u32 vcc, s92, v60\n"
    "s_cbranch_vccnz LS8_k\n"
    "LB8_k:\n"
    "s_barrier\n"
    "buffer_store_dword v61, v12, s[44:47], 0 offen offset:2048 sc1\n"
    "buffer_store_dword v61, v12, s[44:47], 0 offen offset:512 sc1\n"
    "v_mfma_f32_16x16x32_bf16 v[160:163], v[20:23], a[208:211], v[160:163]\n"
    "v_add_u32 v9, 0x10000, v9\n"
    "ds_read_b64_tr_b16 v[16:17], v9 offset:0\n"
    "s_add_u32 m0, s53, 8192\n"
    "v_mfma_f32_16x16x32_bf16 v[164:167], v[20:23], a[212:215], v[164:167]\n"
    "ds_read_b64_tr_b16 v[18:19], v9 offset:2048\n"
    "s_add_u32 s79, s61, 294912\n"
    "v_mfma_f32_16x16x32_bf16 v[168:171], v[20:23], a[216:219], v[168:171]\n"
    "buffer_load_dwordx4 v15, s[40:43], s79 offen lds\n"
    "v_mfma_f32_16x16x32_bf16 v[172:175], v[20:23], a[220:223], v[172:175]\n"
    "s_waitcnt lgkmcnt(2)\n"
    "v_mfma_f32_16x16x32_bf16 v[160:163], v[24:27], a[224:227], v[160:163]\n"
    "ds_read_b64_tr_b16 v[20:21], v9 offset:8192\n"
    "v_mfma_f32_16x16x32_bf16 v[164:167], v[24:27], a[228:231], v[164:167]\n"
    "ds_read_b64_tr_b16 v[22:23], v9 offset:10240\n"
    "v_mfma_f32_16x16x32_bf16 v[168:171], v[24:27], a[232:235], v[168:171]\n"
    "v_mfma_f32_16x16x32_bf16 v[172:175], v[24:27], a[236:239], v[172:175]\n"
    "v_mfma_f32_16x16x32_bf16 v[160:163], v[28:31], a[240:243], v[160:163]\n"
    "v_add_u32 v9, 0xffff0000, v9\n"
    "ds_read_b64_tr_b16 v[24:25], v9 offset:4096\n"
    "v_mfma_f32_16x16x32_bf16 v[164:167], v[28:31], a[244:247], v[164:167]\n"
    "ds_read_b64_tr_b16 v[26:27], v9 offset:6144\n"
    "v_mfma_f32_16x16x32_bf16 v[168:171], v[28:31], a[248:251], v[168:171]\n"
    "v_mfma_f32_16x16x32_bf16 v[172:175], v[28:31], a[252:255], v[172:175]\n"
    "s_waitcnt lgkmcnt(2)\n"
    "v_mfma_f32_16x16x32_bf16 v[160:163], v[16:19], v[64:67], v[160:163]\n"
    "ds_read_b64_tr_b16 v[28:29], v9 offset:12288\n"
    "v_mfma_f32_16x16x32_bf16 v[164:167], v[16:19], v[68:71], v[164:167]\n"
    "ds_read_b64_tr_b16 v[30:31], v9 offset:14336\n"
    "v_mfma_f32_16x16x32_bf16 v[168:171], v[16:19], v[72:75], v[168:171]\n"
    "v_mfma_f32_16x16x32_bf16 v[172:175], v[16:19], v[76:79], v[172:175]\n"
    "v_mfma_f32_16x16x32_bf16 v[160:163], v[20:23], v[80:83], v[160:163]\n"
    "ds_read_b64_tr_b16 v[16:17], v9 offset:20480\n"
    "v_mfma_f32_16x16x32_bf16 v[164:167], v[20:23], v[84:87], v[164:167]\n"
    "ds_read_b64_tr_b16 v[18:19], v9 offset:22528\n"
    "v_mfma_f32_16x16x32_bf16 v[168:171], v[20:23], v[88:91], v[168:171]\n"
    "v_mfma_f32_16x16x32_bf16 v[172:175], v[20:23], v[92:95], v[172:175]\n"
    "s_waitcnt lgkmcnt(2)\n"
    "v_mfma_f32_16x16x32_bf16 v[176:179], v[24:27], a[128:131], v[176:179]\n"
    "ds_read_b64_tr_b16 v[20:21], v9 offset:28672\n"
    "v_mfma_f32_16x16x32_bf16 v[180:183], v[24:27], a[132:135], v[180:183]\n"
    "ds_read_b64_tr_b16 v[22:23], v9 offset:30720\n"
    "v_mfma_f32_16x16x32_bf16 v[184:187], v[24:27], a[136:139], v[184:187]\n"
    "v_mfma_f32_16x16x32_bf16 v[188:191], v[24:27], a[140:143], v[188:191]\n"
    "s_add_u32 s33, s63, 24576\n"
    "buffer_load_dwordx4 a[128:131], v0, s[44:47], s33 offen offset:0 sc1\n"
    "v_mfma_f32_16x16x32_bf16 v[176:179], v[28:31], a[144:147], v[176:179]\n"
    "ds_read_b64_tr_b16 v[24:25], v9 offset:36864\n"
    "v_mul_f32 v160, v48, v160\n"
    "v_mul_f32 v161, v48, v161\n"
    "v_mul_f32 v162, v48, v162\n"
    "v_mfma_f32_16x16x32_bf16 v[180:183], v[28:31], a[148:151], v[180:183]\n"
    "ds_read_b64_tr_b16 v[26:27], v9 offset:38912\n"
    "v_mul_f32 v163, v48, v163\n"
    "v_exp_f32 v160, v160\n"
    "v_mfma_f32_16x16x32_bf16 v[184:187], v[28:31], a[152:155], v[184:187]\n"
    "buffer_load_dwordx4 a[132:135], v0, s[44:47], s33 offen offset:1024 sc1\n"
    "v_exp_f32 v161, v161\n"
    "v_mfma_f32_16x16x32_bf16 v[188:191], v[28:31], a[156:159], v[188:191]\n"
    "buffer_load_dwordx4 a[136:139], v0, s[44:47], s33 offen offset:2048 sc1\n"
    "v_fma_f32 v160, v160, v52, v52\n"
    "s_waitcnt lgkmcnt(2)\n"
    "v_mfma_f32_16x16x32_bf16 v[176:179], v[16:19], a[160:163], v[176:179]\n"
    "ds_read_b64_tr_b16 v[28:29], v9 offset:45056\n"
    "v_exp_f32 v162, v162\n"
    "v_fma_f32 v161, v161, v52, v52\n"
    "v_mfma_f32_16x16x32_bf16 v[180:183], v[16:19], a[164:167], v[180:183]\n"
    "ds_read_b64_tr_b16 v[30:31], v9 offset:47104\n"
    "v_exp_f32 v163, v163\n"
    "v_fma_f32 v162, v162, v52, v52\n"
    "v_mfma_f32_16x16x32_bf16 v[184:187], v[16:19], a[168:171], v[184:187]\n"
    "buffer_load_dwordx4 a[140:143], v0, s[44:47], s33 offen offset:3072 sc1\n"
    "v_fma_f32 v163, v163, v52, v52\n"
    "v_mul_f32 v164, v49, v164\n"
    "v_mfma_f32_16x16x32_bf16 v[188:191], v[16:19], a[172:175], v[188:191]\n"
    "v_rcp_f32 v160, v160\n"
    "v_rcp_f32 v161, v161\n"
    "v_mfma_f32_16x16x32_bf16 v[176:179], v[20:23], a[176:179], v[176:179]\n"
    "ds_read_b64_tr_b16 v[16:17], v9 offset:53248\n"
    "v_rcp_f32 v162, v162\n"
    "v_mul_f32 v165, v49, v165\n"
    "v_mfma_f32_16x16x32_bf16 v[180:183], v[20:23], a[180:183], v[180:183]\n"
    "ds_read_b64_tr_b16 v[18:19], v9 offset:55296\n"
    "v_rcp_f32 v163, v163\n"
    "v_mul_f32 v166, v49, v166\n"
    "v_mfma_f32_16x16x32_bf16 v[184:187], v[20:23], a[184:187], v[184:187]\n"
    "s_add_u32 s33, s63, 28672\n"
    "buffer_load_dwordx4 a[144:147], v0, s[44:47], s33 offen offset:0 sc1\n"
    "v_mul_f32 v167, v49, v167\n"
    "v_mfma_f32_16x16x32_bf16 v[188:191], v[20:23], a[188:191], v[188:191]\n"
    "v_exp_f32 v164, v164\n"
    "v_mul_f32 v168, v50, v168\n"
    "s_waitcnt lgkmcnt(2)\n"
    "v_mfma_f32_16x16x32_bf16 v[176:179], v[24:27], a[192:195], v[176:179]\n"
    "ds_read_b64_tr_b16 v[20:21], v9 offset:61440\n"
    "v_exp_f32 v165, v165\n"
    "v_fma_f32 v164, v164, v53, v53\n"
    "v_mfma_f32_16x16x32_bf16 v[180:183], v[24:27], a[196:199], v[180:183]\n"
    "ds_read_b64_tr_b16 v[22:23], v9 offset:63488\n"
    "v_exp_f32 v166, v166\n"
    "v_fma_f32 v165, v165, v53, v53\n"
    "v_mfma_f32_16x16x32_bf16 v[184:187], v[24:27], a[200:203], v[184:187]\n"
    "buffer_load_dwordx4 a[148:151], v0, s[44:47], s33 offen offset:1024 sc1\n"
    "v_exp_f32 v167, v167\n"
    "v_mfma_f32_16x16x32_bf16 v[188:191], v[24:27], a[204:207], v[188:191]\n"
    "v_fma_f32 v166, v166, v53, v53\n"
    "v_fma_f32 v167, v167, v53, v53\n"
    "v_rcp_f32 v164, v164\n"
    "v_mfma_f32_16x16x32_bf16 v[176:179], v[28:31], a[208:211], v[176:179]\n"
    "v_add_u32 v9, 0x10000, v9\n"
    "ds_read_b64_tr_b16 v[24:25], v9 offset:4096\n"
    "v_rcp_f32 v165, v165\n"
    "v_mul_f32 v169, v50, v169\n"
    "v_mfma_f32_16x16x32_bf16 v[180:183], v[28:31], a[212:215], v[180:183]\n"
    "ds_read_b64_tr_b16 v[26:27], v9 offset:6144\n"
    "v_rcp_f32 v166, v166\n"
    "v_mul_f32 v170, v50, v170\n"
    "v_mfma_f32_16x16x32_bf16 v[184:187], v[28:31], a[216:219], v[184:187]\n"
    "buffer_load_dwordx4 a[152:155], v0, s[44:47], s33 offen offset:2048 sc1\n"
    "v_rcp_f32 v167, v167\n"
    "v_mfma_f32_16x16x32_bf16 v[188:191], v[28:31], a[220:223], v[188:191]\n"
    "v_mul_f32 v171, v50, v171\n"
    "v_mul_f32 v172, v51, v172\n"
    "s_waitcnt lgkmcnt(2)\n"
    "v_mfma_f32_16x16x32_bf16 v[176:179], v[16:19], a[224:227], v[176:179]\n"
    "ds_read_b64_tr_b16 v[28:29], v9 offset:12288\n"
    "v_exp_f32 v168, v168\n"
    "v_mfma_f32_16x16x32_bf16 v[180:183], v[16:19], a[228:231], v[180:183]\n"
    "ds_read_b64_tr_b16 v[30:31], v9 offset:14336\n"
    "v_exp_f32 v169, v169\n"
    "v_mfma_f32_16x16x32_bf16 v[184:187], v[16:19], a[232:235], v[184:187]\n"
    "buffer_load_dwordx4 a[156:159], v0, s[44:47], s33 offen offset:3072 sc1\n"
    "v_fma_f32 v168, v168, v54, v54\n"
    "v_mfma_f32_16x16x32_bf16 v[188:191], v[16:19], a[236:239], v[188:191]\n"
    "v_exp_f32 v170, v170\n"
    "v_fma_f32 v169, v169, v54, v54\n"
    "v_mfma_f32_16x16x32_bf16 v[176:179], v[20:23], a[240:243], v[176:179]\n"
    "ds_read_b64_tr_b16 v[16:17], v10 offset:12288\n"
    "v_exp_f32 v171, v171\n"
    "v_mfma_f32_16x16x32_bf16 v[180:183], v[20:23], a[244:247], v[180:183]\n"
    "ds_read_b64_tr_b16 v[18:19], v10 offset:12800\n"
    "v_fma_f32 v170, v170, v54, v54\n"
    "v_fma_f32 v171, v171, v54, v54\n"
    "v_mfma_f32_16x16x32_bf16 v[184:187], v[20:23], a[248:251], v[184:187]\n"
    "v_rcp_f32 v168, v168\n"
    "v_mul_f32 v173, v51, v173\n"
    "v_mfma_f32_16x16x32_bf16 v[188:191], v[20:23], a[252:255], v[188:191]\n"
    "v_rcp_f32 v169, v169\n"
    "s_waitcnt lgkmcnt(2)\n"
    "v_mfma_f32_16x16x32_bf16 v[176:179], v[24:27], v[64:67], v[176:179]\n"
    "ds_read_b64_tr_b16 v[20:21], v10 offset:13312\n"
    "v_rcp_f32 v170, v170\n"
    "v_mfma_f32_16x16x32_bf16 v[180:183], v[24:27], v[68:71], v[180:183]\n"
    "ds_read_b64_tr_b16 v[22:23], v10 offset:13824\n"
    "v_rcp_f32 v171, v171\n"
    "v_mfma_f32_16x16x32_bf16 v[184:187], v[24:27], v[72:75], v[184:187]\n"
    "v_mul_f32 v174, v51, v174\n"
    "v_mul_f32 v175, v51, v175\n"
    "v_mfma_f32_16x16x32_bf16 v[188:191], v[24:27], v[76:79], v[188:191]\n"
    "v_exp_f32 v172, v172\n"
    "v_mfma_f32_16x16x32_bf16 v[176:179], v[28:31], v[80:83], v[176:179]\n"
    "ds_read_b64_tr_b16 v[24:25], v10 offset:14336\n"
    "v_exp_f32 v173, v173\n"
    "v_mfma_f32_16x16x32_bf16 v[180:183], v[28:31], v[84:87], v[180:183]\n"
    "ds_read_b64_tr_b16 v[26:27], v10 offset:14848\n"
    "v_exp_f32 v174, v174\n"
    "v_mfma_f32_16x16x32_bf16 v[184:187], v[28:31], v[88:91], v[184:187]\n"
    "v_exp_f32 v175, v175\n"
    "v_fma_f32 v172, v172, v55, v55\n"
    "v_mfma_f32_16x16x32_bf16 v[188:191], v[28:31], v[92:95], v[188:191]\n"
    "v_fma_f32 v173, v173, v55, v55\n"
    "v_fma_f32 v174, v174, v55, v55\n"
    "s_waitcnt lgkmcnt(2)\n"
    "v_mfma_f32_16x16x32_bf16 v[192:195], v[16:19], a[224:227], v[192:195]\n"
    "ds_read_b64_tr_b16 v[28:29], v10 offset:15360\n"
    "v_fma_f32 v175, v175, v55, v55\n"
    "v_mfma_f32_16x16x32_bf16 v[196:199], v[16:19], a[228:231], v[196:199]\n"
    "ds_read_b64_tr_b16 v[30:31], v10 offset:15872\n"
    "v_rcp_f32 v172, v172\n"
    "v_mfma_f32_16x16x32_bf16 v[200:203], v[16:19], a[232:235], v[200:203]\n"
    "v_rcp_f32 v173, v173\n"
    "v_mfma_f32_16x16x32_bf16 v[204:207], v[16:19], a[236:239], v[204:207]\n"
    "s_barrier\n"
    "v_mfma_f32_16x16x32_bf16 v[208:211], v[20:23], a[224:227], v[208:211]\n"
    "ds_read_b64_tr_b16 v[16:17], v10 offset:0\n"
    "s_add_u32 m0, s52, 65536\n"
    "v_mul_f32 v176, v160, v176\n"
    "v_mfma_f32_16x16x32_bf16 v[212:215], v[20:23], a[228:231], v[212:215]\n"
    "ds_read_b64_tr_b16 v[18:19], v10 offset:512\n"
    "s_add_u32 s79, s59, 36864\n"
    "v_mul_f32 v177, v161, v177\n"
    "v_mfma_f32_16x16x32_bf16 v[216:219], v[20:23], a[232:235], v[216:219]\n"
    "buffer_load_dwordx4 v14, s[40:43], s79 offen lds\n"
    "v_mfma_f32_16x16x32_bf16 v[220:223], v[20:23], a[236:239], v[220:223]\n"
    "buffer_load_dwordx4 v2, s[40:43], s79 offen offset:1024 lds\n"
    "s_waitcnt lgkmcnt(2)\n"
    "v_mfma_f32_16x16x32_bf16 v[192:195], v[24:27], a[240:243], v[192:195]\n"
    "ds_read_b64_tr_b16 v[20:21], v10 offset:1024\n"
    "buffer_load_dwordx4 v3, s[40:43], s79 offen offset:2048 lds\n"
    "v_mfma_f32_16x16x32_bf16 v[196:199], v[24:27], a[244:247], v[196:199]\n"
    "ds_read_b64_tr_b16 v[22:23], v10 offset:1536\n"
    "v_rcp_f32 v174, v174\n"
    "v_mfma_f32_16x16x32_bf16 v[200:203], v[24:27], a[248:251], v[200:203]\n"
    "buffer_load_dword v60, v11, s[44:47], 0 offen offset:512 sc1\n"
    "v_mul_f32 v178, v162, v178\n"
    "v_mfma_f32_16x16x32_bf16 v[204:207], v[24:27], a[252:255], v[204:207]\n"
    "buffer_load_dwordx4 v4, s[40:43], s79 offen offset:3072 lds\n"
    "v_mfma_f32_16x16x32_bf16 v[208:211], v[28:31], a[240:243], v[208:211]\n"
    "ds_read_b64_tr_b16 v[24:25], v10 offset:2048\n"
    "v_rcp_f32 v175, v175\n"
    "v_mfma_f32_16x16x32_bf16 v[212:215], v[28:31], a[244:247], v[212:215]\n"
    "ds_read_b64_tr_b16 v[26:27], v10 offset:2560\n"
    "v_mul_f32 v179, v163, v179\n"
    "v_cvt_pk_bf16_f32 v34, v176, v177\n"
    "v_mfma_f32_16x16x32_bf16 v[216:219], v[28:31], a[248:251], v[216:219]\n"
    "v_cvt_pk_bf16_f32 v35, v178, v179\n"
    "s_add_u32 s35, s57, 49152\n"
    "v_mul_f32 v180, v164, v180\n"
    "v_mfma_f32_16x16x32_bf16 v[220:223], v[28:31], a[252:255], v[220:223]\n"
    "buffer_store_dwordx4 v[32:35], v0, s[44:47], s35 offen offset:0 sc1\n"
    "s_waitcnt lgkmcnt(2)\n"
    "v_mfma_f32_16x16x32_bf16 v[192:195], v[16:19], v[64:67], v[192:195]\n"
    "ds_read_b64_tr_b16 v[28:29], v10 offset:3072\n"
    "v_mul_f32 v181, v165, v181\n"
    "v_mul_f32 v182, v166, v182\n"
    "v_mfma_f32_16x16x32_bf16 v[196:199], v[16:19], v[68:71], v[196:199]\n"
    "ds_read_b64_tr_b16 v[30:31], v10 offset:3584\n"
    "v_mul_f32 v183, v167, v183\n"
    "v_cvt_pk_bf16_f32 v38, v180, v181\n"
    "v_mfma_f32_16x16x32_bf16 v[200:203], v[16:19], v[72:75], v[200:203]\n"
    "v_cvt_pk_bf16_f32 v39, v182, v183\n"
    "v_mul_f32 v184, v168, v184\n"
    "v_mul_f32 v185, v169, v185\n"
    "v_mfma_f32_16x16x32_bf16 v[204:207], v[16:19], v[76:79], v[204:207]\n"
    "v_mul_f32 v186, v170, v186\n"
    "v_mul_f32 v187, v171, v187\n"
    "v_cvt_pk_bf16_f32 v42, v184, v185\n"
    "v_mfma_f32_16x16x32_bf16 v[208:211], v[20:23], v[64:67], v[208:211]\n"
    "v_cvt_pk_bf16_f32 v43, v186, v187\n"
    "s_add_u32 s76, s57, 344064\n"
    "v_mul_f32 v188, v172, v188\n"
    "v_mfma_f32_16x16x32_bf16 v[212:215], v[20:23], v[68:71], v[212:215]\n"
    "buffer_store_dwordx4 v[36:39], v0, s[44:47], s35 offen offset:1024 sc1\n"
    "v_mul_f32 v189, v173, v189\n"
    "v_mfma_f32_16x16x32_bf16 v[216:219], v[20:23], v[72:75], v[216:219]\n"
    "v_add_u32 v6, 0xfffe0000, v6\n"
    "ds_read_b64_tr_b16 v[232:233], v6 offset:4096\n"
    "v_mul_f32 v190, v174, v190\n"
    "v_mul_f32 v191, v175, v191\n"
    "v_mfma_f32_16x16x32_bf16 v[220:223], v[20:23], v[76:79], v[220:223]\n"
    "ds_read_b64_tr_b16 v[234:235], v6 offset:6144\n"
    "v_cvt_pk_bf16_f32 v46, v188, v189\n"
    "s_waitcnt lgkmcnt(2)\n"
    "v_mfma_f32_16x16x32_bf16 v[192:195], v[24:27], v[80:83], v[192:195]\n"
    "v_add_u32 v7, 0xfffe0000, v7\n"
    "ds_read_b64_tr_b16 v[236:237], v7 offset:4096\n"
    "v_cvt_pk_bf16_f32 v47, v190, v191\n"
    "v_mfma_f32_16x16x32_bf16 v[196:199], v[24:27], v[84:87], v[196:199]\n"
    "ds_read_b64_tr_b16 v[238:239], v7 offset:6144\n"
    "v_mfma_f32_16x16x32_bf16 v[200:203], v[24:27], v[88:91], v[200:203]\n"
    "v_add_u32 v8, 0xfffe0000, v8\n"
    "ds_read_b64_tr_b16 v[16:17], v8 offset:4096\n"
    "v_mfma_f32_16x16x32_bf16 v[204:207], v[24:27], v[92:95], v[204:207]\n"
    "ds_read_b64_tr_b16 v[18:19], v8 offset:6144\n"
    "buffer_store_dwordx4 v[40:43], v0, s[44:47], s76 offen offset:0 sc1\n"
    "v_mfma_f32_16x16x32_bf16 v[208:211], v[28:31], v[80:83], v[208:211]\n"
    "v_add_u32 v9, 0xfffe0000, v9\n"
    "ds_read_b64_tr_b16 v[20:21], v9 offset:4096\n"
    "v_mfma_f32_16x16x32_bf16 v[212:215], v[28:31], v[84:87], v[212:215]\n"
    "ds_read_b64_tr_b16 v[22:23], v9 offset:6144\n"
    "v_mfma_f32_16x16x32_bf16 v[216:219], v[28:31], v[88:91], v[216:219]\n"
    "ds_read_b64_tr_b16 v[24:25], v8 offset:0\n"
    "v_mfma_f32_16x16x32_bf16 v[220:223], v[28:31], v[92:95], v[220:223]\n"
    "ds_read_b64_tr_b16 v[26:27], v8 offset:2048\n"
    "s_waitcnt lgkmcnt(6)\n"
    "v_mfma_f32_16x16x32_bf16 v[64:67], v[232:235], a[0:3], 0\n"
    "ds_read_b64_tr_b16 v[28:29], v9 offset:0\n"
    "v_lshrrev_b32 v62, 1, v0\n"
    "v_and_b32 v63, 0xf0, v0\n"
    "v_mfma_f32_16x16x32_bf16 v[68:71], v[232:235], a[4:7], 0\n"
    "ds_read_b64_tr_b16 v[30:31], v9 offset:2048\n"
    "buffer_store_dwordx4 v[44:47], v0, s[44:47], s76 offen offset:1024 sc1\n"
    "v_mfma_f32_16x16x32_bf16 v[72:75], v[236:239], a[0:3], 0\n"
    "ds_read_b64_tr_b16 v[232:233], v10 offset:4096\n"
    "v_mul_f32 v192, v48, v192\n"
    "v_mul_f32 v193, v48, v193\n"
    "v_mfma_f32_16x16x32_bf16 v[76:79], v[236:239], a[4:7], 0\n"
    "ds_read_b64_tr_b16 v[234:235], v10 offset:4608\n"
    "v_mul_f32 v194, v48, v194\n"
    "s_waitcnt lgkmcnt(6)\n"
    "v_mfma_f32_16x16x32_bf16 v[80:83], v[16:19], a[0:3], 0\n"
    "ds_read_b64_tr_b16 v[236:237], v6 offset:0\n"
    "v_mul_f32 v195, v48, v195\n"
    "v_mul_f32 v196, v49, v196\n"
    "v_mfma_f32_16x16x32_bf16 v[84:87], v[16:19], a[4:7], 0\n"
    "ds_read_b64_tr_b16 v[238:239], v6 offset:2048\n"
    "v_exp_f32 v192, v192\n"
    "v_mfma_f32_16x16x32_bf16 v[88:91], v[20:23], a[0:3], 0\n"
    "ds_read_b64_tr_b16 v[16:17], v7 offset:0\n"
    "v_exp_f32 v193, v193\n"
    "v_mfma_f32_16x16x32_bf16 v[92:95], v[20:23], a[4:7], 0\n"
    "ds_read_b64_tr_b16 v[18:19], v7 offset:2048\n"
    "v_fma_f32 v192, v192, v52, v52\n"
    "s_waitcnt lgkmcnt(6)\n"
    "v_mfma_f32_16x16x32_bf16 v[96:99], v[24:27], a[0:3], 0\n"
    "ds_read_b64_tr_b16 v[20:21], v6 offset:12288\n"
    "v_exp_f32 v194, v194\n"
    "v_mfma_f32_16x16x32_bf16 v[100:103], v[24:27], a[4:7], 0\n"
    "ds_read_b64_tr_b16 v[22:23], v6 offset:14336\n"
    "v_exp_f32 v195, v195\n"
    "v_mfma_f32_16x16x32_bf16 v[104:107], v[28:31], a[0:3], 0\n"
    "ds_read_b64_tr_b16 v[24:25], v7 offset:12288\n"
    "v_fma_f32 v193, v193, v52, v52\n"
    "v_fma_f32 v194, v194, v52, v52\n"
    "v_mfma_f32_16x16x32_bf16 v[108:111], v[28:31], a[4:7], 0\n"
    "ds_read_b64_tr_b16 v[26:27], v7 offset:14336\n"
    "v_fma_f32 v195, v195, v52, v52\n"
    "s_waitcnt lgkmcnt(6)\n"
    "v_mfma_f32_16x16x32_bf16 v[224:227], v[232:235], a[0:3], 0\n"
    "ds_read_b64_tr_b16 v[28:29], v8 offset:12288\n"
    "v_rcp_f32 v192, v192\n"
    "v_mfma_f32_16x16x32_bf16 v[228:231], v[232:235], a[4:7], 0\n"
    "ds_read_b64_tr_b16 v[30:31], v8 offset:14336\n"
    "v_rcp_f32 v193, v193\n"
    "v_mfma_f32_16x16x32_bf16 v[112:115], v[236:239], a[0:3], 0\n"
    "ds_read_b64_tr_b16 v[232:233], v9 offset:12288\n"
    "s_add_u32 m0, s52, 81920\n"
    "s_add_u32 s79, s59, 184320\n"
    "v_mfma_f32_16x16x32_bf16 v[116:119], v[236:239], a[4:7], 0\n"
    "ds_read_b64_tr_b16 v[234:235], v9 offset:14336\n"
    "v_mul_f32 v197, v49, v197\n"
    "s_waitcnt lgkmcnt(6)\n"
    "v_mfma_f32_16x16x32_bf16 v[120:123], v[16:19], a[0:3], 0\n"
    "ds_read_b64_tr_b16 v[236:237], v8 offset:8192\n"
    "buffer_load_dwordx4 v14, s[40:43], s79 offen lds\n"
    "v_mfma_f32_16x16x32_bf16 v[124:127], v[16:19], a[4:7], 0\n"
    "ds_read_b64_tr_b16 v[238:239], v8 offset:10240\n"
    "buffer_load_dwordx4 v2, s[40:43], s79 offen offset:1024 lds\n"
    "v_mfma_f32_16x16x32_bf16 v[64:67], v[20:23], a[8:11], v[64:67]\n"
    "ds_read_b64_tr_b16 v[16:17], v9 offset:8192\n"
    "v_rcp_f32 v194, v194\n"
    "v_mfma_f32_16x16x32_bf16 v[68:71], v[20:23], a[12:15], v[68:71]\n"
    "ds_read_b64_tr_b16 v[18:19], v9 offset:10240\n"
    "v_mul_f32 v198, v49, v198\n"
    "s_waitcnt lgkmcnt(6)\n"
    "v_mfma_f32_16x16x32_bf16 v[72:75], v[24:27], a[8:11], v[72:75]\n"
    "ds_read_b64_tr_b16 v[20:21], v10 offset:5120\n"
    "v_rcp_f32 v195, v195\n"
    "v_mfma_f32_16x16x32_bf16 v[76:79], v[24:27], a[12:15], v[76:79]\n"
    "ds_read_b64_tr_b16 v[22:23], v10 offset:5632\n"
    "buffer_load_dwordx4 v3, s[40:43], s79 offen offset:2048 lds\n"
    "v_mfma_f32_16x16x32_bf16 v[80:83], v[28:31], a[8:11], v[80:83]\n"
    "ds_read_b64_tr_b16 v[24:25], v6 offset:8192\n"
    "v_mul_f32 v199, v49, v199\n"
    "v_mul_f32 v200, v50, v200\n"
    "v_mfma_f32_16x16x32_bf16 v[84:87], v[28:31], a[12:15], v[84:87]\n"
    "ds_read_b64_tr_b16 v[26:27], v6 offset:10240\n"
    "v_mul_f32 v201, v50, v201\n"
    "s_waitcnt lgkmcnt(6)\n"
    "v_mfma_f32_16x16x32_bf16 v[88:91], v[232:235], a[8:11], v[88:91]\n"
    "ds_read_b64_tr_b16 v[28:29], v7 offset:8192\n"
    "v_exp_f32 v196, v196\n"
    "v_mfma_f32_16x16x32_bf16 v[92:95], v[232:235], a[12:15], v[92:95]\n"
    "ds_read_b64_tr_b16 v[30:31], v7 offset:10240\n"
    "buffer_load_dwordx4 v4, s[40:43], s79 offen offset:3072 lds\n"
    "v_mfma_f32_16x16x32_bf16 v[96:99], v[236:239], a[8:11], v[96:99]\n"
    "ds_read_b64_tr_b16 v[232:233], v6 offset:20480\n"
    "v_exp_f32 v197, v197\n"
    "v_mfma_f32_16x16x32_bf16 v[100:103], v[236:239], a[12:15], v[100:103]\n"
    "ds_read_b64_tr_b16 v[234:235], v6 offset:22528\n"
    "v_fma_f32 v196, v196, v53, v53\n"
    "s_waitcnt lgkmcnt(6)\n"
    "v_mfma_f32_16x16x32_bf16 v[104:107], v[16:19], a[8:11], v[104:107]\n"
    "ds_read_b64_tr_b16 v[236:237], v7 offset:20480\n"
    "s_add_u32 m0, s52, 98304\n"
    "v_fma_f32 v197, v197, v53, v53\n"
    "v_mfma_f32_16x16x32_bf16 v[108:111], v[16:19], a[12:15], v[108:111]\n"
    "ds_read_b64_tr_b16 v[238:239], v7 offset:22528\n"
    "s_add_u32 s79, s59, 331776\n"
    "v_mul_f32 v202, v50, v202\n"
    "v_mfma_f32_16x16x32_bf16 v[224:227], v[20:23], a[8:11], v[224:227]\n"
    "ds_read_b64_tr_b16 v[16:17], v8 offset:20480\n"
    "buffer_load_dwordx4 v14, s[40:43], s79 offen lds\n"
    "v_mfma_f32_16x16x32_bf16 v[228:231], v[20:23], a[12:15], v[228:231]\n"
    "ds_read_b64_tr_b16 v[18:19], v8 offset:22528\n"
    "v_mul_f32 v203, v50, v203\n"
    "s_waitcnt lgkmcnt(6)\n"
    "v_mfma_f32_16x16x32_bf16 v[112:115], v[24:27], a[8:11], v[112:115]\n"
    "ds_read_b64_tr_b16 v[20:21], v9 offset:20480\n"
    "buffer_load_dwordx4 v2, s[40:43], s79 offen offset:1024 lds\n"
    "v_mfma_f32_16x16x32_bf16 v[116:119], v[24:27], a[12:15], v[116:119]\n"
    "ds_read_b64_tr_b16 v[22:23], v9 offset:22528\n"
    "v_exp_f32 v198, v198\n"
    "v_mfma_f32_16x16x32_bf16 v[120:123], v[28:31], a[8:11], v[120:123]\n"
    "ds_read_b64_tr_b16 v[24:25], v8 offset:16384\n"
    "v_exp_f32 v199, v199\n"
    "v_mfma_f32_16x16x32_bf16 v[124:127], v[28:31], a[12:15], v[124:127]\n"
    "ds_read_b64_tr_b16 v[26:27], v8 offset:18432\n"
    "s_waitcnt vmcnt(11)\n"
    "v_cmp_ne_u32 vcc, s93, v60\n"
    "s_cbranch_vccnz LS2_k\n"
    "LB2_k:\n"
    "s_barrier\n"
    "s_waitcnt lgkmcnt(6)\n"
    "v_mfma_f32_16x16x32_bf16 v[64:67], v[232:235], a[16:19], v[64:67]\n"
    "ds_read_b64_tr_b16 v[28:29], v9 offset:16384\n"
    "buffer_load_dwordx4 v3, s[40:43], s79 offen offset:2048 lds\n"
    "v_mfma_f32_16x16x32_bf16 v[68:71], v[232:235], a[20:23], v[68:71]\n"
    "ds_read_b64_tr_b16 v[30:31], v9 offset:18432\n"
    "v_fma_f32 v198, v198, v53, v53\n"
    "v_fma_f32 v199, v199, v53, v53\n"
    "v_mfma_f32_16x16x32_bf16 v[72:75], v[236:239], a[16:19], v[72:75]\n"
    "ds_read_b64_tr_b16 v[232:233], v10 offset:6144\n"
    "v_rcp_f32 v196, v196\n"
    "v_mfma_f32_16x16x32_bf16 v[76:79], v[236:239], a[20:23], v[76:79]\n"
    "ds_read_b64_tr_b16 v[234:235], v10 offset:6656\n"
    "s_waitcnt lgkmcnt(6)\n"
    "v_mfma_f32_16x16x32_bf16 v[80:83], v[16:19], a[16:19], v[80:83]\n"
    "ds_read_b64_tr_b16 v[236:237], v6 offset:16384\n"
    "buffer_load_dwordx4 v4, s[40:43], s79 offen offset:3072 lds\n"
    "v_mfma_f32_16x16x32_bf16 v[84:87], v[16:19], a[20:23], v[84:87]\n"
    "ds_read_b64_tr_b16 v[238:239], v6 offset:18432\n"
    "v_rcp_f32 v197, v197\n"
    "v_mfma_f32_16x16x32_bf16 v[88:91], v[20:23], a[16:19], v[88:91]\n"
    "ds_read_b64_tr_b16 v[16:17], v7 offset:16384\n"
    "v_rcp_f32 v198, v198\n"
    "v_mfma_f32_16x16x32_bf16 v[92:95], v[20:23], a[20:23], v[92:95]\n"
    "ds_read_b64_tr_b16 v[18:19], v7 offset:18432\n"
    "s_waitcnt lgkmcnt(6)\n"
    "v_mfma_f32_16x16x32_bf16 v[96:99], v[24:27], a[16:19], v[96:99]\n"
    "ds_read_b64_tr_b16 v[20:21], v6 offset:28672\n"
    "s_add_u32 m0, s53, 0\n"
    "v_mfma_f32_16x16x32_bf16 v[100:103], v[24:27], a[20:23], v[100:103]\n"
    "ds_read_b64_tr_b16 v[22:23], v6 offset:30720\n"
    "s_add_u32 s79, s61, 36864\n"
    "v_mfma_f32_16x16x32_bf16 v[104:107], v[28:31], a[16:19], v[104:107]\n"
    "ds_read_b64_tr_b16 v[24:25], v7 offset:28672\n"
    "buffer_load_dwordx4 v15, s[40:43], s79 offen lds\n"
    "v_mfma_f32_16x16x32_bf16 v[108:111], v[28:31], a[20:23], v[108:111]\n"
    "ds_read_b64_tr_b16 v[26:27], v7 offset:30720\n"
    "s_waitcnt lgkmcnt(6)\n"
    "v_mfma_f32_16x16x32_bf16 v[224:227], v[232:235], a[16:19], v[224:227]\n"
    "ds_read_b64_tr_b16 v[28:29], v8 offset:28672\n"
    "v_rcp_f32 v199, v199\n"
    "v_mfma_f32_16x16x32_bf16 v[228:231], v[232:235], a[20:23], v[228:231]\n"
    "ds_read_b64_tr_b16 v[30:31], v8 offset:30720\n"
    "v_exp_f32 v200, v200\n"
    "v_mfma_f32_16x16x32_bf16 v[112:115], v[236:239], a[16:19], v[112:115]\n"
    "ds_read_b64_tr_b16 v[232:233], v9 offset:28672\n"
    "v_exp_f32 v201, v201\n"
    "v_mfma_f32_16x16x32_bf16 v[116:119], v[236:239], a[20:23], v[116:119]\n"
    "ds_read_b64_tr_b16 v[234:235], v9 offset:30720\n"
    "v_fma_f32 v200, v200, v54, v54\n"
    "s_waitcnt lgkmcnt(6)\n"
    "v_mfma_f32_16x16x32_bf16 v[120:123], v[16:19], a[16:19], v[120:123]\n"
    "ds_read_b64_tr_b16 v[236:237], v8 offset:24576\n"
    "v_exp_f32 v202, v202\n"
    "v_mfma_f32_16x16x32_bf16 v[124:127], v[16:19], a[20:23], v[124:127]\n"
    "ds_read_b64_tr_b16 v[238:239], v8 offset:26624\n"
    "v_exp_f32 v203, v203\n"
    "v_mfma_f32_16x16x32_bf16 v[64:67], v[20:23], a[24:27], v[64:67]\n"
    "ds_read_b64_tr_b16 v[16:17], v9 offset:24576\n"
    "v_fma_f32 v201, v201, v54, v54\n"
    "v_fma_f32 v202, v202, v54, v54\n"
    "v_mfma_f32_16x16x32_bf16 v[68:71], v[20:23], a[28:31], v[68:71]\n"
    "ds_read_b64_tr_b16 v[18:19], v9 offset:26624\n"
    "v_fma_f32 v203, v203, v54, v54\n"
    "s_waitcnt lgkmcnt(6)\n"
    "v_mfma_f32_16x16x32_bf16 v[72:75], v[24:27], a[24:27], v[72:75]\n"
    "ds_read_b64_tr_b16 v[20:21], v10 offset:7168\n"
    "s_add_u32 m0, s53, 12288\n"
    "v_mul_f32 v204, v51, v204\n"
    "v_mfma_f32_16x16x32_bf16 v[76:79], v[24:27], a[28:31], v[76:79]\n"
    "ds_read_b64_tr_b16 v[22:23], v10 offset:7680\n"
    "s_add_u32 s79, s61, 331776\n"
    "v_mul_f32 v205, v51, v205\n"
    "v_mfma_f32_16x16x32_bf16 v[80:83], v[28:31], a[24:27], v[80:83]\n"
    "ds_read_b64_tr_b16 v[24:25], v6 offset:24576\n"
    "buffer_load_dwordx4 v15, s[40:43], s79 offen lds\n"
    "v_mfma_f32_16x16x32_bf16 v[84:87], v[28:31], a[28:31], v[84:87]\n"
    "ds_read_b64_tr_b16 v[26:27], v6 offset:26624\n"
    "v_mul_f32 v206, v51, v206\n"
    "s_waitcnt lgkmcnt(6)\n"
    "v_mfma_f32_16x16x32_bf16 v[88:91], v[232:235], a[24:27], v[88:91]\n"
    "ds_read_b64_tr_b16 v[28:29], v7 offset:24576\n"
    "v_rcp_f32 v200, v200\n"
    "v_mfma_f32_16x16x32_bf16 v[92:95], v[232:235], a[28:31], v[92:95]\n"
    "ds_read_b64_tr_b16 v[30:31], v7 offset:26624\n"
    "v_rcp_f32 v201, v201\n"
    "v_mfma_f32_16x16x32_bf16 v[96:99], v[236:239], a[24:27], v[96:99]\n"
    "ds_read_b64_tr_b16 v[232:233], v6 offset:36864\n"
    "v_rcp_f32 v202, v202\n"
    "v_mfma_f32_16x16x32_bf16 v[100:103], v[236:239], a[28:31], v[100:103]\n"
    "ds_read_b64_tr_b16 v[234:235], v6 offset:38912\n"
    "v_mul_f32 v207, v51, v207\n"
    "s_waitcnt lgkmcnt(6)\n"
    "v_mfma_f32_16x16x32_bf16 v[104:107], v[16:19], a[24:27], v[104:107]\n"
    "ds_read_b64_tr_b16 v[236:237], v7 offset:36864\n"
    "v_rcp_f32 v203, v203\n"
    "v_mfma_f32_16x16x32_bf16 v[108:111], v[16:19], a[28:31], v[108:111]\n"
    "ds_read_b64_tr_b16 v[238:239], v7 offset:38912\n"
    "v_exp_f32 v204, v204\n"
    "v_mfma_f32_16x16x32_bf16 v[224:227], v[20:23], a[24:27], v[224:227]\n"
    "ds_read_b64_tr_b16 v[16:17], v8 offset:36864\n"
    "v_exp_f32 v205, v205\n"
    "v_mfma_f32_16x16x32_bf16 v[228:231], v[20:23], a[28:31], v[228:231]\n"
    "ds_read_b64_tr_b16 v[18:19], v8 offset:38912\n"
    "v_fma_f32 v204, v204, v55, v55\n"
    "s_waitcnt lgkmcnt(6)\n"
    "v_mfma_f32_16x16x32_bf16 v[112:115], v[24:27], a[24:27], v[112:115]\n"
    "ds_read_b64_tr_b16 v[20:21], v9 offset:36864\n"
    "s_add_u32 m0, s52, 0\n"
    "v_fma_f32 v205, v205, v55, v55\n"
    "v_mfma_f32_16x16x32_bf16 v[116:119], v[24:27], a[28:31], v[116:119]\n"
    "ds_read_b64_tr_b16 v[22:23], v9 offset:38912\n"
    "s_add_u32 s79, s59, 479232\n"
    "v_mul_f32 v208, v192, v208\n"
    "v_mfma_f32_16x16x32_bf16 v[120:123], v[28:31], a[24:27], v[120:123]\n"
    "ds_read_b64_tr_b16 v[24:25], v8 offset:32768\n"
    "buffer_load_dwordx4 v14, s[40:43], s79 offen lds\n"
    "v_mfma_f32_16x16x32_bf16 v[124:127], v[28:31], a[28:31], v[124:127]\n"
    "ds_read_b64_tr_b16 v[26:27], v8 offset:34816\n"
    "v_mul_f32 v209, v193, v209\n"
    "s_waitcnt lgkmcnt(6)\n"
    "v_mfma_f32_16x16x32_bf16 v[64:67], v[232:235], a[32:35], v[64:67]\n"
    "ds_read_b64_tr_b16 v[28:29], v9 offset:32768\n"
    "buffer_load_dwordx4 v2, s[40:43], s79 offen offset:1024 lds\n"
    "v_mfma_f32_16x16x32_bf16 v[68:71], v[232:235], a[36:39], v[68:71]\n"
    "ds_read_b64_tr_b16 v[30:31], v9 offset:34816\n"
    "v_exp_f32 v206, v206\n"
    "v_mfma_f32_16x16x32_bf16 v[72:75], v[236:239], a[32:35], v[72:75]\n"
    "ds_read_b64_tr_b16 v[232:233], v10 offset:8192\n"
    "buffer_load_dwordx4 v3, s[40:43], s79 offen offset:2048 lds\n"
    "v_mfma_f32_16x16x32_bf16 v[76:79], v[236:239], a[36:39], v[76:79]\n"
    "ds_read_b64_tr_b16 v[234:235], v10 offset:8704\n"
    "v_fma_f32 v206, v206, v55, v55\n"
    "s_waitcnt lgkmcnt(6)\n"
    "v_mfma_f32_16x16x32_bf16 v[80:83], v[16:19], a[32:35], v[80:83]\n"
    "ds_read_b64_tr_b16 v[236:237], v6 offset:32768\n"
    "v_exp_f32 v207, v207\n"
    "v_mfma_f32_16x16x32_bf16 v[84:87], v[16:19], a[36:39], v[84:87]\n"
    "ds_read_b64_tr_b16 v[238:239], v6 offset:34816\n"
    "buffer_load_dwordx4 v4, s[40:43], s79 offen offset:3072 lds\n"
    "v_mfma_f32_16x16x32_bf16 v[88:91], v[20:23], a[32:35], v[88:91]\n"
    "ds_read_b64_tr_b16 v[16:17], v7 offset:32768\n"
    "v_fma_f32 v207, v207, v55, v55\n"
    "v_mul_f32 v210, v194, v210\n"
    "v_mfma_f32_16x16x32_bf16 v[92:95], v[20:23], a[36:39], v[92:95]\n"
    "ds_read_b64_tr_b16 v[18:19], v7 offset:34816\n"
    "v_mul_f32 v211, v195, v211\n"
    "s_waitcnt lgkmcnt(6)\n"
    "v_mfma_f32_16x16x32_bf16 v[96:99], v[24:27], a[32:35], v[96:99]\n"
    "ds_read_b64_tr_b16 v[20:21], v6 offset:45056\n"
    "v_rcp_f32 v204, v204\n"
    "v_mfma_f32_16x16x32_bf16 v[100:103], v[24:27], a[36:39], v[100:103]\n"
    "ds_read_b64_tr_b16 v[22:23], v6 offset:47104\n"
    "s_add_u32 m0, s52, 114688\n"
    "v_cvt_pk_bf16_f32 v32, v208, v209\n"
    "v_mfma_f32_16x16x32_bf16 v[104:107], v[28:31], a[32:35], v[104:107]\n"
    "ds_read_b64_tr_b16 v[24:25], v7 offset:45056\n"
    "s_add_u32 s79, s59, 589824\n"
    "v_cvt_pk_bf16_f32 v33, v210, v211\n"
    "v_mfma_f32_16x16x32_bf16 v[108:111], v[28:31], a[36:39], v[108:111]\n"
    "ds_read_b64_tr_b16 v[26:27], v7 offset:47104\n"
    "s_add_u32 s35, s58, 0\n"
    "s_waitcnt lgkmcnt(6)\n"
    "v_mfma_f32_16x16x32_bf16 v[224:227], v[232:235], a[32:35], v[224:227]\n"
    "ds_read_b64_tr_b16 v[28:29], v8 offset:45056\n"
    "buffer_load_dwordx4 v14, s[40:43], s79 offen lds\n"
    "v_mfma_f32_16x16x32_bf16 v[228:231], v[232:235], a[36:39], v[228:231]\n"
    "ds_read_b64_tr_b16 v[30:31], v8 offset:47104\n"
    "buffer_load_dwordx4 v2, s[40:43], s79 offen offset:1024 lds\n"
    "v_mfma_f32_16x16x32_bf16 v[112:115], v[236:239], a[32:35], v[112:115]\n"
    "ds_read_b64_tr_b16 v[232:233], v9 offset:45056\n"
    "v_rcp_f32 v205, v205\n"
    "v_mfma_f32_16x16x32_bf16 v[116:119], v[236:239], a[36:39], v[116:119]\n"
    "ds_read_b64_tr_b16 v[234:235], v9 offset:47104\n"
    "v_mul_f32 v212, v196, v212\n"
    "s_waitcnt lgkmcnt(6)\n"
    "v_mfma_f32_16x16x32_bf16 v[120:123], v[16:19], a[32:35], v[120:123]\n"
    "ds_read_b64_tr_b16 v[236:237], v8 offset:40960\n"
    "buffer_load_dwordx4 v3, s[40:43], s79 offen offset:2048 lds\n"
    "v_mfma_f32_16x16x32_bf16 v[124:127], v[16:19], a[36:39], v[124:127]\n"
    "ds_read_b64_tr_b16 v[238:239], v8 offset:43008\n"
    "v_rcp_f32 v206, v206\n"
    "v_mfma_f32_16x16x32_bf16 v[64:67], v[20:23], a[40:43], v[64:67]\n"
    "ds_read_b64_tr_b16 v[16:17], v9 offset:40960\n"
    "v_rcp_f32 v207, v207\n"
    "v_mfma_f32_16x16x32_bf16 v[68:71], v[20:23], a[44:47], v[68:71]\n"
    "ds_read_b64_tr_b16 v[18:19], v9 offset:43008\n"
    "v_mul_f32 v213, v197, v213\n"
    "s_waitcnt lgkmcnt(6)\n"
    "v_mfma_f32_16x16x32_bf16 v[72:75], v[24:27], a[40:43], v[72:75]\n"
    "ds_read_b64_tr_b16 v[20:21], v10 offset:9216\n"
    "buffer_store_dwordx2 v[32:33], v62, s[44:47], s35 offen offset:0 sc1\n"
    "v_mfma_f32_16x16x32_bf16 v[76:79], v[24:27], a[44:47], v[76:79]\n"
    "ds_read_b64_tr_b16 v[22:23], v10 offset:9728\n"
    "v_mul_f32 v214, v198, v214\n"
    "v_mul_f32 v215, v199, v215\n"
    "v_mfma_f32_16x16x32_bf16 v[80:83], v[28:31], a[40:43], v[80:83]\n"
    "ds_read_b64_tr_b16 v[24:25], v6 offset:40960\n"
    "v_cvt_pk_bf16_f32 v36, v212, v213\n"
    "v_cvt_pk_bf16_f32 v37, v214, v215\n"
    "v_mfma_f32_16x16x32_bf16 v[84:87], v[28:31], a[44:47], v[84:87]\n"
    "ds_read_b64_tr_b16 v[26:27], v6 offset:43008\n"
    "v_mul_f32 v216, v200, v216\n"
    "s_waitcnt lgkmcnt(6)\n"
    "v_mfma_f32_16x16x32_bf16 v[88:91], v[232:235], a[40:43], v[88:91]\n"
    "ds_read_b64_tr_b16 v[28:29], v7 offset:40960\n"
    "v_mul_f32 v217, v201, v217\n"
    "v_mul_f32 v218, v202, v218\n"
    "v_mfma_f32_16x16x32_bf16 v[92:95], v[232:235], a[44:47], v[92:95]\n"
    "ds_read_b64_tr_b16 v[30:31], v7 offset:43008\n"
    "v_mul_f32 v219, v203, v219\n"
    "v_cvt_pk_bf16_f32 v40, v216, v217\n"
    "v_mfma_f32_16x16x32_bf16 v[96:99], v[236:239], a[40:43], v[96:99]\n"
    "ds_read_b64_tr_b16 v[232:233], v6 offset:53248\n"
    "v_cvt_pk_bf16_f32 v41, v218, v219\n"
    "v_mfma_f32_16x16x32_bf16 v[100:103], v[236:239], a[44:47], v[100:103]\n"
    "ds_read_b64_tr_b16 v[234:235], v6 offset:55296\n"
    "s_waitcnt vmcnt(9)\n"
    "s_barrier\n"
    "s_waitcnt lgkmcnt(6)\n"
    "v_mfma_f32_16x16x32_bf16 v[104:107], v[16:19], a[40:43], v[104:107]\n"
    "ds_read_b64_tr_b16 v[236:237], v7 offset:53248\n"
    "buffer_load_dwordx4 v4, s[40:43], s79 offen offset:3072 lds\n"
    "v_mfma_f32_16x16x32_bf16 v[108:111], v[16:19], a[44:47], v[108:111]\n"
    "ds_read_b64_tr_b16 v[238:239], v7 offset:55296\n"
    "s_add_u32 s76, s58, 294912\n"
    "v_mfma_f32_16x16x32_bf16 v[224:227], v[20:23], a[40:43], v[224:227]\n"
    "ds_read_b64_tr_b16 v[16:17], v8 offset:53248\n"
    "v_mul_f32 v220, v204, v220\n"
    "v_mfma_f32_16x16x32_bf16 v[228:231], v[20:23], a[44:47], v[228:231]\n"
    "ds_read_b64_tr_b16 v[18:19], v8 offset:55296\n"
    "s_waitcnt lgkmcnt(6)\n"
    "v_mfma_f32_16x16x32_bf16 v[112:115], v[24:27], a[40:43], v[112:115]\n"
    "ds_read_b64_tr_b16 v[20:21], v9 offset:53248\n"
    "s_add_u32 m0, s53, 4096\n"
    "v_mfma_f32_16x16x32_bf16 v[116:119], v[24:27], a[44:47], v[116:119]\n"
    "ds_read_b64_tr_b16 v[22:23], v9 offset:55296\n"
    "s_add_u32 s79, s61, 589824\n"
    "v_mfma_f32_16x16x32_bf16 v[120:123], v[28:31], a[40:43], v[120:123]\n"
    "ds_read_b64_tr_b16 v[24:25], v8 offset:49152\n"
    "buffer_load_dwordx4 v15, s[40:43], s79 offen lds\n"
    "v_mfma_f32_16x16x32_bf16 v[124:127], v[28:31], a[44:47], v[124:127]\n"
    "ds_read_b64_tr_b16 v[26:27], v8 offset:51200\n"
    "s_waitcnt lgkmcnt(6)\n"
    "v_mfma_f32_16x16x32_bf16 v[64:67], v[232:235], a[48:51], v[64:67]\n"
    "ds_read_b64_tr_b16 v[28:29], v9 offset:49152\n"
    "v_mul_f32 v221, v205, v221\n"
    "v_mfma_f32_16x16x32_bf16 v[68:71], v[232:235], a[52:55], v[68:71]\n"
    "ds_read_b64_tr_b16 v[30:31], v9 offset:51200\n"
    "v_mul_f32 v222, v206, v222\n"
    "v_mfma_f32_16x16x32_bf16 v[72:75], v[236:239], a[48:51], v[72:75]\n"
    "ds_read_b64_tr_b16 v[232:233], v10 offset:10240\n"
    "v_mul_f32 v223, v207, v223\n"
    "v_mfma_f32_16x16x32_bf16 v[76:79], v[236:239], a[52:55], v[76:79]\n"
    "ds_read_b64_tr_b16 v[234:235], v10 offset:10752\n"
    "s_waitcnt lgkmcnt(6)\n"
    "v_mfma_f32_16x16x32_bf16 v[80:83], v[16:19], a[48:51], v[80:83]\n"
    "ds_read_b64_tr_b16 v[236:237], v6 offset:49152\n"
    "v_cvt_pk_bf16_f32 v44, v220, v221\n"
    "v_mfma_f32_16x16x32_bf16 v[84:87], v[16:19], a[52:55], v[84:87]\n"
    "ds_read_b64_tr_b16 v[238:239], v6 offset:51200\n"
    "v_mfma_f32_16x16x32_bf16 v[88:91], v[20:23], a[48:51], v[88:91]\n"
    "ds_read_b64_tr_b16 v[16:17], v7 offset:49152\n"
    "v_mfma_f32_16x16x32_bf16 v[92:95], v[20:23], a[52:55], v[92:95]\n"
    "ds_read_b64_tr_b16 v[18:19], v7 offset:51200\n"
    "s_waitcnt lgkmcnt(6)\n"
    "v_mfma_f32_16x16x32_bf16 v[96:99], v[24:27], a[48:51], v[96:99]\n"
    "ds_read_b64_tr_b16 v[20:21], v6 offset:61440\n"
    "s_add_u32 s33, s63, 32768\n"
    "v_mfma_f32_16x16x32_bf16 v[100:103], v[24:27], a[52:55], v[100:103]\n"
    "ds_read_b64_tr_b16 v[22:23], v6 offset:63488\n"
    "buffer_load_dwordx4 a[0:3], v0, s[44:47], s33 offen offset:0 sc1\n"
    "v_mfma_f32_16x16x32_bf16 v[104:107], v[28:31], a[48:51], v[104:107]\n"
    "ds_read_b64_tr_b16 v[24:25], v7 offset:61440\n"
    "v_mfma_f32_16x16x32_bf16 v[108:111], v[28:31], a[52:55], v[108:111]\n"
    "ds_read_b64_tr_b16 v[26:27], v7 offset:63488\n"
    "s_waitcnt lgkmcnt(6)\n"
    "v_mfma_f32_16x16x32_bf16 v[224:227], v[232:235], a[48:51], v[224:227]\n"
    "ds_read_b64_tr_b16 v[28:29], v8 offset:61440\n"
    "buffer_store_dwordx2 v[36:37], v62, s[44:47], s35 offen offset:1024 sc1\n"
    "v_mfma_f32_16x16x32_bf16 v[228:231], v[232:235], a[52:55], v[228:231]\n"
    "ds_read_b64_tr_b16 v[30:31], v8 offset:63488\n"
    "v_cvt_pk_bf16_f32 v45, v222, v223\n"
    "v_mfma_f32_16x16x32_bf16 v[112:115], v[236:239], a[48:51], v[112:115]\n"
    "ds_read_b64_tr_b16 v[232:233], v9 offset:61440\n"
    "v_mfma_f32_16x16x32_bf16 v[116:119], v[236:239], a[52:55], v[116:119]\n"
    "ds_read_b64_tr_b16 v[234:235], v9 offset:63488\n"
    "s_waitcnt lgkmcnt(6)\n"
    "v_mfma_f32_16x16x32_bf16 v[120:123], v[16:19], a[48:51], v[120:123]\n"
    "ds_read_b64_tr_b16 v[236:237], v8 offset:57344\n"
    "v_mfma_f32_16x16x32_bf16 v[124:127], v[16:19], a[52:55], v[124:127]\n"
    "ds_read_b64_tr_b16 v[238:239], v8 offset:59392\n"
    "v_mfma_f32_16x16x32_bf16 v[64:67], v[20:23], a[56:59], v[64:67]\n"
    "ds_read_b64_tr_b16 v[16:17], v9 offset:57344\n"
    "buffer_store_dwordx2 v[40:41], v62, s[44:47], s76 offen offset:0 sc1\n"
    "v_mfma_f32_16x16x32_bf16 v[68:71], v[20:23], a[60:63], v[68:71]\n"
    "ds_read_b64_tr_b16 v[18:19], v9 offset:59392\n"
    "s_waitcnt lgkmcnt(6)\n"
    "v_mfma_f32_16x16x32_bf16 v[72:75], v[24:27], a[56:59], v[72:75]\n"
    "ds_read_b64_tr_b16 v[20:21], v10 offset:11264\n"
    "buffer_store_dwordx2 v[44:45], v62, s[44:47], s76 offen offset:1024 sc1\n"
    "v_mfma_f32_16x16x32_bf16 v[76:79], v[24:27], a[60:63], v[76:79]\n"
    "ds_read_b64_tr_b16 v[22:23], v10 offset:11776\n"
    "v_mfma_f32_16x16x32_bf16 v[80:83], v[28:31], a[56:59], v[80:83]\n"
    "ds_read_b64_tr_b16 v[24:25], v6 offset:57344\n"
    "v_mfma_f32_16x16x32_bf16 v[84:87], v[28:31], a[60:63], v[84:87]\n"
    "ds_read_b64_tr_b16 v[26:27], v6 offset:59392\n"
    "s_waitcnt lgkmcnt(6)\n"
    "v_mfma_f32_16x16x32_bf16 v[88:91], v[232:235], a[56:59], v[88:91]\n"
    "ds_read_b64_tr_b16 v[28:29], v7 offset:57344\n"
    "v_mfma_f32_16x16x32_bf16 v[92:95], v[232:235], a[60:63], v[92:95]\n"
    "ds_read_b64_tr_b16 v[30:31], v7 offset:59392\n"
    "v_mfma_f32_16x16x32_bf16 v[96:99], v[236:239], a[56:59], v[96:99]\n"
    "v_add_u32 v6, 0x10000, v6\n"
    "ds_read_b64_tr_b16 v[232:233], v6 offset:4096\n"
    "v_mfma_f32_16x16x32_bf16 v[100:103], v[236:239], a[60:63], v[100:103]\n"
    "ds_read_b64_tr_b16 v[234:235], v6 offset:6144\n"
    "s_waitcnt lgkmcnt(6)\n"
    "v_mfma_f32_16x16x32_bf16 v[104:107], v[16:19], a[56:59], v[104:107]\n"
    "v_add_u32 v7, 0x10000, v7\n"
    "ds_read_b64_tr_b16 v[236:237], v7 offset:4096\n"
    "v_mfma_f32_16x16x32_bf16 v[108:111], v[16:19], a[60:63], v[108:111]\n"
    "ds_read_b64_tr_b16 v[238:239], v7 offset:6144\n"
    "v_mfma_f32_16x16x32_bf16 v[224:227], v[20:23], a[56:59], v[224:227]\n"
    "v_add_u32 v8, 0x10000, v8\n"
    "ds_read_b64_tr_b16 v[16:17], v8 offset:4096\n"
    "v_mfma_f32_16x16x32_bf16 v[228:231], v[20:23], a[60:63], v[228:231]\n"
    "ds_read_b64_tr_b16 v[18:19], v8 offset:6144\n"
    "s_waitcnt lgkmcnt(6)\n"
    "v_mfma_f32_16x16x32_bf16 v[112:115], v[24:27], a[56:59], v[112:115]\n"
    "v_add_u32 v9, 0x10000, v9\n"
    "ds_read_b64_tr_b16 v[20:21], v9 offset:4096\n"
    "v_mfma_f32_16x16x32_bf16 v[116:119], v[24:27], a[60:63], v[116:119]\n"
    "ds_read_b64_tr_b16 v[22:23], v9 offset:6144\n"
    "v_mfma_f32_16x16x32_bf16 v[120:123], v[28:31], a[56:59], v[120:123]\n"
    "ds_read_b64_tr_b16 v[24:25], v8 offset:0\n"
    "v_mfma_f32_16x16x32_bf16 v[124:127], v[28:31], a[60:63], v[124:127]\n"
    "ds_read_b64_tr_b16 v[26:27], v8 offset:2048\n"
    "s_waitcnt lgkmcnt(6)\n"
    "v_mfma_f32_16x16x32_bf16 v[64:67], v[232:235], a[96:99], v[64:67]\n"
    "ds_read_b64_tr_b16 v[28:29], v9 offset:0\n"
    "buffer_load_dwordx4 a[4:7], v0, s[44:47], s33 offen offset:1024 sc1\n"
    "v_mfma_f32_16x16x32_bf16 v[68:71], v[232:235], a[100:103], v[68:71]\n"
    "ds_read_b64_tr_b16 v[30:31], v9 offset:2048\n"
    "v_mfma_f32_16x16x32_bf16 v[72:75], v[236:239], a[96:99], v[72:75]\n"
    "ds_read_b64_tr_b16 v[232:233], v10 offset:0\n"
    "v_mfma_f32_16x16x32_bf16 v[76:79], v[236:239], a[100:103], v[76:79]\n"
    "ds_read_b64_tr_b16 v[234:235], v10 offset:512\n"
    "s_waitcnt lgkmcnt(6)\n"
    "v_mfma_f32_16x16x32_bf16 v[80:83], v[16:19], a[96:99], v[80:83]\n"
    "ds_read_b64_tr_b16 v[236:237], v6 offset:0\n"
    "s_add_u32 m0, s52, 16384\n"
    "v_mfma_f32_16x16x32_bf16 v[84:87], v[16:19], a[100:103], v[84:87]\n"
    "ds_read_b64_tr_b16 v[238:239], v6 offset:2048\n"
    "s_add_u32 s79, s59, 737280\n"
    "v_mfma_f32_16x16x32_bf16 v[88:91], v[20:23], a[96:99], v[88:91]\n"
    "ds_read_b64_tr_b16 v[16:17], v7 offset:0\n"
    "buffer_load_dwordx4 v14, s[40:43], s79 offen lds\n"
    "v_mfma_f32_16x16x32_bf16 v[92:95], v[20:23], a[100:103], v[92:95]\n"
    "ds_read_b64_tr_b16 v[18:19], v7 offset:2048\n"
    "s_waitcnt lgkmcnt(6)\n"
    "v_mfma_f32_16x16x32_bf16 v[96:99], v[24:27], a[96:99], v[96:99]\n"
    "ds_read_b64_tr_b16 v[20:21], v6 offset:12288\n"
    "buffer_load_dwordx4 v2, s[40:43], s79 offen offset:1024 lds\n"
    "v_mfma_f32_16x16x32_bf16 v[100:103], v[24:27], a[100:103], v[100:103]\n"
    "ds_read_b64_tr_b16 v[22:23], v6 offset:14336\n"
    "v_mfma_f32_16x16x32_bf16 v[104:107], v[28:31], a[96:99], v[104:107]\n"
    "ds_read_b64_tr_b16 v[24:25], v7 offset:12288\n"
    "buffer_load_dwordx4 v3, s[40:43], s79 offen offset:2048 lds\n"
    "v_mfma_f32_16x16x32_bf16 v[108:111], v[28:31], a[100:103], v[108:111]\n"
    "ds_read_b64_tr_b16 v[26:27], v7 offset:14336\n"
    "s_waitcnt lgkmcnt(6)\n"
    "v_mfma_f32_16x16x32_bf16 v[224:227], v[232:235], a[96:99], v[224:227]\n"
    "ds_read_b64_tr_b16 v[28:29], v8 offset:12288\n"
    "v_mfma_f32_16x16x32_bf16 v[228:231], v[232:235], a[100:103], v[228:231]\n"
    "ds_read_b64_tr_b16 v[30:31], v8 offset:14336\n"
    "buffer_load_dwordx4 v4, s[40:43], s79 offen offset:3072 lds\n"
    "v_mfma_f32_16x16x32_bf16 v[112:115], v[236:239], a[96:99], v[112:115]\n"
    "ds_read_b64_tr_b16 v[232:233], v9 offset:12288\n"
    "v_mfma_f32_16x16x32_bf16 v[116:119], v[236:239], a[100:103], v[116:119]\n"
    "ds_read_b64_tr_b16 v[234:235], v9 offset:14336\n"
    "s_waitcnt lgkmcnt(6)\n"
    "v_mfma_f32_16x16x32_bf16 v[120:123], v[16:19], a[96:99], v[120:123]\n"
    "ds_read_b64_tr_b16 v[236:237], v8 offset:8192\n"
    "v_mfma_f32_16x16x32_bf16 v[124:127], v[16:19], a[100:103], v[124:127]\n"
    "ds_read_b64_tr_b16 v[238:239], v8 offset:10240\n"
    "s_add_u32 m0, s52, 131072\n"
    "v_mfma_f32_16x16x32_bf16 v[64:67], v[20:23], a[104:107], v[64:67]\n"
    "ds_read_b64_tr_b16 v[16:17], v9 offset:8192\n"
    "s_add_u32 s79, s59, 884736\n"
    "v_mfma_f32_16x16x32_bf16 v[68:71], v[20:23], a[108:111], v[68:71]\n"
    "ds_read_b64_tr_b16 v[18:19], v9 offset:10240\n"
    "s_waitcnt vmcnt(19)\n"
    "s_barrier\n"
    "buffer_store_dword v61, v12, s[44:47], 0 offen offset:768 sc1\n"
    "s_waitcnt lgkmcnt(6)\n"
    "v_mfma_f32_16x16x32_bf16 v[72:75], v[24:27], a[104:107], v[72:75]\n"
    "ds_read_b64_tr_b16 v[20:21], v10 offset:1024\n"
    "buffer_load_dwordx4 v14, s[40:43], s79 offen lds\n"
    "v_mfma_f32_16x16x32_bf16 v[76:79], v[24:27], a[108:111], v[76:79]\n"
    "ds_read_b64_tr_b16 v[22:23], v10 offset:1536\n"
    "buffer_load_dwordx4 v2, s[40:43], s79 offen offset:1024 lds\n"
    "v_mfma_f32_16x16x32_bf16 v[80:83], v[28:31], a[104:107], v[80:83]\n"
    "ds_read_b64_tr_b16 v[24:25], v6 offset:8192\n"
    "v_mfma_f32_16x16x32_bf16 v[84:87], v[28:31], a[108:111], v[84:87]\n"
    "ds_read_b64_tr_b16 v[26:27], v6 offset:10240\n"
    "s_waitcnt lgkmcnt(6)\n"
    "v_mfma_f32_16x16x32_bf16 v[88:91], v[232:235], a[104:107], v[88:91]\n"
    "ds_read_b64_tr_b16 v[28:29], v7 offset:8192\n"
    "buffer_load_dwordx4 v3, s[40:43], s79 offen offset:2048 lds\n"
    "v_mfma_f32_16x16x32_bf16 v[92:95], v[232:235], a[108:111], v[92:95]\n"
    "ds_read_b64_tr_b16 v[30:31], v7 offset:10240\n"
    "v_mfma_f32_16x16x32_bf16 v[96:99], v[236:239], a[104:107], v[96:99]\n"
    "ds_read_b64_tr_b16 v[232:233], v6 offset:20480\n"
    "v_mfma_f32_16x16x32_bf16 v[100:103], v[236:239], a[108:111], v[100:103]\n"
    "ds_read_b64_tr_b16 v[234:235], v6 offset:22528\n"
    "s_waitcnt lgkmcnt(6)\n"
    "v_mfma_f32_16x16x32_bf16 v[104:107], v[16:19], a[104:107], v[104:107]\n"
    "ds_read_b64_tr_b16 v[236:237], v7 offset:20480\n"
    "buffer_load_dwordx4 v4, s[40:43], s79 offen offset:3072 lds\n"
    "v_mfma_f32_16x16x32_bf16 v[108:111], v[16:19], a[108:111], v[108:111]\n"
    "ds_read_b64_tr_b16 v[238:239], v7 offset:22528\n"
    "v_mfma_f32_16x16x32_bf16 v[224:227], v[20:23], a[104:107], v[224:227]\n"
    "ds_read_b64_tr_b16 v[16:17], v8 offset:20480\n"
    "v_mfma_f32_16x16x32_bf16 v[228:231], v[20:23], a[108:111], v[228:231]\n"
    "ds_read_b64_tr_b16 v[18:19], v8 offset:22528\n"
    "s_waitcnt lgkmcnt(6)\n"
    "v_mfma_f32_16x16x32_bf16 v[112:115], v[24:27], a[104:107], v[112:115]\n"
    "ds_read_b64_tr_b16 v[20:21], v9 offset:20480\n"
    "buffer_load_dwordx4 a[8:11], v0, s[44:47], s33 offen offset:2048 sc1\n"
    "v_mfma_f32_16x16x32_bf16 v[116:119], v[24:27], a[108:111], v[116:119]\n"
    "ds_read_b64_tr_b16 v[22:23], v9 offset:22528\n"
    "v_mfma_f32_16x16x32_bf16 v[120:123], v[28:31], a[104:107], v[120:123]\n"
    "ds_read_b64_tr_b16 v[24:25], v8 offset:16384\n"
    "v_mfma_f32_16x16x32_bf16 v[124:127], v[28:31], a[108:111], v[124:127]\n"
    "ds_read_b64_tr_b16 v[26:27], v8 offset:18432\n"
    "s_waitcnt lgkmcnt(6)\n"
    "v_mfma_f32_16x16x32_bf16 v[64:67], v[232:235], a[112:115], v[64:67]\n"
    "ds_read_b64_tr_b16 v[28:29], v9 offset:16384\n"
    "buffer_load_dwordx4 a[12:15], v0, s[44:47], s33 offen offset:3072 sc1\n"
    "v_mfma_f32_16x16x32_bf16 v[68:71], v[232:235], a[116:119], v[68:71]\n"
    "ds_read_b64_tr_b16 v[30:31], v9 offset:18432\n"
    "v_mfma_f32_16x16x32_bf16 v[72:75], v[236:239], a[112:115], v[72:75]\n"
    "ds_read_b64_tr_b16 v[232:233], v10 offset:2048\n"
    "v_mfma_f32_16x16x32_bf16 v[76:79], v[236:239], a[116:119], v[76:79]\n"
    "ds_read_b64_tr_b16 v[234:235], v10 offset:2560\n"
    "s_waitcnt lgkmcnt(6)\n"
    "v_mfma_f32_16x16x32_bf16 v[80:83], v[16:19], a[112:115], v[80:83]\n"
    "ds_read_b64_tr_b16 v[236:237], v6 offset:16384\n"
    "s_add_u32 s33, s63, 36864\n"
    "v_mfma_f32_16x16x32_bf16 v[84:87], v[16:19], a[116:119], v[84:87]\n"
    "ds_read_b64_tr_b16 v[238:239], v6 offset:18432\n"
    "buffer_load_dwordx4 a[16:19], v0, s[44:47], s33 offen offset:0 sc1\n"
    "v_mfma_f32_16x16x32_bf16 v[88:91], v[20:23], a[112:115], v[88:91]\n"
    "ds_read_b64_tr_b16 v[16:17], v7 offset:16384\n"
    "buffer_load_dwordx4 a[20:23], v0, s[44:47], s33 offen offset:1024 sc1\n"
    "v_mfma_f32_16x16x32_bf16 v[92:95], v[20:23], a[116:119], v[92:95]\n"
    "ds_read_b64_tr_b16 v[18:19], v7 offset:18432\n"
    "s_waitcnt lgkmcnt(6)\n"
    "v_mfma_f32_16x16x32_bf16 v[96:99], v[24:27], a[112:115], v[96:99]\n"
    "ds_read_b64_tr_b16 v[20:21], v6 offset:28672\n"
    "v_mfma_f32_16x16x32_bf16 v[100:103], v[24:27], a[116:119], v[100:103]\n"
    "ds_read_b64_tr_b16 v[22:23], v6 offset:30720\n"
    "v_mfma_f32_16x16x32_bf16 v[104:107], v[28:31], a[112:115], v[104:107]\n"
    "ds_read_b64_tr_b16 v[24:25], v7 offset:28672\n"
    "buffer_load_dwordx4 a[24:27], v0, s[44:47], s33 offen offset:2048 sc1\n"
    "v_mfma_f32_16x16x32_bf16 v[108:111], v[28:31], a[116:119], v[108:111]\n"
    "ds_read_b64_tr_b16 v[26:27], v7 offset:30720\n"
    "s_waitcnt lgkmcnt(6)\n"
    "v_mfma_f32_16x16x32_bf16 v[224:227], v[232:235], a[112:115], v[224:227]\n"
    "ds_read_b64_tr_b16 v[28:29], v8 offset:28672\n"
    "v_mfma_f32_16x16x32_bf16 v[228:231], v[232:235], a[116:119], v[228:231]\n"
    "ds_read_b64_tr_b16 v[30:31], v8 offset:30720\n"
    "buffer_load_dwordx4 a[28:31], v0, s[44:47], s33 offen offset:3072 sc1\n"
    "v_mfma_f32_16x16x32_bf16 v[112:115], v[236:239], a[112:115], v[112:115]\n"
    "ds_read_b64_tr_b16 v[232:233], v9 offset:28672\n"
    "v_mfma_f32_16x16x32_bf16 v[116:119], v[236:239], a[116:119], v[116:119]\n"
    "ds_read_b64_tr_b16 v[234:235], v9 offset:30720\n"
    "s_waitcnt lgkmcnt(6)\n"
    "v_mfma_f32_16x16x32_bf16 v[120:123], v[16:19], a[112:115], v[120:123]\n"
    "ds_read_b64_tr_b16 v[236:237], v8 offset:24576\n"
    "v_mfma_f32_16x16x32_bf16 v[124:127], v[16:19], a[116:119], v[124:127]\n"
    "ds_read_b64_tr_b16 v[238:239], v8 offset:26624\n"
    "s_add_u32 m0, s52, 32768\n"
    "v_mfma_f32_16x16x32_bf16 v[64:67], v[20:23], a[120:123], v[64:67]\n"
    "ds_read_b64_tr_b16 v[16:17], v9 offset:24576\n"
    "s_add_u32 s79, s59, 1032192\n"
    "v_mfma_f32_16x16x32_bf16 v[68:71], v[20:23], a[124:127], v[68:71]\n"
    "ds_read_b64_tr_b16 v[18:19], v9 offset:26624\n"
    "s_waitcnt lgkmcnt(6)\n"
    "v_mfma_f32_16x16x32_bf16 v[72:75], v[24:27], a[120:123], v[72:75]\n"
    "ds_read_b64_tr_b16 v[20:21], v10 offset:3072\n"
    "buffer_load_dwordx4 v14, s[40:43], s79 offen lds\n"
    "v_mfma_f32_16x16x32_bf16 v[76:79], v[24:27], a[124:127], v[76:79]\n"
    "ds_read_b64_tr_b16 v[22:23], v10 offset:3584\n"
    "buffer_load_dwordx4 v2, s[40:43], s79 offen offset:1024 lds\n"
    "v_mfma_f32_16x16x32_bf16 v[80:83], v[28:31], a[120:123], v[80:83]\n"
    "ds_read_b64_tr_b16 v[24:25], v6 offset:24576\n"
    "v_mfma_f32_16x16x32_bf16 v[84:87], v[28:31], a[124:127], v[84:87]\n"
    "ds_read_b64_tr_b16 v[26:27], v6 offset:26624\n"
    "s_waitcnt lgkmcnt(6)\n"
    "v_mfma_f32_16x16x32_bf16 v[88:91], v[232:235], a[120:123], v[88:91]\n"
    "ds_read_b64_tr_b16 v[28:29], v7 offset:24576\n"
    "buffer_load_dwordx4 v3, s[40:43], s79 offen offset:2048 lds\n"
    "v_mfma_f32_16x16x32_bf16 v[92:95], v[232:235], a[124:127], v[92:95]\n"
    "ds_read_b64_tr_b16 v[30:31], v7 offset:26624\n"
    "v_mfma_f32_16x16x32_bf16 v[96:99], v[236:239], a[120:123], v[96:99]\n"
    "ds_read_b64_tr_b16 v[232:233], v6 offset:36864\n"
    "v_mfma_f32_16x16x32_bf16 v[100:103], v[236:239], a[124:127], v[100:103]\n"
    "ds_read_b64_tr_b16 v[234:235], v6 offset:38912\n"
    "s_waitcnt lgkmcnt(6)\n"
    "v_mfma_f32_16x16x32_bf16 v[104:107], v[16:19], a[120:123], v[104:107]\n"
    "ds_read_b64_tr_b16 v[236:237], v7 offset:36864\n"
    "buffer_load_dwordx4 v4, s[40:43], s79 offen offset:3072 lds\n"
    "v_mfma_f32_16x16x32_bf16 v[108:111], v[16:19], a[124:127], v[108:111]\n"
    "ds_read_b64_tr_b16 v[238:239], v7 offset:38912\n"
    "v_mfma_f32_16x16x32_bf16 v[224:227], v[20:23], a[120:123], v[224:227]\n"
    "ds_read_b64_tr_b16 v[16:17], v8 offset:36864\n"
    "v_mfma_f32_16x16x32_bf16 v[228:231], v[20:23], a[124:127], v[228:231]\n"
    "ds_read_b64_tr_b16 v[18:19], v8 offset:38912\n"
    "s_waitcnt lgkmcnt(6)\n"
    "v_mfma_f32_16x16x32_bf16 v[112:115], v[24:27], a[120:123], v[112:115]\n"
    "ds_read_b64_tr_b16 v[20:21], v9 offset:36864\n"
    "s_add_u32 m0, s53, 8192\n"
    "v_mfma_f32_16x16x32_bf16 v[116:119], v[24:27], a[124:127], v[116:119]\n"
    "ds_read_b64_tr_b16 v[22:23], v9 offset:38912\n"
    "s_add_u32 s79, s61, 884736\n"
    "v_mfma_f32_16x16x32_bf16 v[120:123], v[28:31], a[120:123], v[120:123]\n"
    "ds_read_b64_tr_b16 v[24:25], v8 offset:32768\n"
    "buffer_load_dwordx4 v15, s[40:43], s79 offen lds\n"
    "v_mfma_f32_16x16x32_bf16 v[124:127], v[28:31], a[124:127], v[124:127]\n"
    "ds_read_b64_tr_b16 v[26:27], v8 offset:34816\n"
    "s_waitcnt lgkmcnt(6)\n"
    "v_mfma_f32_16x16x32_bf16 v[64:67], v[232:235], a[128:131], v[64:67]\n"
    "ds_read_b64_tr_b16 v[28:29], v9 offset:32768\n"
    "v_mfma_f32_16x16x32_bf16 v[68:71], v[232:235], a[132:135], v[68:71]\n"
    "ds_read_b64_tr_b16 v[30:31], v9 offset:34816\n"
    "v_mfma_f32_16x16x32_bf16 v[72:75], v[236:239], a[128:131], v[72:75]\n"
    "ds_read_b64_tr_b16 v[232:233], v10 offset:12288\n"
    "v_mfma_f32_16x16x32_bf16 v[76:79], v[236:239], a[132:135], v[76:79]\n"
    "ds_read_b64_tr_b16 v[234:235], v10 offset:12800\n"
    "s_waitcnt lgkmcnt(6)\n"
    "v_mfma_f32_16x16x32_bf16 v[80:83], v[16:19], a[128:131], v[80:83]\n"
    "ds_read_b64_tr_b16 v[236:237], v6 offset:32768\n"
    "v_mfma_f32_16x16x32_bf16 v[84:87], v[16:19], a[132:135], v[84:87]\n"
    "ds_read_b64_tr_b16 v[238:239], v6 offset:34816\n"
    "v_mfma_f32_16x16x32_bf16 v[88:91], v[20:23], a[128:131], v[88:91]\n"
    "ds_read_b64_tr_b16 v[16:17], v7 offset:32768\n"
    "v_mfma_f32_16x16x32_bf16 v[92:95], v[20:23], a[132:135], v[92:95]\n"
    "ds_read_b64_tr_b16 v[18:19], v7 offset:34816\n"
    "s_waitcnt lgkmcnt(6)\n"
    "v_mfma_f32_16x16x32_bf16 v[96:99], v[24:27], a[128:131], v[96:99]\n"
    "ds_read_b64_tr_b16 v[20:21], v6 offset:45056\n"
    "s_add_u32 s33, s63, 40960\n"
    "v_mfma_f32_16x16x32_bf16 v[100:103], v[24:27], a[132:135], v[100:103]\n"
    "ds_read_b64_tr_b16 v[22:23], v6 offset:47104\n"
    "buffer_load_dwordx4 a[32:35], v0, s[44:47], s33 offen offset:0 sc1\n"
    "v_mfma_f32_16x16x32_bf16 v[104:107], v[28:31], a[128:131], v[104:107]\n"
    "ds_read_b64_tr_b16 v[24:25], v7 offset:45056\n"
    "v_mfma_f32_16x16x32_bf16 v[108:111], v[28:31], a[132:135], v[108:111]\n"
    "ds_read_b64_tr_b16 v[26:27], v7 offset:47104\n"
    "s_waitcnt vmcnt(22)\n"
    "s_barrier\n"
    "buffer_store_dword v61, v12, s[44:47], 0 offen offset:1024 sc1\n"
    "s_waitcnt lgkmcnt(6)\n"
    "v_mfma_f32_16x16x32_bf16 v[224:227], v[232:235], a[128:131], v[224:227]\n"
    "ds_read_b64_tr_b16 v[28:29], v8 offset:45056\n"
    "buffer_load_dwordx4 a[36:39], v0, s[44:47], s33 offen offset:1024 sc1\n"
    "v_mfma_f32_16x16x32_bf16 v[228:231], v[232:235], a[132:135], v[228:231]\n"
    "ds_read_b64_tr_b16 v[30:31], v8 offset:47104\n"
    "v_mfma_f32_16x16x32_bf16 v[112:115], v[236:239], a[128:131], v[112:115]\n"
    "ds_read_b64_tr_b16 v[232:233], v9 offset:45056\n"
    "v_mfma_f32_16x16x32_bf16 v[116:119], v[236:239], a[132:135], v[116:119]\n"
    "ds_read_b64_tr_b16 v[234:235], v9 offset:47104\n"
    "s_waitcnt lgkmcnt(6)\n"
    "v_mfma_f32_16x16x32_bf16 v[120:123], v[16:19], a[128:131], v[120:123]\n"
    "ds_read_b64_tr_b16 v[236:237], v8 offset:40960\n"
    "buffer_load_dword v60, v11, s[44:47], 0 offen offset:768 sc1\n"
    "v_mfma_f32_16x16x32_bf16 v[124:127], v[16:19], a[132:135], v[124:127]\n"
    "ds_read_b64_tr_b16 v[238:239], v8 offset:43008\n"
    "buffer_load_dwordx4 a[40:43], v0, s[44:47], s33 offen offset:2048 sc1\n"
    "v_mfma_f32_16x16x32_bf16 v[64:67], v[20:23], a[136:139], v[64:67]\n"
    "ds_read_b64_tr_b16 v[16:17], v9 offset:40960\n"
    "v_mfma_f32_16x16x32_bf16 v[68:71], v[20:23], a[140:143], v[68:71]\n"
    "ds_read_b64_tr_b16 v[18:19], v9 offset:43008\n"
    "s_waitcnt lgkmcnt(6)\n"
    "v_mfma_f32_16x16x32_bf16 v[72:75], v[24:27], a[136:139], v[72:75]\n"
    "ds_read_b64_tr_b16 v[20:21], v10 offset:13312\n"
    "buffer_load_dwordx4 a[44:47], v0, s[44:47], s33 offen offset:3072 sc1\n"
    "v_mfma_f32_16x16x32_bf16 v[76:79], v[24:27], a[140:143], v[76:79]\n"
    "ds_read_b64_tr_b16 v[22:23], v10 offset:13824\n"
    "v_mfma_f32_16x16x32_bf16 v[80:83], v[28:31], a[136:139], v[80:83]\n"
    "ds_read_b64_tr_b16 v[24:25], v6 offset:40960\n"
    "v_mfma_f32_16x16x32_bf16 v[84:87], v[28:31], a[140:143], v[84:87]\n"
    "ds_read_b64_tr_b16 v[26:27], v6 offset:43008\n"
    "s_waitcnt lgkmcnt(6)\n"
    "v_mfma_f32_16x16x32_bf16 v[88:91], v[232:235], a[136:139], v[88:91]\n"
    "ds_read_b64_tr_b16 v[28:29], v7 offset:40960\n"
    "s_add_u32 s33, s63, 45056\n"
    "v_mfma_f32_16x16x32_bf16 v[92:95], v[232:235], a[140:143], v[92:95]\n"
    "ds_read_b64_tr_b16 v[30:31], v7 offset:43008\n"
    "buffer_load_dwordx4 a[48:51], v0, s[44:47], s33 offen offset:0 sc1\n"
    "v_mfma_f32_16x16x32_bf16 v[96:99], v[236:239], a[136:139], v[96:99]\n"
    "v_add_u32 v6, 0xffff0000, v6\n"
    "ds_read_b64_tr_b16 v[232:233], v6 offset:4096\n"
    "buffer_load_dwordx4 a[52:55], v0, s[44:47], s33 offen offset:1024 sc1\n"
    "v_mfma_f32_16x16x32_bf16 v[100:103], v[236:239], a[140:143], v[100:103]\n"
    "ds_read_b64_tr_b16 v[234:235], v6 offset:6144\n"
    "s_waitcnt lgkmcnt(6)\n"
    "v_mfma_f32_16x16x32_bf16 v[104:107], v[16:19], a[136:139], v[104:107]\n"
    "v_add_u32 v7, 0xffff0000, v7\n"
    "ds_read_b64_tr_b16 v[236:237], v7 offset:4096\n"
    "v_mfma_f32_16x16x32_bf16 v[108:111], v[16:19], a[140:143], v[108:111]\n"
    "ds_read_b64_tr_b16 v[238:239], v7 offset:6144\n"
    "v_mfma_f32_16x16x32_bf16 v[224:227], v[20:23], a[136:139], v[224:227]\n"
    "v_add_u32 v8, 0xffff0000, v8\n"
    "ds_read_b64_tr_b16 v[16:17], v8 offset:4096\n"
    "buffer_load_dwordx4 a[56:59], v0, s[44:47], s33 offen offset:2048 sc1\n"
    "v_mfma_f32_16x16x32_bf16 v[228:231], v[20:23], a[140:143], v[228:231]\n"
    "ds_read_b64_tr_b16 v[18:19], v8 offset:6144\n"
    "s_waitcnt lgkmcnt(6)\n"
    "v_mfma_f32_16x16x32_bf16 v[112:115], v[24:27], a[136:139], v[112:115]\n"
    "v_add_u32 v9, 0xffff0000, v9\n"
    "ds_read_b64_tr_b16 v[20:21], v9 offset:4096\n"
    "v_mfma_f32_16x16x32_bf16 v[116:119], v[24:27], a[140:143], v[116:119]\n"
    "ds_read_b64_tr_b16 v[22:23], v9 offset:6144\n"
    "v_mfma_f32_16x16x32_bf16 v[120:123], v[28:31], a[136:139], v[120:123]\n"
    "ds_read_b64_tr_b16 v[24:25], v8 offset:0\n"
    "buffer_load_dwordx4 a[60:63], v0, s[44:47], s33 offen offset:3072 sc1\n"
    "v_mfma_f32_16x16x32_bf16 v[124:127], v[28:31], a[140:143], v[124:127]\n"
    "ds_read_b64_tr_b16 v[26:27], v8 offset:2048\n"
    "s_waitcnt lgkmcnt(6)\n"
    "v_mfma_f32_16x16x32_bf16 v[64:67], v[232:235], a[144:147], v[64:67]\n"
    "ds_read_b64_tr_b16 v[28:29], v9 offset:0\n"
    "v_mfma_f32_16x16x32_bf16 v[68:71], v[232:235], a[148:151], v[68:71]\n"
    "ds_read_b64_tr_b16 v[30:31], v9 offset:2048\n"
    "s_add_u32 m0, s52, 49152\n"
    "v_mfma_f32_16x16x32_bf16 v[72:75], v[236:239], a[144:147], v[72:75]\n"
    "ds_read_b64_tr_b16 v[232:233], v10 offset:14336\n"
    "s_add_u32 s79, s59, 626688\n"
    "v_mfma_f32_16x16x32_bf16 v[76:79], v[236:239], a[148:151], v[76:79]\n"
    "ds_read_b64_tr_b16 v[234:235], v10 offset:14848\n"
    "s_waitcnt lgkmcnt(6)\n"
    "v_mfma_f32_16x16x32_bf16 v[80:83], v[16:19], a[144:147], v[80:83]\n"
    "ds_read_b64_tr_b16 v[236:237], v6 offset:0\n"
    "buffer_load_dwordx4 v14, s[40:43], s79 offen lds\n"
    "v_mfma_f32_16x16x32_bf16 v[84:87], v[16:19], a[148:151], v[84:87]\n"
    "ds_read_b64_tr_b16 v[238:239], v6 offset:2048\n"
    "buffer_load_dwordx4 v2, s[40:43], s79 offen offset:1024 lds\n"
    "v_mfma_f32_16x16x32_bf16 v[88:91], v[20:23], a[144:147], v[88:91]\n"
    "ds_read_b64_tr_b16 v[16:17], v7 offset:0\n"
    "v_mfma_f32_16x16x32_bf16 v[92:95], v[20:23], a[148:151], v[92:95]\n"
    "ds_read_b64_tr_b16 v[18:19], v7 offset:2048\n"
    "s_waitcnt lgkmcnt(6)\n"
    "v_mfma_f32_16x16x32_bf16 v[96:99], v[24:27], a[144:147], v[96:99]\n"
    "ds_read_b64_tr_b16 v[20:21], v6 offset:12288\n"
    "buffer_load_dwordx4 v3, s[40:43], s79 offen offset:2048 lds\n"
    "v_mfma_f32_16x16x32_bf16 v[100:103], v[24:27], a[148:151], v[100:103]\n"
    "ds_read_b64_tr_b16 v[22:23], v6 offset:14336\n"
    "v_mfma_f32_16x16x32_bf16 v[104:107], v[28:31], a[144:147], v[104:107]\n"
    "ds_read_b64_tr_b16 v[24:25], v7 offset:12288\n"
    "v_mfma_f32_16x16x32_bf16 v[108:111], v[28:31], a[148:151], v[108:111]\n"
    "ds_read_b64_tr_b16 v[26:27], v7 offset:14336\n"
    "s_waitcnt lgkmcnt(6)\n"
    "v_mfma_f32_16x16x32_bf16 v[224:227], v[232:235], a[144:147], v[224:227]\n"
    "ds_read_b64_tr_b16 v[28:29], v8 offset:12288\n"
    "buffer_load_dwordx4 v4, s[40:43], s79 offen offset:3072 lds\n"
    "v_mfma_f32_16x16x32_bf16 v[228:231], v[232:235], a[148:151], v[228:231]\n"
    "ds_read_b64_tr_b16 v[30:31], v8 offset:14336\n"
    "v_mfma_f32_16x16x32_bf16 v[112:115], v[236:239], a[144:147], v[112:115]\n"
    "ds_read_b64_tr_b16 v[232:233], v9 offset:12288\n"
    "v_mfma_f32_16x16x32_bf16 v[116:119], v[236:239], a[148:151], v[116:119]\n"
    "ds_read_b64_tr_b16 v[234:235], v9 offset:14336\n"
    "s_waitcnt lgkmcnt(6)\n"
    "v_mfma_f32_16x16x32_bf16 v[120:123], v[16:19], a[144:147], v[120:123]\n"
    "ds_read_b64_tr_b16 v[236:237], v8 offset:8192\n"
    "s_add_u32 m0, s52, 65536\n"
    "v_mfma_f32_16x16x32_bf16 v[124:127], v[16:19], a[148:151], v[124:127]\n"
    "ds_read_b64_tr_b16 v[238:239], v8 offset:10240\n"
    "s_add_u32 s79, s59, 774144\n"
    "v_mfma_f32_16x16x32_bf16 v[64:67], v[20:23], a[152:155], v[64:67]\n"
    "ds_read_b64_tr_b16 v[16:17], v9 offset:8192\n"
    "buffer_load_dwordx4 v14, s[40:43], s79 offen lds\n"
    "v_mfma_f32_16x16x32_bf16 v[68:71], v[20:23], a[156:159], v[68:71]\n"
    "ds_read_b64_tr_b16 v[18:19], v9 offset:10240\n"
    "s_waitcnt lgkmcnt(6)\n"
    "v_mfma_f32_16x16x32_bf16 v[72:75], v[24:27], a[152:155], v[72:75]\n"
    "ds_read_b64_tr_b16 v[20:21], v10 offset:15360\n"
    "buffer_load_dwordx4 v2, s[40:43], s79 offen offset:1024 lds\n"
    "v_mfma_f32_16x16x32_bf16 v[76:79], v[24:27], a[156:159], v[76:79]\n"
    "ds_read_b64_tr_b16 v[22:23], v10 offset:15872\n"
    "v_mfma_f32_16x16x32_bf16 v[80:83], v[28:31], a[152:155], v[80:83]\n"
    "ds_read_b64_tr_b16 v[24:25], v6 offset:8192\n"
    "v_mfma_f32_16x16x32_bf16 v[84:87], v[28:31], a[156:159], v[84:87]\n"
    "ds_read_b64_tr_b16 v[26:27], v6 offset:10240\n"
    "s_waitcnt lgkmcnt(6)\n"
    "v_mfma_f32_16x16x32_bf16 v[88:91], v[232:235], a[152:155], v[88:91]\n"
    "ds_read_b64_tr_b16 v[28:29], v7 offset:8192\n"
    "buffer_load_dwordx4 v3, s[40:43], s79 offen offset:2048 lds\n"
    "v_mfma_f32_16x16x32_bf16 v[92:95], v[232:235], a[156:159], v[92:95]\n"
    "ds_read_b64_tr_b16 v[30:31], v7 offset:10240\n"
    "v_mfma_f32_16x16x32_bf16 v[96:99], v[236:239], a[152:155], v[96:99]\n"
    "v_add_u32 v6, 0x10000, v6\n"
    "ds_read_b64_tr_b16 v[232:233], v6 offset:53248\n"
    "buffer_load_dwordx4 v4, s[40:43], s79 offen offset:3072 lds\n"
    "v_mfma_f32_16x16x32_bf16 v[100:103], v[236:239], a[156:159], v[100:103]\n"
    "ds_read_b64_tr_b16 v[234:235], v6 offset:55296\n"
    "s_waitcnt lgkmcnt(6)\n"
    "v_mfma_f32_16x16x32_bf16 v[104:107], v[16:19], a[152:155], v[104:107]\n"
    "v_add_u32 v7, 0x10000, v7\n"
    "ds_read_b64_tr_b16 v[236:237], v7 offset:53248\n"
    "v_mfma_f32_16x16x32_bf16 v[108:111], v[16:19], a[156:159], v[108:111]\n"
    "ds_read_b64_tr_b16 v[238:239], v7 offset:55296\n"
    "v_mfma_f32_16x16x32_bf16 v[224:227], v[20:23], a[152:155], v[224:227]\n"
    "v_add_u32 v8, 0x10000, v8\n"
    "ds_read_b64_tr_b16 v[16:17], v8 offset:53248\n"
    "s_add_u32 m0, s52, 81920\n"
    "s_add_u32 s79, s59, 921600\n"
    "v_mfma_f32_16x16x32_bf16 v[228:231], v[20:23], a[156:159], v[228:231]\n"
    "ds_read_b64_tr_b16 v[18:19], v8 offset:55296\n"
    "s_waitcnt lgkmcnt(6)\n"
    "v_mfma_f32_16x16x32_bf16 v[112:115], v[24:27], a[152:155], v[112:115]\n"
    "v_add_u32 v9, 0x10000, v9\n"
    "ds_read_b64_tr_b16 v[20:21], v9 offset:53248\n"
    "buffer_load_dwordx4 v14, s[40:43], s79 offen lds\n"
    "v_mfma_f32_16x16x32_bf16 v[116:119], v[24:27], a[156:159], v[116:119]\n"
    "ds_read_b64_tr_b16 v[22:23], v9 offset:55296\n"
    "v_mfma_f32_16x16x32_bf16 v[120:123], v[28:31], a[152:155], v[120:123]\n"
    "ds_read_b64_tr_b16 v[24:25], v8 offset:49152\n"
    "buffer_load_dwordx4 v2, s[40:43], s79 offen offset:1024 lds\n"
    "v_mfma_f32_16x16x32_bf16 v[124:127], v[28:31], a[156:159], v[124:127]\n"
    "ds_read_b64_tr_b16 v[26:27], v8 offset:51200\n"
    "s_waitcnt vmcnt(40) lgkmcnt(6)\n"
    "v_mfma_f32_16x16x32_bf16 v[64:67], v[232:235], a[0:3], v[64:67]\n"
    "ds_read_b64_tr_b16 v[28:29], v9 offset:49152\n"
    "v_mfma_f32_16x16x32_bf16 v[68:71], v[232:235], a[4:7], v[68:71]\n"
    "ds_read_b64_tr_b16 v[30:31], v9 offset:51200\n"
    "buffer_load_dwordx4 v3, s[40:43], s79 offen offset:2048 lds\n"
    "v_mfma_f32_16x16x32_bf16 v[72:75], v[236:239], a[0:3], v[72:75]\n"
    "ds_read_b64_tr_b16 v[232:233], v10 offset:4096\n"
    "v_mfma_f32_16x16x32_bf16 v[76:79], v[236:239], a[4:7], v[76:79]\n"
    "ds_read_b64_tr_b16 v[234:235], v10 offset:4608\n"
    "s_waitcnt vmcnt(17)\n"
    "v_cmp_ne_u32 vcc, s93, v60\n"
    "s_cbranch_vccnz LS3_k\n"
    "LB3_k:\n"
    "s_barrier\n"
    "s_waitcnt lgkmcnt(6)\n"
    "v_mfma_f32_16x16x32_bf16 v[80:83], v[16:19], a[0:3], v[80:83]\n"
    "ds_read_b64_tr_b16 v[236:237], v6 offset:49152\n"
    "v_mfma_f32_16x16x32_bf16 v[84:87], v[16:19], a[4:7], v[84:87]\n"
    "ds_read_b64_tr_b16 v[238:239], v6 offset:51200\n"
    "buffer_load_dwordx4 v4, s[40:43], s79 offen offset:3072 lds\n"
    "v_mfma_f32_16x16x32_bf16 v[88:91], v[20:23], a[0:3], v[88:91]\n"
    "ds_read_b64_tr_b16 v[16:17], v7 offset:49152\n"
    "v_mfma_f32_16x16x32_bf16 v[92:95], v[20:23], a[4:7], v[92:95]\n"
    "ds_read_b64_tr_b16 v[18:19], v7 offset:51200\n"
    "s_waitcnt lgkmcnt(6)\n"
    "v_mfma_f32_16x16x32_bf16 v[96:99], v[24:27], a[0:3], v[96:99]\n"
    "ds_read_b64_tr_b16 v[20:21], v6 offset:61440\n"
    "buffer_load_dword v249, v11, s[44:47], 0 offen offset:1024 sc1\n"
    "v_mfma_f32_16x16x32_bf16 v[100:103], v[24:27], a[4:7], v[100:103]\n"
    "ds_read_b64_tr_b16 v[22:23], v6 offset:63488\n"
    "s_add_u32 m0, s52, 0\n"
    "v_mfma_f32_16x16x32_bf16 v[104:107], v[28:31], a[0:3], v[104:107]\n"
    "ds_read_b64_tr_b16 v[24:25], v7 offset:61440\n"
    "s_add_u32 s79, s59, 1069056\n"
    "v_mfma_f32_16x16x32_bf16 v[108:111], v[28:31], a[4:7], v[108:111]\n"
    "ds_read_b64_tr_b16 v[26:27], v7 offset:63488\n"
    "s_waitcnt lgkmcnt(6)\n"
    "v_mfma_f32_16x16x32_bf16 v[224:227], v[232:235], a[0:3], v[224:227]\n"
    "ds_read_b64_tr_b16 v[28:29], v8 offset:61440\n"
    "buffer_load_dwordx4 v14, s[40:43], s79 offen lds\n"
    "v_mfma_f32_16x16x32_bf16 v[228:231], v[232:235], a[4:7], v[228:231]\n"
    "ds_read_b64_tr_b16 v[30:31], v8 offset:63488\n"
    "buffer_load_dwordx4 v2, s[40:43], s79 offen offset:1024 lds\n"
    "v_mfma_f32_16x16x32_bf16 v[112:115], v[236:239], a[0:3], v[112:115]\n"
    "ds_read_b64_tr_b16 v[232:233], v9 offset:61440\n"
    "v_mfma_f32_16x16x32_bf16 v[116:119], v[236:239], a[4:7], v[116:119]\n"
    "ds_read_b64_tr_b16 v[234:235], v9 offset:63488\n"
    "s_waitcnt lgkmcnt(6)\n"
    "v_mfma_f32_16x16x32_bf16 v[120:123], v[16:19], a[0:3], v[120:123]\n"
    "ds_read_b64_tr_b16 v[236:237], v8 offset:57344\n"
    "v_mfma_f32_16x16x32_bf16 v[124:127], v[16:19], a[4:7], v[124:127]\n"
    "ds_read_b64_tr_b16 v[238:239], v8 offset:59392\n"
    "buffer_load_dwordx4 v3, s[40:43], s79 offen offset:2048 lds\n"
    "v_mfma_f32_16x16x32_bf16 v[64:67], v[20:23], a[8:11], v[64:67]\n"
    "ds_read_b64_tr_b16 v[16:17], v9 offset:57344\n"
    "v_mfma_f32_16x16x32_bf16 v[68:71], v[20:23], a[12:15], v[68:71]\n"
    "ds_read_b64_tr_b16 v[18:19], v9 offset:59392\n"
    "s_waitcnt lgkmcnt(6)\n"
    "v_mfma_f32_16x16x32_bf16 v[72:75], v[24:27], a[8:11], v[72:75]\n"
    "ds_read_b64_tr_b16 v[20:21], v10 offset:5120\n"
    "buffer_load_dwordx4 v4, s[40:43], s79 offen offset:3072 lds\n"
    "v_mfma_f32_16x16x32_bf16 v[76:79], v[24:27], a[12:15], v[76:79]\n"
    "ds_read_b64_tr_b16 v[22:23], v10 offset:5632\n"
    "v_mfma_f32_16x16x32_bf16 v[80:83], v[28:31], a[8:11], v[80:83]\n"
    "ds_read_b64_tr_b16 v[24:25], v6 offset:57344\n"
    "v_mfma_f32_16x16x32_bf16 v[84:87], v[28:31], a[12:15], v[84:87]\n"
    "ds_read_b64_tr_b16 v[26:27], v6 offset:59392\n"
    "s_waitcnt lgkmcnt(6)\n"
    "v_mfma_f32_16x16x32_bf16 v[88:91], v[232:235], a[8:11], v[88:91]\n"
    "ds_read_b64_tr_b16 v[28:29], v7 offset:57344\n"
    "s_add_u32 m0, s52, 98304\n"
    "v_mfma_f32_16x16x32_bf16 v[92:95], v[232:235], a[12:15], v[92:95]\n"
    "ds_read_b64_tr_b16 v[30:31], v7 offset:59392\n"
    "s_add_u32 s79, s60, 0\n"
    "v_mfma_f32_16x16x32_bf16 v[96:99], v[236:239], a[8:11], v[96:99]\n"
    "v_add_u32 v6, 0xffff0000, v6\n"
    "ds_read_b64_tr_b16 v[232:233], v6 offset:20480\n"
    "buffer_load_dwordx4 v14, s[40:43], s79 offen lds\n"
    "v_mfma_f32_16x16x32_bf16 v[100:103], v[236:239], a[12:15], v[100:103]\n"
    "ds_read_b64_tr_b16 v[234:235], v6 offset:22528\n"
    "s_waitcnt lgkmcnt(6)\n"
    "v_mfma_f32_16x16x32_bf16 v[104:107], v[16:19], a[8:11], v[104:107]\n"
    "v_add_u32 v7, 0xffff0000, v7\n"
    "ds_read_b64_tr_b16 v[236:237], v7 offset:20480\n"
    "buffer_load_dwordx4 v2, s[40:43], s79 offen offset:1024 lds\n"
    "v_mfma_f32_16x16x32_bf16 v[108:111], v[16:19], a[12:15], v[108:111]\n"
    "ds_read_b64_tr_b16 v[238:239], v7 offset:22528\n"
    "v_mfma_f32_16x16x32_bf16 v[224:227], v[20:23], a[8:11], v[224:227]\n"
    "v_add_u32 v8, 0xffff0000, v8\n"
    "ds_read_b64_tr_b16 v[16:17], v8 offset:20480\n"
    "v_mfma_f32_16x16x32_bf16 v[228:231], v[20:23], a[12:15], v[228:231]\n"
    "ds_read_b64_tr_b16 v[18:19], v8 offset:22528\n"
    "s_waitcnt lgkmcnt(6)\n"
    "v_mfma_f32_16x16x32_bf16 v[112:115], v[24:27], a[8:11], v[112:115]\n"
    "v_add_u32 v9, 0xffff0000, v9\n"
    "ds_read_b64_tr_b16 v[20:21], v9 offset:20480\n"
    "buffer_load_dwordx4 v3, s[40:43], s79 offen offset:2048 lds\n"
    "v_mfma_f32_16x16x32_bf16 v[116:119], v[24:27], a[12:15], v[116:119]\n"
    "ds_read_b64_tr_b16 v[22:23], v9 offset:22528\n"
    "v_mfma_f32_16x16x32_bf16 v[120:123], v[28:31], a[8:11], v[120:123]\n"
    "ds_read_b64_tr_b16 v[24:25], v8 offset:16384\n"
    "v_mfma_f32_16x16x32_bf16 v[124:127], v[28:31], a[12:15], v[124:127]\n"
    "ds_read_b64_tr_b16 v[26:27], v8 offset:18432\n"
    "s_waitcnt lgkmcnt(6)\n"
    "v_mfma_f32_16x16x32_bf16 v[64:67], v[232:235], a[16:19], v[64:67]\n"
    "ds_read_b64_tr_b16 v[28:29], v9 offset:16384\n"
    "buffer_load_dwordx4 v4, s[40:43], s79 offen offset:3072 lds\n"
    "v_mfma_f32_16x16x32_bf16 v[68:71], v[232:235], a[20:23], v[68:71]\n"
    "ds_read_b64_tr_b16 v[30:31], v9 offset:18432\n"
    "v_mfma_f32_16x16x32_bf16 v[72:75], v[236:239], a[16:19], v[72:75]\n"
    "ds_read_b64_tr_b16 v[232:233], v10 offset:6144\n"
    "s_add_u32 s33, s63, 49152\n"
    "v_mfma_f32_16x16x32_bf16 v[76:79], v[236:239], a[20:23], v[76:79]\n"
    "ds_read_b64_tr_b16 v[234:235], v10 offset:6656\n"
    "s_waitcnt lgkmcnt(6)\n"
    "v_mfma_f32_16x16x32_bf16 v[80:83], v[16:19], a[16:19], v[80:83]\n"
    "ds_read_b64_tr_b16 v[236:237], v6 offset:16384\n"
    "buffer_load_dwordx4 a[0:3], v0, s[44:47], s33 offen offset:0 sc1\n"
    "v_mfma_f32_16x16x32_bf16 v[84:87], v[16:19], a[20:23], v[84:87]\n"
    "ds_read_b64_tr_b16 v[238:239], v6 offset:18432\n"
    "v_mfma_f32_16x16x32_bf16 v[88:91], v[20:23], a[16:19], v[88:91]\n"
    "ds_read_b64_tr_b16 v[16:17], v7 offset:16384\n"
    "buffer_load_dwordx4 a[4:7], v0, s[44:47], s33 offen offset:1024 sc1\n"
    "v_mfma_f32_16x16x32_bf16 v[92:95], v[20:23], a[20:23], v[92:95]\n"
    "ds_read_b64_tr_b16 v[18:19], v7 offset:18432\n"
    "s_waitcnt lgkmcnt(6)\n"
    "v_mfma_f32_16x16x32_bf16 v[96:99], v[24:27], a[16:19], v[96:99]\n"
    "ds_read_b64_tr_b16 v[20:21], v6 offset:28672\n"
    "v_mfma_f32_16x16x32_bf16 v[100:103], v[24:27], a[20:23], v[100:103]\n"
    "ds_read_b64_tr_b16 v[22:23], v6 offset:30720\n"
    "buffer_load_dwordx4 a[8:11], v0, s[44:47], s33 offen offset:2048 sc1\n"
    "v_mfma_f32_16x16x32_bf16 v[104:107], v[28:31], a[16:19], v[104:107]\n"
    "ds_read_b64_tr_b16 v[24:25], v7 offset:28672\n"
    "v_mfma_f32_16x16x32_bf16 v[108:111], v[28:31], a[20:23], v[108:111]\n"
    "ds_read_b64_tr_b16 v[26:27], v7 offset:30720\n"
    "s_waitcnt lgkmcnt(6)\n"
    "v_mfma_f32_16x16x32_bf16 v[224:227], v[232:235], a[16:19], v[224:227]\n"
    "ds_read_b64_tr_b16 v[28:29], v8 offset:28672\n"
    "v_mfma_f32_16x16x32_bf16 v[228:231], v[232:235], a[20:23], v[228:231]\n"
    "ds_read_b64_tr_b16 v[30:31], v8 offset:30720\n"
    "buffer_load_dwordx4 a[12:15], v0, s[44:47], s33 offen offset:3072 sc1\n"
    "v_mfma_f32_16x16x32_bf16 v[112:115], v[236:239], a[16:19], v[112:115]\n"
    "ds_read_b64_tr_b16 v[232:233], v9 offset:28672\n"
    "v_mfma_f32_16x16x32_bf16 v[116:119], v[236:239], a[20:23], v[116:119]\n"
    "ds_read_b64_tr_b16 v[234:235], v9 offset:30720\n"
    "s_waitcnt lgkmcnt(6)\n"
    "v_mfma_f32_16x16x32_bf16 v[120:123], v[16:19], a[16:19], v[120:123]\n"
    "ds_read_b64_tr_b16 v[236:237], v8 offset:24576\n"
    "v_mfma_f32_16x16x32_bf16 v[124:127], v[16:19], a[20:23], v[124:127]\n"
    "ds_read_b64_tr_b16 v[238:239], v8 offset:26624\n"
    "s_add_u32 s33, s63, 53248\n"
    "v_mfma_f32_16x16x32_bf16 v[64:67], v[20:23], a[24:27], v[64:67]\n"
    "ds_read_b64_tr_b16 v[16:17], v9 offset:24576\n"
    "buffer_load_dwordx4 a[16:19], v0, s[44:47], s33 offen offset:0 sc1\n"
    "v_mfma_f32_16x16x32_bf16 v[68:71], v[20:23], a[28:31], v[68:71]\n"
    "ds_read_b64_tr_b16 v[18:19], v9 offset:26624\n"
    "s_waitcnt lgkmcnt(6)\n"
    "v_mfma_f32_16x16x32_bf16 v[72:75], v[24:27], a[24:27], v[72:75]\n"
    "ds_read_b64_tr_b16 v[20:21], v10 offset:7168\n"
    "buffer_load_dwordx4 a[20:23], v0, s[44:47], s33 offen offset:1024 sc1\n"
    "v_mfma_f32_16x16x32_bf16 v[76:79], v[24:27], a[28:31], v[76:79]\n"
    "ds_read_b64_tr_b16 v[22:23], v10 offset:7680\n"
    "v_mfma_f32_16x16x32_bf16 v[80:83], v[28:31], a[24:27], v[80:83]\n"
    "ds_read_b64_tr_b16 v[24:25], v6 offset:24576\n"
    "v_mfma_f32_16x16x32_bf16 v[84:87], v[28:31], a[28:31], v[84:87]\n"
    "ds_read_b64_tr_b16 v[26:27], v6 offset:26624\n"
    "s_waitcnt lgkmcnt(6)\n"
    "v_mfma_f32_16x16x32_bf16 v[88:91], v[232:235], a[24:27], v[88:91]\n"
    "ds_read_b64_tr_b16 v[28:29], v7 offset:24576\n"
    "buffer_load_dwordx4 a[64:67], v0, s[44:47], s33 offen offset:2048 sc1\n"
    "v_mfma_f32_16x16x32_bf16 v[92:95], v[232:235], a[28:31], v[92:95]\n"
    "ds_read_b64_tr_b16 v[30:31], v7 offset:26624\n"
    "v_mfma_f32_16x16x32_bf16 v[96:99], v[236:239], a[24:27], v[96:99]\n"
    "v_add_u32 v6, 0x20000, v6\n"
    "ds_read_b64_tr_b16 v[232:233], v6 offset:4096\n"
    "v_mfma_f32_16x16x32_bf16 v[100:103], v[236:239], a[28:31], v[100:103]\n"
    "ds_read_b64_tr_b16 v[234:235], v6 offset:6144\n"
    "s_waitcnt lgkmcnt(6)\n"
    "v_mfma_f32_16x16x32_bf16 v[104:107], v[16:19], a[24:27], v[104:107]\n"
    "v_add_u32 v7, 0x20000, v7\n"
    "ds_read_b64_tr_b16 v[236:237], v7 offset:4096\n"
    "buffer_load_dwordx4 a[68:71], v0, s[44:47], s33 offen offset:3072 sc1\n"
    "v_mfma_f32_16x16x32_bf16 v[108:111], v[16:19], a[28:31], v[108:111]\n"
    "ds_read_b64_tr_b16 v[238:239], v7 offset:6144\n"
    "v_mfma_f32_16x16x32_bf16 v[224:227], v[20:23], a[24:27], v[224:227]\n"
    "v_add_u32 v8, 0x20000, v8\n"
    "ds_read_b64_tr_b16 v[16:17], v8 offset:4096\n"
    "v_mfma_f32_16x16x32_bf16 v[228:231], v[20:23], a[28:31], v[228:231]\n"
    "ds_read_b64_tr_b16 v[18:19], v8 offset:6144\n"
    "s_waitcnt vmcnt(16)\n"
    "v_cmp_ne_u32 vcc, s93, v249\n"
    "s_cbranch_vccnz LS4_k\n"
    "LB4_k:\n"
    "s_barrier\n"
    "s_waitcnt lgkmcnt(6)\n"
    "v_mfma_f32_16x16x32_bf16 v[112:115], v[24:27], a[24:27], v[112:115]\n"
    "v_add_u32 v9, 0x20000, v9\n"
    "ds_read_b64_tr_b16 v[20:21], v9 offset:4096\n"
    "s_add_u32 s33, s63, 57344\n"
    "v_mfma_f32_16x16x32_bf16 v[116:119], v[24:27], a[28:31], v[116:119]\n"
    "ds_read_b64_tr_b16 v[22:23], v9 offset:6144\n"
    "buffer_load_dwordx4 a[72:75], v0, s[44:47], s33 offen offset:0 sc1\n"
    "v_mfma_f32_16x16x32_bf16 v[120:123], v[28:31], a[24:27], v[120:123]\n"
    "ds_read_b64_tr_b16 v[24:25], v8 offset:0\n"
    "v_mfma_f32_16x16x32_bf16 v[124:127], v[28:31], a[28:31], v[124:127]\n"
    "ds_read_b64_tr_b16 v[26:27], v8 offset:2048\n"
    "s_waitcnt lgkmcnt(6)\n"
    "v_mfma_f32_16x16x32_bf16 v[64:67], v[232:235], a[32:35], v[64:67]\n"
    "ds_read_b64_tr_b16 v[28:29], v9 offset:0\n"
    "s_add_u32 m0, s52, 114688\n"
    "v_mfma_f32_16x16x32_bf16 v[68:71], v[232:235], a[36:39], v[68:71]\n"
    "ds_read_b64_tr_b16 v[30:31], v9 offset:2048\n"
    "s_add_u32 s79, s60, 73728\n"
    "v_mfma_f32_16x16x32_bf16 v[72:75], v[236:239], a[32:35], v[72:75]\n"
    "ds_read_b64_tr_b16 v[232:233], v10 offset:8192\n"
    "buffer_load_dwordx4 v14, s[40:43], s79 offen lds\n"
    "v_mfma_f32_16x16x32_bf16 v[76:79], v[236:239], a[36:39], v[76:79]\n"
    "ds_read_b64_tr_b16 v[234:235], v10 offset:8704\n"
    "s_waitcnt lgkmcnt(6)\n"
    "v_mfma_f32_16x16x32_bf16 v[80:83], v[16:19], a[32:35], v[80:83]\n"
    "ds_read_b64_tr_b16 v[236:237], v6 offset:0\n"
    "buffer_load_dwordx4 v2, s[40:43], s79 offen offset:1024 lds\n"
    "v_mfma_f32_16x16x32_bf16 v[84:87], v[16:19], a[36:39], v[84:87]\n"
    "ds_read_b64_tr_b16 v[238:239], v6 offset:2048\n"
    "v_mfma_f32_16x16x32_bf16 v[88:91], v[20:23], a[32:35], v[88:91]\n"
    "ds_read_b64_tr_b16 v[16:17], v7 offset:0\n"
    "buffer_load_dwordx4 v3, s[40:43], s79 offen offset:2048 lds\n"
    "v_mfma_f32_16x16x32_bf16 v[92:95], v[20:23], a[36:39], v[92:95]\n"
    "ds_read_b64_tr_b16 v[18:19], v7 offset:2048\n"
    "s_waitcnt lgkmcnt(6)\n"
    "v_mfma_f32_16x16x32_bf16 v[96:99], v[24:27], a[32:35], v[96:99]\n"
    "ds_read_b64_tr_b16 v[20:21], v6 offset:12288\n"
    "v_mfma_f32_16x16x32_bf16 v[100:103], v[24:27], a[36:39], v[100:103]\n"
    "ds_read_b64_tr_b16 v[22:23], v6 offset:14336\n"
    "buffer_load_dwordx4 v4, s[40:43], s79 offen offset:3072 lds\n"
    "v_mfma_f32_16x16x32_bf16 v[104:107], v[28:31], a[32:35], v[104:107]\n"
    "ds_read_b64_tr_b16 v[24:25], v7 offset:12288\n"
    "v_mfma_f32_16x16x32_bf16 v[108:111], v[28:31], a[36:39], v[108:111]\n"
    "ds_read_b64_tr_b16 v[26:27], v7 offset:14336\n"
    "s_waitcnt lgkmcnt(6)\n"
    "v_mfma_f32_16x16x32_bf16 v[224:227], v[232:235], a[32:35], v[224:227]\n"
    "ds_read_b64_tr_b16 v[28:29], v8 offset:12288\n"
    "v_mfma_f32_16x16x32_bf16 v[228:231], v[232:235], a[36:39], v[228:231]\n"
    "ds_read_b64_tr_b16 v[30:31], v8 offset:14336\n"
    "buffer_load_dwordx4 a[76:79], v0, s[44:47], s33 offen offset:1024 sc1\n"
    "v_mfma_f32_16x16x32_bf16 v[112:115], v[236:239], a[32:35], v[112:115]\n"
    "ds_read_b64_tr_b16 v[232:233], v9 offset:12288\n"
    "v_mfma_f32_16x16x32_bf16 v[116:119], v[236:239], a[36:39], v[116:119]\n"
    "ds_read_b64_tr_b16 v[234:235], v9 offset:14336\n"
    "s_waitcnt lgkmcnt(6)\n"
    "v_mfma_f32_16x16x32_bf16 v[120:123], v[16:19], a[32:35], v[120:123]\n"
    "ds_read_b64_tr_b16 v[236:237], v8 offset:8192\n"
    "v_mfma_f32_16x16x32_bf16 v[124:127], v[16:19], a[36:39], v[124:127]\n"
    "ds_read_b64_tr_b16 v[238:239], v8 offset:10240\n"
    "buffer_load_dwordx4 a[24:27], v0, s[44:47], s33 offen offset:2048 sc1\n"
    "v_mfma_f32_16x16x32_bf16 v[64:67], v[20:23], a[40:43], v[64:67]\n"
    "ds_read_b64_tr_b16 v[16:17], v9 offset:8192\n"
    "v_mfma_f32_16x16x32_bf16 v[68:71], v[20:23], a[44:47], v[68:71]\n"
    "ds_read_b64_tr_b16 v[18:19], v9 offset:10240\n"
    "s_waitcnt lgkmcnt(6)\n"
    "v_mfma_f32_16x16x32_bf16 v[72:75], v[24:27], a[40:43], v[72:75]\n"
    "ds_read_b64_tr_b16 v[20:21], v10 offset:9216\n"
    "buffer_load_dwordx4 a[28:31], v0, s[44:47], s33 offen offset:3072 sc1\n"
    "v_mfma_f32_16x16x32_bf16 v[76:79], v[24:27], a[44:47], v[76:79]\n"
    "ds_read_b64_tr_b16 v[22:23], v10 offset:9728\n"
    "v_mfma_f32_16x16x32_bf16 v[80:83], v[28:31], a[40:43], v[80:83]\n"
    "ds_read_b64_tr_b16 v[24:25], v6 offset:8192\n"
    "v_mfma_f32_16x16x32_bf16 v[84:87], v[28:31], a[44:47], v[84:87]\n"
    "ds_read_b64_tr_b16 v[26:27], v6 offset:10240\n"
    "s_waitcnt lgkmcnt(6)\n"
    "v_mfma_f32_16x16x32_bf16 v[88:91], v[232:235], a[40:43], v[88:91]\n"
    "ds_read_b64_tr_b16 v[28:29], v7 offset:8192\n"
    "s_add_u32 s33, s63, 61440\n"
    "v_mfma_f32_16x16x32_bf16 v[92:95], v[232:235], a[44:47], v[92:95]\n"
    "ds_read_b64_tr_b16 v[30:31], v7 offset:10240\n"
    "buffer_load_dwordx4 a[32:35], v0, s[44:47], s33 offen offset:0 sc1\n"
    "v_mfma_f32_16x16x32_bf16 v[96:99], v[236:239], a[40:43], v[96:99]\n"
    "v_add_u32 v6, 0xfffe0000, v6\n"
    "ds_read_b64_tr_b16 v[232:233], v6 offset:36864\n"
    "v_mfma_f32_16x16x32_bf16 v[100:103], v[236:239], a[44:47], v[100:103]\n"
    "ds_read_b64_tr_b16 v[234:235], v6 offset:38912\n"
    "s_waitcnt lgkmcnt(6)\n"
    "v_mfma_f32_16x16x32_bf16 v[104:107], v[16:19], a[40:43], v[104:107]\n"
    "v_add_u32 v7, 0xfffe0000, v7\n"
    "ds_read_b64_tr_b16 v[236:237], v7 offset:36864\n"
    "buffer_load_dwordx4 a[36:39], v0, s[44:47], s33 offen offset:1024 sc1\n"
    "v_mfma_f32_16x16x32_bf16 v[108:111], v[16:19], a[44:47], v[108:111]\n"
    "ds_read_b64_tr_b16 v[238:239], v7 offset:38912\n"
    "v_mfma_f32_16x16x32_bf16 v[224:227], v[20:23], a[40:43], v[224:227]\n"
    "v_add_u32 v8, 0xfffe0000, v8\n"
    "ds_read_b64_tr_b16 v[16:17], v8 offset:36864\n"
    "v_mfma_f32_16x16x32_bf16 v[228:231], v[20:23], a[44:47], v[228:231]\n"
    "ds_read_b64_tr_b16 v[18:19], v8 offset:38912\n"
    "s_waitcnt lgkmcnt(6)\n"
    "v_mfma_f32_16x16x32_bf16 v[112:115], v[24:27], a[40:43], v[112:115]\n"
    "v_add_u32 v9, 0xfffe0000, v9\n"
    "ds_read_b64_tr_b16 v[20:21], v9 offset:36864\n"
    "buffer_load_dwordx4 a[80:83], v0, s[44:47], s33 offen offset:2048 sc1\n"
    "v_mfma_f32_16x16x32_bf16 v[116:119], v[24:27], a[44:47], v[116:119]\n"
    "ds_read_b64_tr_b16 v[22:23], v9 offset:38912\n"
    "v_mfma_f32_16x16x32_bf16 v[120:123], v[28:31], a[40:43], v[120:123]\n"
    "ds_read_b64_tr_b16 v[24:25], v8 offset:32768\n"
    "v_mfma_f32_16x16x32_bf16 v[124:127], v[28:31], a[44:47], v[124:127]\n"
    "ds_read_b64_tr_b16 v[26:27], v8 offset:34816\n"
    "s_waitcnt lgkmcnt(6)\n"
    "v_mfma_f32_16x16x32_bf16 v[64:67], v[232:235], a[48:51], v[64:67]\n"
    "ds_read_b64_tr_b16 v[28:29], v9 offset:32768\n"
    "buffer_load_dwordx4 a[84:87], v0, s[44:47], s33 offen offset:3072 sc1\n"
    "v_mfma_f32_16x16x32_bf16 v[68:71], v[232:235], a[52:55], v[68:71]\n"
    "ds_read_b64_tr_b16 v[30:31], v9 offset:34816\n"
    "v_mfma_f32_16x16x32_bf16 v[72:75], v[236:239], a[48:51], v[72:75]\n"
    "ds_read_b64_tr_b16 v[232:233], v10 offset:10240\n"
    "s_add_u32 s33, s63, 65536\n"
    "v_mfma_f32_16x16x32_bf16 v[76:79], v[236:239], a[52:55], v[76:79]\n"
    "ds_read_b64_tr_b16 v[234:235], v10 offset:10752\n"
    "s_waitcnt lgkmcnt(6)\n"
    "v_mfma_f32_16x16x32_bf16 v[80:83], v[16:19], a[48:51], v[80:83]\n"
    "ds_read_b64_tr_b16 v[236:237], v6 offset:32768\n"
    "buffer_load_dwordx2 a[40:41], v62, s[44:47], s33 offen offset:0 sc1\n"
    "v_mfma_f32_16x16x32_bf16 v[84:87], v[16:19], a[52:55], v[84:87]\n"
    "ds_read_b64_tr_b16 v[238:239], v6 offset:34816\n"
    "buffer_load_dwordx2 a[42:43], v62, s[44:47], s33 offen offset:512 sc1\n"
    "v_mfma_f32_16x16x32_bf16 v[88:91], v[20:23], a[48:51], v[88:91]\n"
    "ds_read_b64_tr_b16 v[16:17], v7 offset:32768\n"
    "buffer_load_dwordx2 a[44:45], v62, s[44:47], s33 offen offset:1024 sc1\n"
    "v_mfma_f32_16x16x32_bf16 v[92:95], v[20:23], a[52:55], v[92:95]\n"
    "ds_read_b64_tr_b16 v[18:19], v7 offset:34816\n"
    "s_waitcnt lgkmcnt(6)\n"
    "v_mfma_f32_16x16x32_bf16 v[96:99], v[24:27], a[48:51], v[96:99]\n"
    "ds_read_b64_tr_b16 v[20:21], v6 offset:45056\n"
    "buffer_load_dwordx2 a[46:47], v62, s[44:47], s33 offen offset:1536 sc1\n"
    "v_mfma_f32_16x16x32_bf16 v[100:103], v[24:27], a[52:55], v[100:103]\n"
    "ds_read_b64_tr_b16 v[22:23], v6 offset:47104\n"
    "buffer_load_dwordx2 a[88:89], v62, s[44:47], s33 offen offset:2048 sc1\n"
    "v_mfma_f32_16x16x32_bf16 v[104:107], v[28:31], a[48:51], v[104:107]\n"
    "ds_read_b64_tr_b16 v[24:25], v7 offset:45056\n"
    "v_mfma_f32_16x16x32_bf16 v[108:111], v[28:31], a[52:55], v[108:111]\n"
    "ds_read_b64_tr_b16 v[26:27], v7 offset:47104\n"
    "s_waitcnt lgkmcnt(6)\n"
    "v_mfma_f32_16x16x32_bf16 v[224:227], v[232:235], a[48:51], v[224:227]\n"
    "ds_read_b64_tr_b16 v[28:29], v8 offset:45056\n"
    "buffer_load_dwordx2 a[90:91], v62, s[44:47], s33 offen offset:2560 sc1\n"
    "v_mfma_f32_16x16x32_bf16 v[228:231], v[232:235], a[52:55], v[228:231]\n"
    "ds_read_b64_tr_b16 v[30:31], v8 offset:47104\n"
    "buffer_load_dwordx2 a[92:93], v62, s[44:47], s33 offen offset:3072 sc1\n"
    "v_mfma_f32_16x16x32_bf16 v[112:115], v[236:239], a[48:51], v[112:115]\n"
    "ds_read_b64_tr_b16 v[232:233], v9 offset:45056\n"
    "v_mfma_f32_16x16x32_bf16 v[116:119], v[236:239], a[52:55], v[116:119]\n"
    "ds_read_b64_tr_b16 v[234:235], v9 offset:47104\n"
    "s_waitcnt lgkmcnt(6)\n"
    "v_mfma_f32_16x16x32_bf16 v[120:123], v[16:19], a[48:51], v[120:123]\n"
    "ds_read_b64_tr_b16 v[236:237], v8 offset:40960\n"
    "buffer_load_dwordx2 a[94:95], v62, s[44:47], s33 offen offset:3584 sc1\n"
    "v_mfma_f32_16x16x32_bf16 v[124:127], v[16:19], a[52:55], v[124:127]\n"
    "ds_read_b64_tr_b16 v[238:239], v8 offset:43008\n"
    "s_add_u32 s33, s63, 69632\n"
    "v_mfma_f32_16x16x32_bf16 v[64:67], v[20:23], a[56:59], v[64:67]\n"
    "ds_read_b64_tr_b16 v[16:17], v9 offset:40960\n"
    "buffer_load_dwordx2 a[48:49], v62, s[44:47], s33 offen offset:0 sc1\n"
    "v_mfma_f32_16x16x32_bf16 v[68:71], v[20:23], a[60:63], v[68:71]\n"
    "ds_read_b64_tr_b16 v[18:19], v9 offset:43008\n"
    "s_waitcnt lgkmcnt(6)\n"
    "v_mfma_f32_16x16x32_bf16 v[72:75], v[24:27], a[56:59], v[72:75]\n"
    "ds_read_b64_tr_b16 v[20:21], v10 offset:11264\n"
    "buffer_load_dwordx2 a[50:51], v62, s[44:47], s33 offen offset:512 sc1\n"
    "v_mfma_f32_16x16x32_bf16 v[76:79], v[24:27], a[60:63], v[76:79]\n"
    "ds_read_b64_tr_b16 v[22:23], v10 offset:11776\n"
    "buffer_load_dwordx2 a[52:53], v62, s[44:47], s33 offen offset:1024 sc1\n"
    "v_mfma_f32_16x16x32_bf16 v[80:83], v[28:31], a[56:59], v[80:83]\n"
    "ds_read_b64_tr_b16 v[24:25], v6 offset:40960\n"
    "buffer_load_dwordx2 a[54:55], v62, s[44:47], s33 offen offset:1536 sc1\n"
    "v_mfma_f32_16x16x32_bf16 v[84:87], v[28:31], a[60:63], v[84:87]\n"
    "ds_read_b64_tr_b16 v[26:27], v6 offset:43008\n"
    "s_waitcnt vmcnt(19)\n"
    "s_barrier\n"
    "s_waitcnt lgkmcnt(6)\n"
    "v_mfma_f32_16x16x32_bf16 v[88:91], v[232:235], a[56:59], v[88:91]\n"
    "ds_read_b64_tr_b16 v[28:29], v7 offset:40960\n"
    "buffer_load_dwordx2 a[96:97], v62, s[44:47], s33 offen offset:2048 sc1\n"
    "v_mfma_f32_16x16x32_bf16 v[92:95], v[232:235], a[60:63], v[92:95]\n"
    "ds_read_b64_tr_b16 v[30:31], v7 offset:43008\n"
    "v_mfma_f32_16x16x32_bf16 v[96:99], v[236:239], a[56:59], v[96:99]\n"
    "ds_read_b64_tr_b16 v[232:233], v6 offset:53248\n"
    "buffer_load_dwordx2 a[98:99], v62, s[44:47], s33 offen offset:2560 sc1\n"
    "v_mfma_f32_16x16x32_bf16 v[100:103], v[236:239], a[60:63], v[100:103]\n"
    "ds_read_b64_tr_b16 v[234:235], v6 offset:55296\n"
    "s_waitcnt lgkmcnt(6)\n"
    "v_mfma_f32_16x16x32_bf16 v[104:107], v[16:19], a[56:59], v[104:107]\n"
    "ds_read_b64_tr_b16 v[236:237], v7 offset:53248\n"
    "buffer_load_dwordx2 a[100:101], v62, s[44:47], s33 offen offset:3072 sc1\n"
    "v_mfma_f32_16x16x32_bf16 v[108:111], v[16:19], a[60:63], v[108:111]\n"
    "ds_read_b64_tr_b16 v[238:239], v7 offset:55296\n"
    "buffer_load_dwordx2 a[102:103], v62, s[44:47], s33 offen offset:3584 sc1\n"
    "v_mfma_f32_16x16x32_bf16 v[224:227], v[20:23], a[56:59], v[224:227]\n"
    "ds_read_b64_tr_b16 v[16:17], v6 offset:61440\n"
    "v_mfma_f32_16x16x32_bf16 v[228:231], v[20:23], a[60:63], v[228:231]\n"
    "ds_read_b64_tr_b16 v[18:19], v6 offset:63488\n"
    "s_waitcnt lgkmcnt(6)\n"
    "v_mfma_f32_16x16x32_bf16 v[112:115], v[24:27], a[56:59], v[112:115]\n"
    "ds_read_b64_tr_b16 v[20:21], v7 offset:61440\n"
    "s_add_u32 m0, s53, 0\n"
    "v_mfma_f32_16x16x32_bf16 v[116:119], v[24:27], a[60:63], v[116:119]\n"
    "ds_read_b64_tr_b16 v[22:23], v7 offset:63488\n"
    "s_add_u32 s79, s61, 626688\n"
    "v_mfma_f32_16x16x32_bf16 v[120:123], v[28:31], a[56:59], v[120:123]\n"
    "v_add_u32 v6, 0x10000, v6\n"
    "ds_read_b64_tr_b16 v[24:25], v6 offset:4096\n"
    "buffer_load_dwordx4 v15, s[40:43], s79 offen lds\n"
    "v_mfma_f32_16x16x32_bf16 v[124:127], v[28:31], a[60:63], v[124:127]\n"
    "ds_read_b64_tr_b16 v[26:27], v6 offset:6144\n"
    "s_waitcnt lgkmcnt(6)\n"
    "v_mfma_f32_16x16x32_bf16 v[64:67], v[232:235], a[0:3], v[64:67]\n"
    "v_add_u32 v7, 0x10000, v7\n"
    "ds_read_b64_tr_b16 v[28:29], v7 offset:4096\n"
    "v_mfma_f32_16x16x32_bf16 v[68:71], v[232:235], a[4:7], v[68:71]\n"
    "ds_read_b64_tr_b16 v[30:31], v7 offset:6144\n"
    "v_mfma_f32_16x16x32_bf16 v[72:75], v[236:239], a[0:3], v[72:75]\n"
    "ds_read_b64_tr_b16 v[232:233], v6 offset:12288\n"
    "v_mfma_f32_16x16x32_bf16 v[76:79], v[236:239], a[4:7], v[76:79]\n"
    "ds_read_b64_tr_b16 v[234:235], v6 offset:14336\n"
    "s_waitcnt lgkmcnt(6)\n"
    "v_mfma_f32_16x16x32_bf16 v[64:67], v[16:19], a[8:11], v[64:67]\n"
    "ds_read_b64_tr_b16 v[236:237], v7 offset:12288\n"
    "v_mfma_f32_16x16x32_bf16 v[68:71], v[16:19], a[12:15], v[68:71]\n"
    "ds_read_b64_tr_b16 v[238:239], v7 offset:14336\n"
    "v_mfma_f32_16x16x32_bf16 v[72:75], v[20:23], a[8:11], v[72:75]\n"
    "ds_read_b64_tr_b16 v[16:17], v6 offset:20480\n"
    "v_mfma_f32_16x16x32_bf16 v[76:79], v[20:23], a[12:15], v[76:79]\n"
    "ds_read_b64_tr_b16 v[18:19], v6 offset:22528\n"
    "s_waitcnt lgkmcnt(6)\n"
    "v_mfma_f32_16x16x32_bf16 v[64:67], v[24:27], a[16:19], v[64:67]\n"
    "ds_read_b64_tr_b16 v[20:21], v7 offset:20480\n"
    "s_add_u32 m0, s53, 4096\n"
    "v_mfma_f32_16x16x32_bf16 v[68:71], v[24:27], a[20:23], v[68:71]\n"
    "ds_read_b64_tr_b16 v[22:23], v7 offset:22528\n"
    "s_add_u32 s79, s61, 921600\n"
    "v_mfma_f32_16x16x32_bf16 v[72:75], v[28:31], a[16:19], v[72:75]\n"
    "ds_read_b64_tr_b16 v[24:25], v6 offset:28672\n"
    "buffer_load_dwordx4 v15, s[40:43], s79 offen lds\n"
    "v_mfma_f32_16x16x32_bf16 v[76:79], v[28:31], a[20:23], v[76:79]\n"
    "ds_read_b64_tr_b16 v[26:27], v6 offset:30720\n"
    "s_waitcnt lgkmcnt(6)\n"
    "v_mfma_f32_16x16x32_bf16 v[64:67], v[232:235], a[64:67], v[64:67]\n"
    "ds_read_b64_tr_b16 v[28:29], v7 offset:28672\n"
    "v_mfma_f32_16x16x32_bf16 v[68:71], v[232:235], a[68:71], v[68:71]\n"
    "ds_read_b64_tr_b16 v[30:31], v7 offset:30720\n"
    "v_mfma_f32_16x16x32_bf16 v[72:75], v[236:239], a[64:67], v[72:75]\n"
    "v_add_u32 v6, 0xffff0000, v6\n"
    "ds_read_b64_tr_b16 v[232:233], v6 offset:4096\n"
    "v_mfma_f32_16x16x32_bf16 v[76:79], v[236:239], a[68:71], v[76:79]\n"
    "ds_read_b64_tr_b16 v[234:235], v6 offset:6144\n"
    "s_waitcnt vmcnt(24) lgkmcnt(6)\n"
    "v_mfma_f32_16x16x32_bf16 v[64:67], v[16:19], a[72:75], v[64:67]\n"
    "v_add_u32 v7, 0xffff0000, v7\n"
    "ds_read_b64_tr_b16 v[236:237], v7 offset:4096\n"
    "v_mfma_f32_16x16x32_bf16 v[68:71], v[16:19], a[76:79], v[68:71]\n"
    "ds_read_b64_tr_b16 v[238:239], v7 offset:6144\n"
    "v_mfma_f32_16x16x32_bf16 v[72:75], v[20:23], a[72:75], v[72:75]\n"
    "ds_read_b64_tr_b16 v[16:17], v6 offset:12288\n"
    "v_mfma_f32_16x16x32_bf16 v[76:79], v[20:23], a[76:79], v[76:79]\n"
    "ds_read_b64_tr_b16 v[18:19], v6 offset:14336\n"
    "s_waitcnt vmcnt(22) lgkmcnt(6)\n"
    "v_mfma_f32_16x16x32_bf16 v[64:67], v[24:27], a[24:27], v[64:67]\n"
    "ds_read_b64_tr_b16 v[20:21], v7 offset:12288\n"
    "s_add_u32 m0, s53, 12288\n"
    "v_mfma_f32_16x16x32_bf16 v[68:71], v[24:27], a[28:31], v[68:71]\n"
    "ds_read_b64_tr_b16 v[22:23], v7 offset:14336\n"
    "s_add_u32 s79, s62, 0\n"
    "v_mfma_f32_16x16x32_bf16 v[72:75], v[28:31], a[24:27], v[72:75]\n"
    "v_add_u32 v6, 0x10000, v6\n"
    "ds_read_b64_tr_b16 v[24:25], v6 offset:36864\n"
    "buffer_load_dwordx4 v15, s[40:43], s79 offen lds\n"
    "v_mfma_f32_16x16x32_bf16 v[76:79], v[28:31], a[28:31], v[76:79]\n"
    "ds_read_b64_tr_b16 v[26:27], v6 offset:38912\n"
    "s_waitcnt vmcnt(21) lgkmcnt(6)\n"
    "v_mfma_f32_16x16x32_bf16 v[64:67], v[232:235], a[32:35], v[64:67]\n"
    "v_add_u32 v7, 0x10000, v7\n"
    "ds_read_b64_tr_b16 v[28:29], v7 offset:36864\n"
    "v_mfma_f32_16x16x32_bf16 v[68:71], v[232:235], a[36:39], v[68:71]\n"
    "ds_read_b64_tr_b16 v[30:31], v7 offset:38912\n"
    "v_mfma_f32_16x16x32_bf16 v[72:75], v[236:239], a[32:35], v[72:75]\n"
    "ds_read_b64_tr_b16 v[232:233], v6 offset:45056\n"
    "v_mfma_f32_16x16x32_bf16 v[76:79], v[236:239], a[36:39], v[76:79]\n"
    "ds_read_b64_tr_b16 v[234:235], v6 offset:47104\n"
    "s_waitcnt vmcnt(19) lgkmcnt(6)\n"
    "v_mfma_f32_16x16x32_bf16 v[64:67], v[16:19], a[80:83], v[64:67]\n"
    "ds_read_b64_tr_b16 v[236:237], v7 offset:45056\n"
    "v_mfma_f32_16x16x32_bf16 v[68:71], v[16:19], a[84:87], v[68:71]\n"
    "ds_read_b64_tr_b16 v[238:239], v7 offset:47104\n"
    "v_mfma_f32_16x16x32_bf16 v[72:75], v[20:23], a[80:83], v[72:75]\n"
    "ds_read_b64_tr_b16 v[16:17], v6 offset:53248\n"
    "v_mfma_f32_16x16x32_bf16 v[76:79], v[20:23], a[84:87], v[76:79]\n"
    "ds_read_b64_tr_b16 v[18:19], v6 offset:55296\n"
    "s_waitcnt vmcnt(15) lgkmcnt(6)\n"
    "v_mfma_f32_16x16x32_bf16 v[64:67], v[24:27], a[40:43], v[64:67]\n"
    "ds_read_b64_tr_b16 v[20:21], v7 offset:53248\n"
    "buffer_load_dwordx2 v[176:177], v56, s[44:47], s70 offen offset:0 sc1\n"
    "v_mfma_f32_16x16x32_bf16 v[68:71], v[24:27], a[44:47], v[68:71]\n"
    "ds_read_b64_tr_b16 v[22:23], v7 offset:55296\n"
    "v_mfma_f32_16x16x32_bf16 v[72:75], v[28:31], a[40:43], v[72:75]\n"
    "ds_read_b64_tr_b16 v[24:25], v6 offset:61440\n"
    "v_mfma_f32_16x16x32_bf16 v[76:79], v[28:31], a[44:47], v[76:79]\n"
    "ds_read_b64_tr_b16 v[26:27], v6 offset:63488\n"
    "s_waitcnt vmcnt(12) lgkmcnt(6)\n"
    "v_mfma_f32_16x16x32_bf16 v[64:67], v[232:235], a[88:91], v[64:67]\n"
    "ds_read_b64_tr_b16 v[28:29], v7 offset:61440\n"
    "buffer_load_dwordx2 v[178:179], v57, s[44:47], s70 offen offset:0 sc1\n"
    "v_mfma_f32_16x16x32_bf16 v[68:71], v[232:235], a[92:95], v[68:71]\n"
    "ds_read_b64_tr_b16 v[30:31], v7 offset:63488\n"
    "v_mfma_f32_16x16x32_bf16 v[72:75], v[236:239], a[88:91], v[72:75]\n"
    "ds_read_b64_tr_b16 v[232:233], v8 offset:53248\n"
    "v_mfma_f32_16x16x32_bf16 v[76:79], v[236:239], a[92:95], v[76:79]\n"
    "ds_read_b64_tr_b16 v[234:235], v8 offset:55296\n"
    "s_waitcnt vmcnt(9) lgkmcnt(6)\n"
    "v_mfma_f32_16x16x32_bf16 v[64:67], v[16:19], a[48:51], v[64:67]\n"
    "ds_read_b64_tr_b16 v[236:237], v9 offset:53248\n"
    "v_mfma_f32_16x16x32_bf16 v[68:71], v[16:19], a[52:55], v[68:71]\n"
    "ds_read_b64_tr_b16 v[238:239], v9 offset:55296\n"
    "v_mfma_f32_16x16x32_bf16 v[72:75], v[20:23], a[48:51], v[72:75]\n"
    "ds_read_b64_tr_b16 v[16:17], v8 offset:61440\n"
    "v_mfma_f32_16x16x32_bf16 v[76:79], v[20:23], a[52:55], v[76:79]\n"
    "ds_read_b64_tr_b16 v[18:19], v8 offset:63488\n"
    "s_waitcnt vmcnt(5) lgkmcnt(6)\n"
    "v_mfma_f32_16x16x32_bf16 v[64:67], v[24:27], a[96:99], v[64:67]\n"
    "ds_read_b64_tr_b16 v[20:21], v9 offset:61440\n"
    "v_mfma_f32_16x16x32_bf16 v[68:71], v[24:27], a[100:103], v[68:71]\n"
    "ds_read_b64_tr_b16 v[22:23], v9 offset:63488\n"
    "v_mfma_f32_16x16x32_bf16 v[72:75], v[28:31], a[96:99], v[72:75]\n"
    "v_add_u32 v8, 0x10000, v8\n"
    "ds_read_b64_tr_b16 v[24:25], v8 offset:4096\n"
    "buffer_load_dwordx2 v[180:181], v56, s[44:47], s70 offen offset:1024 sc1\n"
    "v_mfma_f32_16x16x32_bf16 v[76:79], v[28:31], a[100:103], v[76:79]\n"
    "ds_read_b64_tr_b16 v[26:27], v8 offset:6144\n"
    "s_waitcnt lgkmcnt(6)\n"
    "v_mfma_f32_16x16x32_bf16 v[80:83], v[232:235], a[0:3], v[80:83]\n"
    "v_add_u32 v9, 0x10000, v9\n"
    "ds_read_b64_tr_b16 v[28:29], v9 offset:4096\n"
    "v_mfma_f32_16x16x32_bf16 v[84:87], v[232:235], a[4:7], v[84:87]\n"
    "ds_read_b64_tr_b16 v[30:31], v9 offset:6144\n"
    "v_mfma_f32_16x16x32_bf16 v[88:91], v[236:239], a[0:3], v[88:91]\n"
    "ds_read_b64_tr_b16 v[232:233], v8 offset:12288\n"
    "v_mfma_f32_16x16x32_bf16 v[92:95], v[236:239], a[4:7], v[92:95]\n"
    "ds_read_b64_tr_b16 v[234:235], v8 offset:14336\n"
    "s_barrier\n"
    "s_waitcnt lgkmcnt(6)\n"
    "v_mfma_f32_16x16x32_bf16 v[80:83], v[16:19], a[8:11], v[80:83]\n"
    "ds_read_b64_tr_b16 v[236:237], v9 offset:12288\n"
    "v_cvt_pk_bf16_f32 v32, v64, v65\n"
    "v_cvt_pk_bf16_f32 v33, v66, v67\n"
    "v_mfma_f32_16x16x32_bf16 v[84:87], v[16:19], a[12:15], v[84:87]\n"
    "ds_read_b64_tr_b16 v[238:239], v9 offset:14336\n"
    "v_cvt_pk_bf16_f32 v34, v72, v73\n"
    "v_cvt_pk_bf16_f32 v35, v74, v75\n"
    "v_mfma_f32_16x16x32_bf16 v[88:91], v[20:23], a[8:11], v[88:91]\n"
    "ds_read_b64_tr_b16 v[16:17], v8 offset:20480\n"
    "v_min_u32 v32, -2, v32\n"
    "v_min_u32 v33, -2, v33\n"
    "v_mfma_f32_16x16x32_bf16 v[92:95], v[20:23], a[12:15], v[92:95]\n"
    "ds_read_b64_tr_b16 v[18:19], v8 offset:22528\n"
    "s_waitcnt lgkmcnt(6)\n"
    "v_mfma_f32_16x16x32_bf16 v[80:83], v[24:27], a[16:19], v[80:83]\n"
    "ds_read_b64_tr_b16 v[20:21], v9 offset:20480\n"
    "buffer_load_dwordx2 v[182:183], v57, s[44:47], s70 offen offset:1024 sc1\n"
    "v_mfma_f32_16x16x32_bf16 v[84:87], v[24:27], a[20:23], v[84:87]\n"
    "ds_read_b64_tr_b16 v[22:23], v9 offset:22528\n"
    "v_min_u32 v34, -2, v34\n"
    "v_mfma_f32_16x16x32_bf16 v[88:91], v[28:31], a[16:19], v[88:91]\n"
    "ds_read_b64_tr_b16 v[24:25], v8 offset:28672\n"
    "v_min_u32 v35, -2, v35\n"
    "v_mfma_f32_16x16x32_bf16 v[92:95], v[28:31], a[20:23], v[92:95]\n"
    "ds_read_b64_tr_b16 v[26:27], v8 offset:30720\n"
    "s_waitcnt lgkmcnt(6)\n"
    "v_mfma_f32_16x16x32_bf16 v[80:83], v[232:235], a[64:67], v[80:83]\n"
    "ds_read_b64_tr_b16 v[28:29], v9 offset:28672\n"
    "v_cvt_pk_bf16_f32 v36, v68, v69\n"
    "v_mfma_f32_16x16x32_bf16 v[84:87], v[232:235], a[68:71], v[84:87]\n"
    "ds_read_b64_tr_b16 v[30:31], v9 offset:30720\n"
    "v_cvt_pk_bf16_f32 v37, v70, v71\n"
    "v_mfma_f32_16x16x32_bf16 v[88:91], v[236:239], a[64:67], v[88:91]\n"
    "v_add_u32 v8, 0xffff0000, v8\n"
    "ds_read_b64_tr_b16 v[232:233], v8 offset:4096\n"
    "v_cvt_pk_bf16_f32 v38, v76, v77\n"
    "v_mfma_f32_16x16x32_bf16 v[92:95], v[236:239], a[68:71], v[92:95]\n"
    "ds_read_b64_tr_b16 v[234:235], v8 offset:6144\n"
    "s_waitcnt lgkmcnt(6)\n"
    "v_mfma_f32_16x16x32_bf16 v[80:83], v[16:19], a[72:75], v[80:83]\n"
    "v_add_u32 v9, 0xffff0000, v9\n"
    "ds_read_b64_tr_b16 v[236:237], v9 offset:4096\n"
    "v_cvt_pk_bf16_f32 v39, v78, v79\n"
    "v_mfma_f32_16x16x32_bf16 v[84:87], v[16:19], a[76:79], v[84:87]\n"
    "ds_read_b64_tr_b16 v[238:239], v9 offset:6144\n"
    "v_min_u32 v36, -2, v36\n"
    "v_mfma_f32_16x16x32_bf16 v[88:91], v[20:23], a[72:75], v[88:91]\n"
    "ds_read_b64_tr_b16 v[16:17], v8 offset:12288\n"
    "buffer_load_dwordx2 v[172:173], v58, s[44:47], s70 offen offset:0 sc1\n"
    "v_mfma_f32_16x16x32_bf16 v[92:95], v[20:23], a[76:79], v[92:95]\n"
    "ds_read_b64_tr_b16 v[18:19], v8 offset:14336\n"
    "s_waitcnt lgkmcnt(6)\n"
    "v_mfma_f32_16x16x32_bf16 v[80:83], v[24:27], a[24:27], v[80:83]\n"
    "ds_read_b64_tr_b16 v[20:21], v9 offset:12288\n"
    "v_min_u32 v37, -2, v37\n"
    "v_mfma_f32_16x16x32_bf16 v[84:87], v[24:27], a[28:31], v[84:87]\n"
    "ds_read_b64_tr_b16 v[22:23], v9 offset:14336\n"
    "v_min_u32 v38, -2, v38\n"
    "v_mfma_f32_16x16x32_bf16 v[88:91], v[28:31], a[24:27], v[88:91]\n"
    "v_add_u32 v8, 0x10000, v8\n"
    "ds_read_b64_tr_b16 v[24:25], v8 offset:36864\n"
    "v_min_u32 v39, -2, v39\n"
    "v_mfma_f32_16x16x32_bf16 v[92:95], v[28:31], a[28:31], v[92:95]\n"
    "ds_read_b64_tr_b16 v[26:27], v8 offset:38912\n"
    "s_waitcnt lgkmcnt(6)\n"
    "v_mfma_f32_16x16x32_bf16 v[80:83], v[232:235], a[32:35], v[80:83]\n"
    "v_add_u32 v9, 0x10000, v9\n"
    "ds_read_b64_tr_b16 v[28:29], v9 offset:36864\n"
    "v_mfma_f32_16x16x32_bf16 v[84:87], v[232:235], a[36:39], v[84:87]\n"
    "ds_read_b64_tr_b16 v[30:31], v9 offset:38912\n"
    "v_mfma_f32_16x16x32_bf16 v[88:91], v[236:239], a[32:35], v[88:91]\n"
    "ds_read_b64_tr_b16 v[232:233], v8 offset:45056\n"
    "v_mfma_f32_16x16x32_bf16 v[92:95], v[236:239], a[36:39], v[92:95]\n"
    "ds_read_b64_tr_b16 v[234:235], v8 offset:47104\n"
    "s_waitcnt lgkmcnt(6)\n"
    "v_mfma_f32_16x16x32_bf16 v[80:83], v[16:19], a[80:83], v[80:83]\n"
    "ds_read_b64_tr_b16 v[236:237], v9 offset:45056\n"
    "buffer_load_dwordx2 v[174:175], v58, s[44:47], s70 offen offset:1024 sc1\n"
    "v_mfma_f32_16x16x32_bf16 v[84:87], v[16:19], a[84:87], v[84:87]\n"
    "ds_read_b64_tr_b16 v[238:239], v9 offset:47104\n"
    "v_mfma_f32_16x16x32_bf16 v[88:91], v[20:23], a[80:83], v[88:91]\n"
    "ds_read_b64_tr_b16 v[16:17], v8 offset:53248\n"
    "buffer_store_dwordx4 v[32:35], v0, s[44:47], s66 offen offset:0 sc1\n"
    "v_mfma_f32_16x16x32_bf16 v[92:95], v[20:23], a[84:87], v[92:95]\n"
    "ds_read_b64_tr_b16 v[18:19], v8 offset:55296\n"
    "s_waitcnt lgkmcnt(6)\n"
    "v_mfma_f32_16x16x32_bf16 v[80:83], v[24:27], a[40:43], v[80:83]\n"
    "ds_read_b64_tr_b16 v[20:21], v9 offset:53248\n"
    "v_mfma_f32_16x16x32_bf16 v[84:87], v[24:27], a[44:47], v[84:87]\n"
    "ds_read_b64_tr_b16 v[22:23], v9 offset:55296\n"
    "buffer_store_dwordx4 v[36:39], v0, s[44:47], s66 offen offset:1024 sc1\n"
    "v_mfma_f32_16x16x32_bf16 v[88:91], v[28:31], a[40:43], v[88:91]\n"
    "ds_read_b64_tr_b16 v[24:25], v8 offset:61440\n"
    "v_mfma_f32_16x16x32_bf16 v[92:95], v[28:31], a[44:47], v[92:95]\n"
    "ds_read_b64_tr_b16 v[26:27], v8 offset:63488\n"
    "s_waitcnt lgkmcnt(6)\n"
    "v_mfma_f32_16x16x32_bf16 v[80:83], v[232:235], a[88:91], v[80:83]\n"
    "ds_read_b64_tr_b16 v[28:29], v9 offset:61440\n"
    "v_mfma_f32_16x16x32_bf16 v[84:87], v[232:235], a[92:95], v[84:87]\n"
    "ds_read_b64_tr_b16 v[30:31], v9 offset:63488\n"
    "v_mfma_f32_16x16x32_bf16 v[88:91], v[236:239], a[88:91], v[88:91]\n"
    "v_add_u32 v8, 0xffff0000, v8\n"
    "ds_read_b64_tr_b16 v[232:233], v8 offset:49152\n"
    "buffer_load_dwordx2 v[184:185], v63, s[44:47], s72 offen offset:0 sc1\n"
    "v_mfma_f32_16x16x32_bf16 v[92:95], v[236:239], a[92:95], v[92:95]\n"
    "ds_read_b64_tr_b16 v[234:235], v8 offset:51200\n"
    "s_waitcnt lgkmcnt(6)\n"
    "v_mfma_f32_16x16x32_bf16 v[80:83], v[16:19], a[48:51], v[80:83]\n"
    "v_add_u32 v9, 0xffff0000, v9\n"
    "ds_read_b64_tr_b16 v[236:237], v9 offset:49152\n"
    "v_mfma_f32_16x16x32_bf16 v[84:87], v[16:19], a[52:55], v[84:87]\n"
    "ds_read_b64_tr_b16 v[238:239], v9 offset:51200\n"
    "v_mfma_f32_16x16x32_bf16 v[88:91], v[20:23], a[48:51], v[88:91]\n"
    "ds_read_b64_tr_b16 v[16:17], v8 offset:57344\n"
    "v_mfma_f32_16x16x32_bf16 v[92:95], v[20:23], a[52:55], v[92:95]\n"
    "ds_read_b64_tr_b16 v[18:19], v8 offset:59392\n"
    "s_waitcnt lgkmcnt(6)\n"
    "v_mfma_f32_16x16x32_bf16 v[80:83], v[24:27], a[96:99], v[80:83]\n"
    "ds_read_b64_tr_b16 v[20:21], v9 offset:57344\n"
    "v_mfma_f32_16x16x32_bf16 v[84:87], v[24:27], a[100:103], v[84:87]\n"
    "ds_read_b64_tr_b16 v[22:23], v9 offset:59392\n"
    "v_mfma_f32_16x16x32_bf16 v[88:91], v[28:31], a[96:99], v[88:91]\n"
    "v_add_u32 v8, 0x10000, v8\n"
    "ds_read_b64_tr_b16 v[24:25], v8 offset:0\n"
    "v_mfma_f32_16x16x32_bf16 v[92:95], v[28:31], a[100:103], v[92:95]\n"
    "ds_read_b64_tr_b16 v[26:27], v8 offset:2048\n"
    "s_waitcnt lgkmcnt(6)\n"
    "v_mfma_f32_16x16x32_bf16 v[96:99], v[232:235], a[0:3], v[96:99]\n"
    "v_add_u32 v9, 0x10000, v9\n"
    "ds_read_b64_tr_b16 v[28:29], v9 offset:0\n"
    "v_mfma_f32_16x16x32_bf16 v[100:103], v[232:235], a[4:7], v[100:103]\n"
    "ds_read_b64_tr_b16 v[30:31], v9 offset:2048\n"
    "v_mfma_f32_16x16x32_bf16 v[104:107], v[236:239], a[0:3], v[104:107]\n"
    "ds_read_b64_tr_b16 v[232:233], v8 offset:8192\n"
    "v_mfma_f32_16x16x32_bf16 v[108:111], v[236:239], a[4:7], v[108:111]\n"
    "ds_read_b64_tr_b16 v[234:235], v8 offset:10240\n"
    "s_waitcnt lgkmcnt(6)\n"
    "v_mfma_f32_16x16x32_bf16 v[96:99], v[16:19], a[8:11], v[96:99]\n"
    "ds_read_b64_tr_b16 v[236:237], v9 offset:8192\n"
    "v_cvt_pk_bf16_f32 v40, v80, v81\n"
    "v_cvt_pk_bf16_f32 v41, v82, v83\n"
    "v_mfma_f32_16x16x32_bf16 v[100:103], v[16:19], a[12:15], v[100:103]\n"
    "ds_read_b64_tr_b16 v[238:239], v9 offset:10240\n"
    "v_cvt_pk_bf16_f32 v42, v88, v89\n"
    "v_mfma_f32_16x16x32_bf16 v[104:107], v[20:23], a[8:11], v[104:107]\n"
    "ds_read_b64_tr_b16 v[16:17], v8 offset:16384\n"
    "v_cvt_pk_bf16_f32 v43, v90, v91\n"
    "v_mfma_f32_16x16x32_bf16 v[108:111], v[20:23], a[12:15], v[108:111]\n"
    "ds_read_b64_tr_b16 v[18:19], v8 offset:18432\n"
    "s_waitcnt lgkmcnt(6)\n"
    "v_mfma_f32_16x16x32_bf16 v[96:99], v[24:27], a[16:19], v[96:99]\n"
    "ds_read_b64_tr_b16 v[20:21], v9 offset:16384\n"
    "v_min_u32 v40, -2, v40\n"
    "v_mfma_f32_16x16x32_bf16 v[100:103], v[24:27], a[20:23], v[100:103]\n"
    "ds_read_b64_tr_b16 v[22:23], v9 offset:18432\n"
    "v_min_u32 v41, -2, v41\n"
    "v_mfma_f32_16x16x32_bf16 v[104:107], v[28:31], a[16:19], v[104:107]\n"
    "ds_read_b64_tr_b16 v[24:25], v8 offset:24576\n"
    "v_min_u32 v42, -2, v42\n"
    "v_mfma_f32_16x16x32_bf16 v[108:111], v[28:31], a[20:23], v[108:111]\n"
    "ds_read_b64_tr_b16 v[26:27], v8 offset:26624\n"
    "s_waitcnt lgkmcnt(6)\n"
    "v_mfma_f32_16x16x32_bf16 v[96:99], v[232:235], a[64:67], v[96:99]\n"
    "ds_read_b64_tr_b16 v[28:29], v9 offset:24576\n"
    "v_min_u32 v43, -2, v43\n"
    "v_mfma_f32_16x16x32_bf16 v[100:103], v[232:235], a[68:71], v[100:103]\n"
    "ds_read_b64_tr_b16 v[30:31], v9 offset:26624\n"
    "v_cvt_pk_bf16_f32 v44, v84, v85\n"
    "v_mfma_f32_16x16x32_bf16 v[104:107], v[236:239], a[64:67], v[104:107]\n"
    "v_add_u32 v8, 0xffff0000, v8\n"
    "ds_read_b64_tr_b16 v[232:233], v8 offset:0\n"
    "v_cvt_pk_bf16_f32 v45, v86, v87\n"
    "v_mfma_f32_16x16x32_bf16 v[108:111], v[236:239], a[68:71], v[108:111]\n"
    "ds_read_b64_tr_b16 v[234:235], v8 offset:2048\n"
    "s_waitcnt lgkmcnt(6)\n"
    "v_mfma_f32_16x16x32_bf16 v[96:99], v[16:19], a[72:75], v[96:99]\n"
    "v_add_u32 v9, 0xffff0000, v9\n"
    "ds_read_b64_tr_b16 v[236:237], v9 offset:0\n"
    "v_cvt_pk_bf16_f32 v46, v92, v93\n"
    "v_mfma_f32_16x16x32_bf16 v[100:103], v[16:19], a[76:79], v[100:103]\n"
    "ds_read_b64_tr_b16 v[238:239], v9 offset:2048\n"
    "v_cvt_pk_bf16_f32 v47, v94, v95\n"
    "v_mfma_f32_16x16x32_bf16 v[104:107], v[20:23], a[72:75], v[104:107]\n"
    "ds_read_b64_tr_b16 v[16:17], v8 offset:8192\n"
    "v_min_u32 v44, -2, v44\n"
    "v_mfma_f32_16x16x32_bf16 v[108:111], v[20:23], a[76:79], v[108:111]\n"
    "ds_read_b64_tr_b16 v[18:19], v8 offset:10240\n"
    "s_waitcnt vmcnt(9)\n"
    "s_barrier\n"
    "s_waitcnt lgkmcnt(6)\n"
    "v_mfma_f32_16x16x32_bf16 v[96:99], v[24:27], a[24:27], v[96:99]\n"
    "ds_read_b64_tr_b16 v[20:21], v9 offset:8192\n"
    "v_min_u32 v45, -2, v45\n"
    "v_mfma_f32_16x16x32_bf16 v[100:103], v[24:27], a[28:31], v[100:103]\n"
    "ds_read_b64_tr_b16 v[22:23], v9 offset:10240\n"
    "v_min_u32 v46, -2, v46\n"
    "v_mfma_f32_16x16x32_bf16 v[104:107], v[28:31], a[24:27], v[104:107]\n"
    "v_add_u32 v8, 0x10000, v8\n"
    "ds_read_b64_tr_b16 v[24:25], v8 offset:32768\n"
    "v_min_u32 v47, -2, v47\n"
    "v_mfma_f32_16x16x32_bf16 v[108:111], v[28:31], a[28:31], v[108:111]\n"
    "ds_read_b64_tr_b16 v[26:27], v8 offset:34816\n"
    "s_waitcnt lgkmcnt(6)\n"
    "v_mfma_f32_16x16x32_bf16 v[96:99], v[232:235], a[32:35], v[96:99]\n"
    "v_add_u32 v9, 0x10000, v9\n"
    "ds_read_b64_tr_b16 v[28:29], v9 offset:32768\n"
    "v_mfma_f32_16x16x32_bf16 v[100:103], v[232:235], a[36:39], v[100:103]\n"
    "ds_read_b64_tr_b16 v[30:31], v9 offset:34816\n"
    "v_mfma_f32_16x16x32_bf16 v[104:107], v[236:239], a[32:35], v[104:107]\n"
    "ds_read_b64_tr_b16 v[232:233], v8 offset:40960\n"
    "v_mfma_f32_16x16x32_bf16 v[108:111], v[236:239], a[36:39], v[108:111]\n"
    "ds_read_b64_tr_b16 v[234:235], v8 offset:43008\n"
    "s_waitcnt lgkmcnt(6)\n"
    "v_mfma_f32_16x16x32_bf16 v[96:99], v[16:19], a[80:83], v[96:99]\n"
    "ds_read_b64_tr_b16 v[236:237], v9 offset:40960\n"
    "v_mfma_f32_16x16x32_bf16 v[100:103], v[16:19], a[84:87], v[100:103]\n"
    "ds_read_b64_tr_b16 v[238:239], v9 offset:43008\n"
    "v_mfma_f32_16x16x32_bf16 v[104:107], v[20:23], a[80:83], v[104:107]\n"
    "ds_read_b64_tr_b16 v[16:17], v8 offset:49152\n"
    "buffer_store_dwordx4 v[40:43], v0, s[44:47], s67 offen offset:0 sc1\n"
    "v_mfma_f32_16x16x32_bf16 v[108:111], v[20:23], a[84:87], v[108:111]\n"
    "ds_read_b64_tr_b16 v[18:19], v8 offset:51200\n"
    "s_waitcnt lgkmcnt(6)\n"
    "v_mfma_f32_16x16x32_bf16 v[96:99], v[24:27], a[40:43], v[96:99]\n"
    "ds_read_b64_tr_b16 v[20:21], v9 offset:49152\n"
    "v_mfma_f32_16x16x32_bf16 v[100:103], v[24:27], a[44:47], v[100:103]\n"
    "ds_read_b64_tr_b16 v[22:23], v9 offset:51200\n"
    "buffer_store_dwordx4 v[44:47], v0, s[44:47], s67 offen offset:1024 sc1\n"
    "v_mfma_f32_16x16x32_bf16 v[104:107], v[28:31], a[40:43], v[104:107]\n"
    "ds_read_b64_tr_b16 v[24:25], v8 offset:57344\n"
    "v_mfma_f32_16x16x32_bf16 v[108:111], v[28:31], a[44:47], v[108:111]\n"
    "ds_read_b64_tr_b16 v[26:27], v8 offset:59392\n"
    "s_waitcnt lgkmcnt(6)\n"
    "v_mfma_f32_16x16x32_bf16 v[96:99], v[232:235], a[88:91], v[96:99]\n"
    "ds_read_b64_tr_b16 v[28:29], v9 offset:57344\n"
    "v_mfma_f32_16x16x32_bf16 v[100:103], v[232:235], a[92:95], v[100:103]\n"
    "ds_read_b64_tr_b16 v[30:31], v9 offset:59392\n"
    "v_mfma_f32_16x16x32_bf16 v[104:107], v[236:239], a[88:91], v[104:107]\n"
    "ds_read_b64_tr_b16 v[232:233], v10 offset:0\n"
    "v_mfma_f32_16x16x32_bf16 v[108:111], v[236:239], a[92:95], v[108:111]\n"
    "ds_read_b64_tr_b16 v[234:235], v10 offset:512\n"
    "s_waitcnt lgkmcnt(6)\n"
    "v_mfma_f32_16x16x32_bf16 v[96:99], v[16:19], a[48:51], v[96:99]\n"
    "ds_read_b64_tr_b16 v[236:237], v10 offset:1024\n"
    "v_mfma_f32_16x16x32_bf16 v[100:103], v[16:19], a[52:55], v[100:103]\n"
    "ds_read_b64_tr_b16 v[238:239], v10 offset:1536\n"
    "v_mfma_f32_16x16x32_bf16 v[104:107], v[20:23], a[48:51], v[104:107]\n"
    "ds_read_b64_tr_b16 v[16:17], v10 offset:2048\n"
    "v_mfma_f32_16x16x32_bf16 v[108:111], v[20:23], a[52:55], v[108:111]\n"
    "ds_read_b64_tr_b16 v[18:19], v10 offset:2560\n"
    "s_waitcnt lgkmcnt(6)\n"
    "v_mfma_f32_16x16x32_bf16 v[96:99], v[24:27], a[96:99], v[96:99]\n"
    "ds_read_b64_tr_b16 v[20:21], v10 offset:3072\n"
    "v_mfma_f32_16x16x32_bf16 v[100:103], v[24:27], a[100:103], v[100:103]\n"
    "ds_read_b64_tr_b16 v[22:23], v10 offset:3584\n"
    "v_mfma_f32_16x16x32_bf16 v[104:107], v[28:31], a[96:99], v[104:107]\n"
    "ds_read_b64_tr_b16 v[24:25], v10 offset:4096\n"
    "v_mfma_f32_16x16x32_bf16 v[108:111], v[28:31], a[100:103], v[108:111]\n"
    "ds_read_b64_tr_b16 v[26:27], v10 offset:4608\n"
    "s_waitcnt lgkmcnt(6)\n"
    "v_mfma_f32_16x16x32_bf16 v[224:227], v[232:235], a[0:3], v[224:227]\n"
    "ds_read_b64_tr_b16 v[28:29], v10 offset:5120\n"
    "v_mfma_f32_16x16x32_bf16 v[228:231], v[232:235], a[4:7], v[228:231]\n"
    "ds_read_b64_tr_b16 v[30:31], v10 offset:5632\n"
    "v_mfma_f32_16x16x32_bf16 v[224:227], v[236:239], a[8:11], v[224:227]\n"
    "ds_read_b64_tr_b16 v[232:233], v10 offset:6144\n"
    "v_mfma_f32_16x16x32_bf16 v[228:231], v[236:239], a[12:15], v[228:231]\n"
    "ds_read_b64_tr_b16 v[234:235], v10 offset:6656\n"
    "s_waitcnt lgkmcnt(6)\n"
    "v_mfma_f32_16x16x32_bf16 v[224:227], v[16:19], a[16:19], v[224:227]\n"
    "ds_read_b64_tr_b16 v[236:237], v10 offset:7168\n"
    "v_cvt_pk_bf16_f32 v32, v96, v97\n"
    "v_cvt_pk_bf16_f32 v33, v98, v99\n"
    "v_mfma_f32_16x16x32_bf16 v[228:231], v[16:19], a[20:23], v[228:231]\n"
    "ds_read_b64_tr_b16 v[238:239], v10 offset:7680\n"
    "v_cvt_pk_bf16_f32 v34, v104, v105\n"
    "v_mfma_f32_16x16x32_bf16 v[224:227], v[20:23], a[64:67], v[224:227]\n"
    "ds_read_b64_tr_b16 v[16:17], v10 offset:12288\n"
    "v_cvt_pk_bf16_f32 v35, v106, v107\n"
    "v_mfma_f32_16x16x32_bf16 v[228:231], v[20:23], a[68:71], v[228:231]\n"
    "ds_read_b64_tr_b16 v[18:19], v10 offset:12800\n"
    "s_waitcnt lgkmcnt(6)\n"
    "v_mfma_f32_16x16x32_bf16 v[224:227], v[24:27], a[72:75], v[224:227]\n"
    "ds_read_b64_tr_b16 v[20:21], v10 offset:13312\n"
    "v_min_u32 v32, -2, v32\n"
    "v_mfma_f32_16x16x32_bf16 v[228:231], v[24:27], a[76:79], v[228:231]\n"
    "ds_read_b64_tr_b16 v[22:23], v10 offset:13824\n"
    "v_min_u32 v33, -2, v33\n"
    "v_mfma_f32_16x16x32_bf16 v[224:227], v[28:31], a[24:27], v[224:227]\n"
    "ds_read_b64_tr_b16 v[24:25], v10 offset:14336\n"
    "v_min_u32 v34, -2, v34\n"
    "v_mfma_f32_16x16x32_bf16 v[228:231], v[28:31], a[28:31], v[228:231]\n"
    "ds_read_b64_tr_b16 v[26:27], v10 offset:14848\n"
    "s_waitcnt lgkmcnt(6)\n"
    "v_mfma_f32_16x16x32_bf16 v[224:227], v[232:235], a[32:35], v[224:227]\n"
    "ds_read_b64_tr_b16 v[28:29], v10 offset:15360\n"
    "v_min_u32 v35, -2, v35\n"
    "v_mfma_f32_16x16x32_bf16 v[228:231], v[232:235], a[36:39], v[228:231]\n"
    "ds_read_b64_tr_b16 v[30:31], v10 offset:15872\n"
    "v_cvt_pk_bf16_f32 v36, v100, v101\n"
    "v_mfma_f32_16x16x32_bf16 v[224:227], v[236:239], a[80:83], v[224:227]\n"
    "v_add_u32 v6, 0xffff0000, v6\n"
    "ds_read_b64_tr_b16 v[232:233], v6 offset:49152\n"
    "v_cvt_pk_bf16_f32 v37, v102, v103\n"
    "v_mfma_f32_16x16x32_bf16 v[228:231], v[236:239], a[84:87], v[228:231]\n"
    "ds_read_b64_tr_b16 v[234:235], v6 offset:51200\n"
    "s_waitcnt lgkmcnt(6)\n"
    "v_mfma_f32_16x16x32_bf16 v[224:227], v[16:19], a[40:43], v[224:227]\n"
    "v_add_u32 v7, 0xffff0000, v7\n"
    "ds_read_b64_tr_b16 v[236:237], v7 offset:49152\n"
    "v_cvt_pk_bf16_f32 v38, v108, v109\n"
    "v_mfma_f32_16x16x32_bf16 v[228:231], v[16:19], a[44:47], v[228:231]\n"
    "ds_read_b64_tr_b16 v[238:239], v7 offset:51200\n"
    "v_cvt_pk_bf16_f32 v39, v110, v111\n"
    "v_mfma_f32_16x16x32_bf16 v[224:227], v[20:23], a[88:91], v[224:227]\n"
    "ds_read_b64_tr_b16 v[16:17], v6 offset:57344\n"
    "v_min_u32 v36, -2, v36\n"
    "v_mfma_f32_16x16x32_bf16 v[228:231], v[20:23], a[92:95], v[228:231]\n"
    "ds_read_b64_tr_b16 v[18:19], v6 offset:59392\n"
    "s_waitcnt lgkmcnt(6)\n"
    "v_mfma_f32_16x16x32_bf16 v[224:227], v[24:27], a[48:51], v[224:227]\n"
    "ds_read_b64_tr_b16 v[20:21], v7 offset:57344\n"
    "v_min_u32 v37, -2, v37\n"
    "v_mfma_f32_16x16x32_bf16 v[228:231], v[24:27], a[52:55], v[228:231]\n"
    "ds_read_b64_tr_b16 v[22:23], v7 offset:59392\n"
    "v_min_u32 v38, -2, v38\n"
    "v_mfma_f32_16x16x32_bf16 v[224:227], v[28:31], a[96:99], v[224:227]\n"
    "v_add_u32 v6, 0x10000, v6\n"
    "ds_read_b64_tr_b16 v[24:25], v6 offset:0\n"
    "v_min_u32 v39, -2, v39\n"
    "v_mfma_f32_16x16x32_bf16 v[228:231], v[28:31], a[100:103], v[228:231]\n"
    "ds_read_b64_tr_b16 v[26:27], v6 offset:2048\n"
    "s_waitcnt lgkmcnt(6)\n"
    "v_mfma_f32_16x16x32_bf16 v[112:115], v[232:235], a[0:3], v[112:115]\n"
    "v_add_u32 v7, 0x10000, v7\n"
    "ds_read_b64_tr_b16 v[28:29], v7 offset:0\n"
    "v_mfma_f32_16x16x32_bf16 v[116:119], v[232:235], a[4:7], v[116:119]\n"
    "ds_read_b64_tr_b16 v[30:31], v7 offset:2048\n"
    "v_mfma_f32_16x16x32_bf16 v[120:123], v[236:239], a[0:3], v[120:123]\n"
    "ds_read_b64_tr_b16 v[232:233], v6 offset:8192\n"
    "v_mfma_f32_16x16x32_bf16 v[124:127], v[236:239], a[4:7], v[124:127]\n"
    "ds_read_b64_tr_b16 v[234:235], v6 offset:10240\n"
    "s_waitcnt lgkmcnt(6)\n"
    "v_mfma_f32_16x16x32_bf16 v[112:115], v[16:19], a[8:11], v[112:115]\n"
    "ds_read_b64_tr_b16 v[236:237], v7 offset:8192\n"
    "v_cvt_pk_bf16_f32 v40, v224, v225\n"
    "v_mfma_f32_16x16x32_bf16 v[116:119], v[16:19], a[12:15], v[116:119]\n"
    "ds_read_b64_tr_b16 v[238:239], v7 offset:10240\n"
    "v_cvt_pk_bf16_f32 v41, v226, v227\n"
    "v_mfma_f32_16x16x32_bf16 v[120:123], v[20:23], a[8:11], v[120:123]\n"
    "ds_read_b64_tr_b16 v[16:17], v6 offset:16384\n"
    "buffer_store_dwordx4 v[32:35], v0, s[44:47], s65 offen offset:0 sc1\n"
    "v_mfma_f32_16x16x32_bf16 v[124:127], v[20:23], a[12:15], v[124:127]\n"
    "ds_read_b64_tr_b16 v[18:19], v6 offset:18432\n"
    "s_waitcnt lgkmcnt(6)\n"
    "v_mfma_f32_16x16x32_bf16 v[112:115], v[24:27], a[16:19], v[112:115]\n"
    "ds_read_b64_tr_b16 v[20:21], v7 offset:16384\n"
    "v_cvt_pk_bf16_f32 v42, v228, v229\n"
    "v_mfma_f32_16x16x32_bf16 v[116:119], v[24:27], a[20:23], v[116:119]\n"
    "ds_read_b64_tr_b16 v[22:23], v7 offset:18432\n"
    "buffer_store_dwordx4 v[36:39], v0, s[44:47], s65 offen offset:1024 sc1\n"
    "v_mfma_f32_16x16x32_bf16 v[120:123], v[28:31], a[16:19], v[120:123]\n"
    "ds_read_b64_tr_b16 v[24:25], v6 offset:24576\n"
    "v_cvt_pk_bf16_f32 v43, v230, v231\n"
    "v_mfma_f32_16x16x32_bf16 v[124:127], v[28:31], a[20:23], v[124:127]\n"
    "ds_read_b64_tr_b16 v[26:27], v6 offset:26624\n"
    "s_barrier\n"
    "s_waitcnt lgkmcnt(6)\n"
    "v_mfma_f32_16x16x32_bf16 v[112:115], v[232:235], a[64:67], v[112:115]\n"
    "ds_read_b64_tr_b16 v[28:29], v7 offset:24576\n"
    "v_min_u32 v40, -2, v40\n"
    "v_mfma_f32_16x16x32_bf16 v[116:119], v[232:235], a[68:71], v[116:119]\n"
    "ds_read_b64_tr_b16 v[30:31], v7 offset:26624\n"
    "v_min_u32 v41, -2, v41\n"
    "v_mfma_f32_16x16x32_bf16 v[120:123], v[236:239], a[64:67], v[120:123]\n"
    "v_add_u32 v6, 0xffff0000, v6\n"
    "ds_read_b64_tr_b16 v[232:233], v6 offset:0\n"
    "v_min_u32 v42, -2, v42\n"
    "v_mfma_f32_16x16x32_bf16 v[124:127], v[236:239], a[68:71], v[124:127]\n"
    "ds_read_b64_tr_b16 v[234:235], v6 offset:2048\n"
    "s_waitcnt lgkmcnt(6)\n"
    "v_mfma_f32_16x16x32_bf16 v[112:115], v[16:19], a[72:75], v[112:115]\n"
    "v_add_u32 v7, 0xffff0000, v7\n"
    "ds_read_b64_tr_b16 v[236:237], v7 offset:0\n"
    "v_min_u32 v43, -2, v43\n"
    "v_mfma_f32_16x16x32_bf16 v[116:119], v[16:19], a[76:79], v[116:119]\n"
    "ds_read_b64_tr_b16 v[238:239], v7 offset:2048\n"
    "v_mfma_f32_16x16x32_bf16 v[120:123], v[20:23], a[72:75], v[120:123]\n"
    "ds_read_b64_tr_b16 v[16:17], v6 offset:8192\n"
    "v_mfma_f32_16x16x32_bf16 v[124:127], v[20:23], a[76:79], v[124:127]\n"
    "ds_read_b64_tr_b16 v[18:19], v6 offset:10240\n"
    "s_waitcnt lgkmcnt(6)\n"
    "v_mfma_f32_16x16x32_bf16 v[112:115], v[24:27], a[24:27], v[112:115]\n"
    "ds_read_b64_tr_b16 v[20:21], v7 offset:8192\n"
    "v_mfma_f32_16x16x32_bf16 v[116:119], v[24:27], a[28:31], v[116:119]\n"
    "ds_read_b64_tr_b16 v[22:23], v7 offset:10240\n"
    "v_mfma_f32_16x16x32_bf16 v[120:123], v[28:31], a[24:27], v[120:123]\n"
    "v_add_u32 v6, 0x10000, v6\n"
    "ds_read_b64_tr_b16 v[24:25], v6 offset:32768\n"
    "v_mfma_f32_16x16x32_bf16 v[124:127], v[28:31], a[28:31], v[124:127]\n"
    "ds_read_b64_tr_b16 v[26:27], v6 offset:34816\n"
    "s_waitcnt lgkmcnt(6)\n"
    "v_mfma_f32_16x16x32_bf16 v[112:115], v[232:235], a[32:35], v[112:115]\n"
    "v_add_u32 v7, 0x10000, v7\n"
    "ds_read_b64_tr_b16 v[28:29], v7 offset:32768\n"
    "v_mfma_f32_16x16x32_bf16 v[116:119], v[232:235], a[36:39], v[116:119]\n"
    "ds_read_b64_tr_b16 v[30:31], v7 offset:34816\n"
    "buffer_store_dwordx4 v[40:43], v0, s[44:47], s68 offen offset:0 sc1\n"
    "v_mfma_f32_16x16x32_bf16 v[120:123], v[236:239], a[32:35], v[120:123]\n"
    "ds_read_b64_tr_b16 v[232:233], v6 offset:40960\n"
    "v_mfma_f32_16x16x32_bf16 v[124:127], v[236:239], a[36:39], v[124:127]\n"
    "ds_read_b64_tr_b16 v[234:235], v6 offset:43008\n"
    "s_waitcnt lgkmcnt(6)\n"
    "v_mfma_f32_16x16x32_bf16 v[112:115], v[16:19], a[80:83], v[112:115]\n"
    "ds_read_b64_tr_b16 v[236:237], v7 offset:40960\n"
    "s_add_u32 s34, s64, 8192\n"
    "v_mfma_f32_16x16x32_bf16 v[116:119], v[16:19], a[84:87], v[116:119]\n"
    "ds_read_b64_tr_b16 v[238:239], v7 offset:43008\n"
    "buffer_load_dwordx4 v[136:139], v0, s[44:47], s34 offen offset:0 sc1\n"
    "v_mfma_f32_16x16x32_bf16 v[120:123], v[20:23], a[80:83], v[120:123]\n"
    "ds_read_b64_tr_b16 v[16:17], v6 offset:49152\n"
    "buffer_load_dwordx4 v[140:143], v0, s[44:47], s34 offen offset:1024 sc1\n"
    "v_mfma_f32_16x16x32_bf16 v[124:127], v[20:23], a[84:87], v[124:127]\n"
    "ds_read_b64_tr_b16 v[18:19], v6 offset:51200\n"
    "s_waitcnt lgkmcnt(6)\n"
    "v_mfma_f32_16x16x32_bf16 v[112:115], v[24:27], a[40:43], v[112:115]\n"
    "ds_read_b64_tr_b16 v[20:21], v7 offset:49152\n"
    "s_add_u32 s34, s64, 16384\n"
    "v_mfma_f32_16x16x32_bf16 v[116:119], v[24:27], a[44:47], v[116:119]\n"
    "ds_read_b64_tr_b16 v[22:23], v7 offset:51200\n"
    "buffer_load_dwordx4 v[144:147], v0, s[44:47], s34 offen offset:0 sc1\n"
    "v_mfma_f32_16x16x32_bf16 v[120:123], v[28:31], a[40:43], v[120:123]\n"
    "ds_read_b64_tr_b16 v[24:25], v6 offset:57344\n"
    "buffer_load_dwordx4 v[148:151], v0, s[44:47], s34 offen offset:1024 sc1\n"
    "v_mfma_f32_16x16x32_bf16 v[124:127], v[28:31], a[44:47], v[124:127]\n"
    "ds_read_b64_tr_b16 v[26:27], v6 offset:59392\n"
    "s_waitcnt lgkmcnt(6)\n"
    "v_mfma_f32_16x16x32_bf16 v[112:115], v[232:235], a[88:91], v[112:115]\n"
    "ds_read_b64_tr_b16 v[28:29], v7 offset:57344\n"
    "s_add_u32 s34, s64, 0\n"
    "v_mfma_f32_16x16x32_bf16 v[116:119], v[232:235], a[92:95], v[116:119]\n"
    "ds_read_b64_tr_b16 v[30:31], v7 offset:59392\n"
    "v_mfma_f32_16x16x32_bf16 v[120:123], v[236:239], a[88:91], v[120:123]\n"
    "buffer_load_dwordx4 v[128:131], v0, s[44:47], s34 offen offset:0 sc1\n"
    "v_mfma_f32_16x16x32_bf16 v[124:127], v[236:239], a[92:95], v[124:127]\n"
    "s_waitcnt lgkmcnt(4)\n"
    "v_mfma_f32_16x16x32_bf16 v[112:115], v[16:19], a[48:51], v[112:115]\n"
    "buffer_load_dwordx4 v[132:135], v0, s[44:47], s34 offen offset:1024 sc1\n"
    "v_mfma_f32_16x16x32_bf16 v[116:119], v[16:19], a[52:55], v[116:119]\n"
    "s_and_b32 s33, s75, 0x80000000\n"
    "v_or_b32 v249, s33, v0\n"
    "v_mfma_f32_16x16x32_bf16 v[120:123], v[20:23], a[48:51], v[120:123]\n"
    "buffer_load_dwordx4 v[160:163], v249, s[44:47], s69 offen offset:0 sc1\n"
    "v_mfma_f32_16x16x32_bf16 v[124:127], v[20:23], a[52:55], v[124:127]\n"
    "s_waitcnt lgkmcnt(0)\n"
    "v_mfma_f32_16x16x32_bf16 v[112:115], v[24:27], a[96:99], v[112:115]\n"
    "buffer_load_dwordx4 v[164:167], v249, s[44:47], s69 offen offset:1024 sc1\n"
    "v_mfma_f32_16x16x32_bf16 v[116:119], v[24:27], a[100:103], v[116:119]\n"
    "buffer_load_dwordx4 v[168:171], v249, s[44:47], s69 offen offset:2048 sc1\n"
    "v_mfma_f32_16x16x32_bf16 v[120:123], v[28:31], a[96:99], v[120:123]\n"
    "v_mfma_f32_16x16x32_bf16 v[124:127], v[28:31], a[100:103], v[124:127]\n"
    "s_branch LE_k\n"
    "LCO_k:\n"
    "s_nop 7\n"
    "s_mov_b32 s77, 200000\n"
    "LCL_k:\n"
    "s_add_u32 s76, s33, 32768\n"
    "buffer_load_dwordx4 v[88:91], v0, s[44:47], s76 offen offset:0 sc1\n"
    "buffer_load_dwordx4 v[92:95], v0, s[44:47], s76 offen offset:2048 sc1\n"
    "s_add_u32 s78, s78, 1048576\n"
    "s_waitcnt vmcnt(0)\n"
    "v_max3_u32 v247, v88, v89, v90\n"
    "v_max3_u32 v247, v91, v92, v247\n"
    "v_max3_u32 v247, v93, v94, v247\n"
    "v_max_u32 v247, v95, v247\n"
    "v_cmp_eq_u32 vcc, -1, v247\n"
    "s_cbranch_vccnz LCN_k\n"
    "s_add_u32 s76, s33, 0\n"
    "buffer_load_dwordx4 v[16:19], v0, s[44:47], s76 offen offset:0 sc1\n"
    "buffer_load_dwordx4 v[20:23], v0, s[44:47], s76 offen offset:2048 sc1\n"
    "s_add_u32 s76, s33, 4096\n"
    "buffer_load_dwordx4 v[24:27], v0, s[44:47], s76 offen offset:0 sc1\n"
    "buffer_load_dwordx4 v[28:31], v0, s[44:47], s76 offen offset:2048 sc1\n"
    "s_add_u32 s76, s33, 8192\n"
    "buffer_load_dwordx4 v[32:35], v0, s[44:47], s76 offen offset:0 sc1\n"
    "buffer_load_dwordx4 v[36:39], v0, s[44:47], s76 offen offset:2048 sc1\n"
    "s_add_u32 s76, s33, 12288\n"
    "buffer_load_dwordx4 v[40:43], v0, s[44:47], s76 offen offset:0 sc1\n"
    "buffer_load_dwordx4 v[44:47], v0, s[44:47], s76 offen offset:2048 sc1\n"
    "s_add_u32 s76, s33, 16384\n"
    "buffer_load_dwordx4 v[48:51], v0, s[44:47], s76 offen offset:0 sc1\n"
    "buffer_load_dwordx4 v[52:55], v0, s[44:47], s76 offen offset:2048 sc1\n"
    "s_add_u32 s76, s33, 20480\n"
    "buffer_load_dwordx4 v[64:67], v0, s[44:47], s76 offen offset:0 sc1\n"
    "buffer_load_dwordx4 v[68:71], v0, s[44:47], s76 offen offset:2048 sc1\n"
    "s_add_u32 s76, s33, 24576\n"
    "buffer_load_dwordx4 v[72:75], v0, s[44:47], s76 offen offset:0 sc1\n"
    "buffer_load_dwordx4 v[76:79], v0, s[44:47], s76 offen offset:2048 sc1\n"
    "s_add_u32 s76, s33, 28672\n"
    "buffer_load_dwordx4 v[80:83], v0, s[44:47], s76 offen offset:0 sc1\n"
    "buffer_load_dwordx4 v[84:87], v0, s[44:47], s76 offen offset:2048 sc1\n"
    "s_waitcnt vmcnt(0)\n"
    "v_max3_u32 v247, v16, v17, v247\n"
    "v_max3_u32 v247, v18, v23, v247\n"
    "v_max3_u32 v247, v19, v20, v247\n"
    "v_max3_u32 v247, v21, v22, v247\n"
    "v_max3_u32 v247, v24, v25, v247\n"
    "v_max3_u32 v247, v26, v31, v247\n"
    "v_max3_u32 v247, v27, v28, v247\n"
    "v_max3_u32 v247, v29, v30, v247\n"
    "v_max3_u32 v247, v32, v33, v247\n"
    "v_max3_u32 v247, v34, v39, v247\n"
    "v_max3_u32 v247, v35, v36, v247\n"
    "v_max3_u32 v247, v37, v38, v247\n"
    "v_max3_u32 v247, v40, v41, v247\n"
    "v_max3_u32 v247, v42, v47, v247\n"
    "v_max3_u32 v247, v43, v44, v247\n"
    "v_max3_u32 v247, v45, v46, v247\n"
    "v_max3_u32 v247, v48, v49, v247\n"
    "v_max3_u32 v247, v50, v55, v247\n"
    "v_max3_u32 v247, v51, v52, v247\n"
    "v_max3_u32 v247, v53, v54, v247\n"
    "v_max3_u32 v247, v64, v65, v247\n"
    "v_max3_u32 v247, v66, v71, v247\n"
    "v_max3_u32 v247, v67, v68, v247\n"
    "v_max3_u32 v247, v69, v70, v247\n"
    "v_max3_u32 v247, v72, v73, v247\n"
    "v_max3_u32 v247, v74, v79, v247\n"
    "v_max3_u32 v247, v75, v76, v247\n"
    "v_max3_u32 v247, v77, v78, v247\n"
    "v_max3_u32 v247, v80, v81, v247\n"
    "v_max3_u32 v247, v82, v87, v247\n"
    "v_max3_u32 v247, v83, v84, v247\n"
    "v_max3_u32 v247, v85, v86, v247\n"
    "v_cmp_eq_u32 vcc, -1, v247\n"
    "s_cbranch_vccz LCR_k\n"
    "LCN_k:\n"
    "s_sub_u32 s77, s77, 1\n"
    "s_cbranch_scc0 LCL_k\n"
    "LCR_k:\n"
    "s_setpc_b64 s[90:91]\n"
    "LC0_k:\n"
    "s_mov_b32 s77, 200000\n"
    "LCP_k:\n"
    "buffer_load_dwordx4 v[88:91], v0, s[44:47], s79 offen offset:0 sc1\n"
    "buffer_load_dwordx4 v[92:95], v0, s[44:47], s79 offen offset:2048 sc1\n"
    "s_add_u32 s78, s78, 268435456\n"
    "s_waitcnt vmcnt(0)\n"
    "v_max3_u32 v247, v88, v89, v90\n"
    "v_max3_u32 v247, v91, v92, v247\n"
    "v_max3_u32 v247, v93, v94, v247\n"
    "v_max_u32 v247, v95, v247\n"
    "v_cmp_eq_u32 vcc, -1, v247\n"
    "s_cbranch_vccz LD0_k\n"
    "s_sub_u32 s77, s77, 1\n"
    "s_cbranch_scc0 LCP_k\n"
    "s_branch LD0_k\n"
    "LC2_k:\n"
    "s_call_b64 s[90:91], LCO_k\n"
    "s_branch LD2_k\n"
    "LC4_k:\n"
    "s_call_b64 s[90:91], LCO_k\n"
    "s_branch LD4_k\n"
    "LC6_k:\n"
    "s_call_b64 s[90:91], LCO_k\n"
    "s_branch LD6_k\n"
    "LC8_k:\n"
    "s_call_b64 s[90:91], LCO_k\n"
    "s_branch LD8_k\n"
    "LC10_k:\n"
    "s_call_b64 s[90:91], LCO_k\n"
    "s_branch LD10_k\n"
    "LC12_k:\n"
    "s_call_b64 s[90:91], LCO_k\n"
    "s_branch LD12_k\n"
    "LC14_k:\n"
    "s_call_b64 s[90:91], LCO_k\n"
    "s_branch LD14_k\n"
    "LC16_k:\n"
    "s_call_b64 s[90:91], LCO_k\n"
    "s_branch LD16_k\n"
    "LH_k:\n"
    "s_mov_b32 s77, 200000\n"
    "LJ_k:\n"
    "s_add_u32 s33, s56, 32768\n"
    "buffer_load_dwordx4 v[64:67], v0, s[44:47], s33 offen offset:0 sc1\n"
    "buffer_load_dwordx4 v[68:71], v0, s[44:47], s33 offen offset:1024 sc1\n"
    "s_add_u32 s34, s56, 69632\n"
    "buffer_load_dwordx4 v[72:75], v0, s[44:47], s34 offen offset:0 sc1\n"
    "buffer_load_dwordx4 v[76:79], v0, s[44:47], s34 offen offset:1024 sc1\n"
    "buffer_load_dwordx4 v[80:83], v0, s[44:47], s33 offen offset:2048 sc1\n"
    "buffer_load_dwordx4 v[84:87], v0, s[44:47], s33 offen offset:3072 sc1\n"
    "buffer_load_dwordx4 v[88:91], v0, s[44:47], s34 offen offset:2048 sc1\n"
    "buffer_load_dwordx4 v[92:95], v0, s[44:47], s34 offen offset:3072 sc1\n"
    "s_add_u32 s78, s78, 4096\n"
    "s_waitcnt vmcnt(4)\n"
    "v_max3_u32 v247, v64, v65, v66\n"
    "v_max3_u32 v247, v67, v68, v247\n"
    "v_max3_u32 v247, v69, v70, v247\n"
    "v_max_u32 v247, v71, v247\n"
    "v_max3_u32 v247, v72, v73, v247\n"
    "v_max3_u32 v247, v74, v79, v247\n"
    "v_max3_u32 v247, v75, v76, v247\n"
    "v_max3_u32 v247, v77, v78, v247\n"
    "s_waitcnt vmcnt(0)\n"
    "v_max3_u32 v247, v80, v81, v247\n"
    "v_max3_u32 v247, v82, v87, v247\n"
    "v_max3_u32 v247, v83, v84, v247\n"
    "v_max3_u32 v247, v85, v86, v247\n"
    "v_max3_u32 v247, v88, v89, v247\n"
    "v_max3_u32 v247, v90, v95, v247\n"
    "v_max3_u32 v247, v91, v92, v247\n"
    "v_max3_u32 v247, v93, v94, v247\n"
    "v_cmp_eq_u32 vcc, -1, v247\n"
    "s_cbranch_vccz LI_k\n"
    "s_sub_u32 s77, s77, 1\n"
    "s_cbranch_scc0 LJ_k\n"
    "s_branch LI_k\n"
    "LV0_k:\n"
    "s_mov_b32 s77, 200000\n"
    "LX0_k:\n"
    "s_add_u32 s35, s56, 0\n"
    "buffer_load_dwordx4 a[0:3], v0, s[44:47], s35 offen offset:0 sc1\n"
    "buffer_load_dwordx4 a[4:7], v0, s[44:47], s35 offen offset:1024 sc1\n"
    "s_add_u32 s35, s56, 36864\n"
    "buffer_load_dwordx4 a[8:11], v0, s[44:47], s35 offen offset:0 sc1\n"
    "buffer_load_dwordx4 a[12:15], v0, s[44:47], s35 offen offset:1024 sc1\n"
    "s_add_u32 s78, s78, 65536\n"
    "s_waitcnt vmcnt(0)\n"
    "v_accvgpr_read_b32 v32, a0\n"
    "v_accvgpr_read_b32 v33, a1\n"
    "v_accvgpr_read_b32 v34, a2\n"
    "v_accvgpr_read_b32 v35, a3\n"
    "v_accvgpr_read_b32 v36, a4\n"
    "v_accvgpr_read_b32 v37, a5\n"
    "v_accvgpr_read_b32 v38, a6\n"
    "v_accvgpr_read_b32 v39, a7\n"
    "v_accvgpr_read_b32 v40, a8\n"
    "v_accvgpr_read_b32 v41, a9\n"
    "v_accvgpr_read_b32 v42, a10\n"
    "v_accvgpr_read_b32 v43, a11\n"
    "v_accvgpr_read_b32 v44, a12\n"
    "v_accvgpr_read_b32 v45, a13\n"
    "v_accvgpr_read_b32 v46, a14\n"
    "v_accvgpr_read_b32 v47, a15\n"
    "v_max3_u32 v32, v32, v33, v34\n"
    "v_max3_u32 v35, v35, v36, v37\n"
    "v_max3_u32 v38, v38, v39, v40\n"
    "v_max3_u32 v41, v41, v42, v43\n"
    "v_max3_u32 v44, v44, v45, v46\n"
    "v_max3_u32 v32, v32, v35, v38\n"
    "v_max3_u32 v41, v41, v44, v47\n"
    "v_max_u32 v32, v32, v41\n"
    "v_cmp_eq_u32 vcc, -1, v32\n"
    "s_cbranch_vccz LW0_k\n"
    "s_sub_u32 s77, s77, 1\n"
    "s_cbranch_scc0 LX0_k\n"
    "s_branch LW0_k\n"
    "LV1_k:\n"
    "s_mov_b32 s77, 200000\n"
    "LX1_k:\n"
    "s_add_u32 s35, s56, 2048\n"
    "buffer_load_dwordx4 a[16:19], v0, s[44:47], s35 offen offset:0 sc1\n"
    "buffer_load_dwordx4 a[20:23], v0, s[44:47], s35 offen offset:1024 sc1\n"
    "s_add_u32 s35, s56, 38912\n"
    "buffer_load_dwordx4 a[24:27], v0, s[44:47], s35 offen offset:0 sc1\n"
    "buffer_load_dwordx4 a[28:31], v0, s[44:47], s35 offen offset:1024 sc1\n"
    "s_add_u32 s78, s78, 65536\n"
    "s_waitcnt vmcnt(0)\n"
    "v_accvgpr_read_b32 v32, a16\n"
    "v_accvgpr_read_b32 v33, a17\n"
    "v_accvgpr_read_b32 v34, a18\n"
    "v_accvgpr_read_b32 v35, a19\n"
    "v_accvgpr_read_b32 v36, a20\n"
    "v_accvgpr_read_b32 v37, a21\n"
    "v_accvgpr_read_b32 v38, a22\n"
    "v_accvgpr_read_b32 v39, a23\n"
    "v_accvgpr_read_b32 v40, a24\n"
    "v_accvgpr_read_b32 v41, a25\n"
    "v_accvgpr_read_b32 v42, a26\n"
    "v_accvgpr_read_b32 v43, a27\n"
    "v_accvgpr_read_b32 v44, a28\n"
    "v_accvgpr_read_b32 v45, a29\n"
    "v_accvgpr_read_b32 v46, a30\n"
    "v_accvgpr_read_b32 v47, a31\n"
    "v_max3_u32 v32, v32, v33, v34\n"
    "v_max3_u32 v35, v35, v36, v37\n"
    "v_max3_u32 v38, v38, v39, v40\n"
    "v_max3_u32 v41, v41, v42, v43\n"
    "v_max3_u32 v44, v44, v45, v46\n"
    "v_max3_u32 v32, v32, v35, v38\n"
    "v_max3_u32 v41, v41, v44, v47\n"
    "v_max_u32 v32, v32, v41\n"
    "v_cmp_eq_u32 vcc, -1, v32\n"
    "s_cbranch_vccz LW1_k\n"
    "s_sub_u32 s77, s77, 1\n"
    "s_cbranch_scc0 LX1_k\n"
    "s_branch LW1_k\n"
    "LS6_k:\n"
    "s_mov_b32 s77, 200000\n"
    "buffer_load_dword v254, v243, s[44:47], 0 offen offset:0 sc1\n"
    "s_sleep 3\n"
    "buffer_load_dword v255, v243, s[44:47], 0 offen offset:0 sc1\n"
    "LL6_k:\n"
    "s_sleep 3\n"
    "buffer_load_dword v252, v243, s[44:47], 0 offen offset:0 sc1\n"
    "s_add_u32 s78, s78, 16777216\n"
    "s_waitcnt vmcnt(2)\n"
    "v_cmp_ne_u32 vcc, s92, v254\n"
    "s_cbranch_vccz LB6_k\n"
    "s_sleep 3\n"
    "buffer_load_dword v254, v243, s[44:47], 0 offen offset:0 sc1\n"
    "s_add_u32 s78, s78, 16777216\n"
    "s_waitcnt vmcnt(2)\n"
    "v_cmp_ne_u32 vcc, s92, v255\n"
    "s_cbranch_vccz LB6_k\n"
    "s_sleep 3\n"
    "buffer_load_dword v255, v243, s[44:47], 0 offen offset:0 sc1\n"
    "s_add_u32 s78, s78, 16777216\n"
    "s_waitcnt vmcnt(2)\n"
    "v_cmp_ne_u32 vcc, s92, v252\n"
    "s_cbranch_vccz LB6_k\n"
    "s_sub_u32 s77, s77, 1\n"
    "s_cbranch_scc0 LL6_k\n"
    "s_branch LB6_k\n"
    "LS0_k:\n"
    "s_mov_b32 s77, 200000\n"
    "LL0_k:\n"
    "buffer_load_dword v60, v11, s[44:47], 0 offen offset:0 sc1\n"
    "s_add_u32 s78, s78, 1\n"
    "s_waitcnt vmcnt(0)\n"
    "v_cmp_ne_u32 vcc, s93, v60\n"
    "s_cbranch_vccz LB0_k\n"
    "s_sub_u32 s77, s77, 1\n"
    "s_cbranch_scc0 LL0_k\n"
    "s_branch LB0_k\n"
    "LS1_k:\n"
    "s_mov_b32 s77, 200000\n"
    "LL1_k:\n"
    "buffer_load_dword v60, v11, s[44:47], 0 offen offset:256 sc1\n"
    "s_add_u32 s78, s78, 16\n"
    "s_waitcnt vmcnt(0)\n"
    "v_cmp_ne_u32 vcc, s93, v60\n"
    "s_cbranch_vccz LB1_k\n"
    "s_sub_u32 s77, s77, 1\n"
    "s_cbranch_scc0 LL1_k\n"
    "s_branch LB1_k\n"
    "LS8_k:\n"
    "s_mov_b32 s77, 200000\n"
    "LL8_k:\n"
    "buffer_load_dword v60, v63, s[44:47], 0 offen offset:256 sc1\n"
    "s_add_u32 s78, s78, 1\n"
    "s_waitcnt vmcnt(0)\n"
    "v_cmp_ne_u32 vcc, s92, v60\n"
    "s_cbranch_vccz LB8_k\n"
    "s_sub_u32 s77, s77, 1\n"
    "s_cbranch_scc0 LL8_k\n"
    "s_branch LB8_k\n"
    "LS2_k:\n"
    "s_mov_b32 s77, 200000\n"
    "LL2_k:\n"
    "buffer_load_dword v60, v11, s[44:47], 0 offen offset:512 sc1\n"
    "s_add_u32 s78, s78, 256\n"
    "s_waitcnt vmcnt(0)\n"
    "v_cmp_ne_u32 vcc, s93, v60\n"
    "s_cbranch_vccz LB2_k\n"
    "s_sub_u32 s77, s77, 1\n"
    "s_cbranch_scc0 LL2_k\n"
    "s_branch LB2_k\n"
    "LS3_k:\n"
    "s_mov_b32 s77, 200000\n"
    "LL3_k:\n"
    "buffer_load_dword v60, v11, s[44:47], 0 offen offset:768 sc1\n"
    "s_add_u32 s78, s78, 4096\n"
    "s_waitcnt vmcnt(0)\n"
    "v_cmp_ne_u32 vcc, s93, v60\n"
    "s_cbranch_vccz LB3_k\n"
    "s_sub_u32 s77, s77, 1\n"
    "s_cbranch_scc0 LL3_k\n"
    "s_branch LB3_k\n"
    "LS4_k:\n"
    "s_mov_b32 s77, 200000\n"
    "LL4_k:\n"
    "buffer_load_dword v249, v11, s[44:47], 0 offen offset:1024 sc1\n"
    "s_add_u32 s78, s78, 65536\n"
    "s_waitcnt vmcnt(0)\n"
    "v_cmp_ne_u32 vcc, s93, v249\n"
    "s_cbranch_vccz LB4_k\n"
    "s_sub_u32 s77, s77, 1\n"
    "s_cbranch_scc0 LL4_k\n"
    "s_branch LB4_k\n"
    "LE_k:\n"
    "s_waitcnt vmcnt(0)\n"
    "s_nop 7\n"
    : "+{v[0:15]}"(vi), "={v[112:127]}"(own), "={v[128:143]}"(ra), "={v[144:151]}"(rb), "={v[160:175]}"(rs), "={v[176:183]}"(hp), "={v[184:185]}"(mn), "={v[224:231]}"(sg), "+{s78}"(sen),
      "+{v[56:59]}"(vr), "+{v[60:61]}"(fv), "+{v[240:247]}"(vp), "+{v[252:253]}"(vt)
    : "{s[44:47]}"(srdS), "{s[36:39]}"(srdG), "{s[40:43]}"(srD), "{s[92:93]}"(tg), "{s[52:55]}"(sa0), "{s[56:59]}"(__builtin_shufflevector(sa, sa, 4, 5, 6, 7)), "{s[60:67]}"(__builtin_shufflevector(sa, sa, 8, 9, 10, 11, 12, 13, 14, 15)), "{s[68:75]}"(sb), "{s88}"(m0a), "{s32}"(fp2)
    : "v16", "v17", "v18", "v19", "v20", "v21", "v22", "v23", "v24", "v25", "v26", "v27", "v28", "v29", "v30", "v31", "v32", "v33", "v34", "v35", "v36", "v37", "v38", "v39", "v40", "v41", "v42", "v43", "v44", "v45", "v46", "v47", "v48", "v49", "v50", "v51", "v52", "v53", "v54", "v55", "v62", "v63", "v64", "v65", "v66", "v67", "v68", "v69", "v70", "v71", "v72", "v73", "v74", "v75", "v76", "v77", "v78", "v79", "v80", "v81", "v82", "v83", "v84", "v85", "v86", "v87", "v88", "v89", "v90", "v91", "v92", "v93", "v94", "v95", "v96", "v97", "v98", "v99", "v100", "v101", "v102", "v103", "v104", "v105", "v106", "v107", "v108", "v109", "v110", "v111", "v152", "v153", "v154", "v155", "v156", "v157", "v158", "v159", "v186", "v187", "v188", "v189", "v190", "v191", "v192", "v193", "v194", "v195", "v196", "v197", "v198", "v199", "v200", "v201", "v202", "v203", "v204", "v205", "v206", "v207", "v208", "v209", "v210", "v211", "v212", "v213", "v214", "v215", "v216", "v217", "v218", "v219", "v220", "v221", "v222", "v223", "v232", "v233", "v234", "v235", "v236", "v237", "v238", "v239", "v248", "v249", "v250", "v251", "v254", "v255", "a0", "a1", "a2", "a3", "a4", "a5", "a6", "a7", "a8", "a9", "a10", "a11", "a12", "a13", "a14", "a15", "a16", "a17", "a18", "a19", "a20", "a21", "a22", "a23", "a24", "a25", "a26", "a27", "a28", "a29", "a30", "a31", "a32", "a33", "a34", "a35", "a36", "a37", "a38", "a39", "a40", "a41", "a42", "a43", "a44", "a45", "a46", "a47", "a48", "a49", "a50", "a51", "a52", "a53", "a54", "a55", "a56", "a57", "a58", "a59", "a60", "a61", "a62", "a63", "a64", "a65", "a66", "a67", "a68", "a69", "a70", "a71", "a72", "a73", "a74", "a75", "a76", "a77", "a78", "a79", "a80", "a81", "a82", "a83", "a84", "a85", "a86", "a87", "a88", "a89", "a90", "a91", "a92", "a93", "a94", "a95", "a96", "a97", "a98", "a99", "a100", "a101", "a102", "a103", "a104", "a105", "a106", "a107", "a108", "a109", "a110", "a111", "a112", "a113", "a114", "a115", "a116", "a117", "a118", "a119", "a120", "a121", "a122", "a123", "a124", "a125", "a126", "a127", "a128", "a129", "a130", "a131", "a132", "a133", "a134", "a135", "a136", "a137", "a138", "a139", "a140", "a141", "a142", "a143", "a144", "a145", "a146", "a147", "a148", "a149", "a150", "a151", "a152", "a153", "a154", "a155", "a156", "a157", "a158", "a159", "a160", "a161", "a162", "a163", "a164", "a165", "a166", "a167", "a168", "a169", "a170", "a171", "a172", "a173", "a174", "a175", "a176", "a177", "a178", "a179", "a180", "a181", "a182", "a183", "a184", "a185", "a186", "a187", "a188", "a189", "a190", "a191", "a192", "a193", "a194", "a195", "a196", "a197", "a198", "a199", "a200", "a201", "a202", "a203", "a204", "a205", "a206", "a207", "a208", "a209", "a210", "a211", "a212", "a213", "a214", "a215", "a216", "a217", "a218", "a219", "a220", "a221", "a222", "a223", "a224", "a225", "a226", "a227", "a228", "a229", "a230", "a231", "a232", "a233", "a234", "a235", "a236", "a237", "a238", "a239", "a240", "a241", "a242", "a243", "a244", "a245", "a246", "a247", "a248", "a249", "a250", "a251", "a252", "a253", "a254", "a255", "s33", "s34", "s35", "s76", "s77", "s79", "vcc", "scc", "memory");
  {
  const u32 vL = vi[0];
  const u32x4 srS = srdS;
  u32x4 rp[9];
  for (int i = 0; i < 4; ++i) { rp[0][i] = ra[i]; rp[1][i] = ra[4 + i]; rp[2][i] = ra[8 + i]; rp[3][i] = ra[12 + i]; rp[4][i] = rb[i]; rp[5][i] = rb[4 + i]; rp[6][i] = rs[i]; rp[7][i] = rs[4 + i]; rp[8][i] = rs[8 + i]; }
  const bool sown = (int)sb[7] >= 0;
  u32 nit = 0;
  const u32 vS = vL | (sb[7] & 0x80000000u);
  for (u32 it = 0; it < 200000; ++it) {
    u32 miss = 0;
    for (int e = 0; e < 3; ++e) {
      u32 mx = 0;
      for (int j = 0; j < 3; ++j) { const u32x4 g = rp[j < 2 ? 2 * e + j : 6 + e]; mx = max(max(mx, max(g[0], g[1])), max(g[2], g[3])); }
      miss |= (__builtin_amdgcn_ballot_w64(mx == ~0u) != 0) << e;
    }
    if (!miss) break;
    ++nit;
    asm volatile("s_sleep 1\n\t"
                 "s_bitcmp1_b32 %15, 0\n\ts_cbranch_scc0 1f\n\tbuffer_load_dwordx4 %0, %9, %10, %11 offen sc1\n\tbuffer_load_dwordx4 %1, %9, %10, %11 offen offset:1024 sc1\n\tbuffer_load_dwordx4 %6, %16, %10, %14 offen offset:0 sc1\n1:\n\t"
                 "s_bitcmp1_b32 %15, 1\n\ts_cbranch_scc0 2f\n\tbuffer_load_dwordx4 %2, %9, %10, %12 offen sc1\n\tbuffer_load_dwordx4 %3, %9, %10, %12 offen offset:1024 sc1\n\tbuffer_load_dwordx4 %7, %16, %10, %14 offen offset:1024 sc1\n2:\n\t"
                 "s_bitcmp1_b32 %15, 2\n\ts_cbranch_scc0 3f\n\tbuffer_load_dwordx4 %4, %9, %10, %13 offen sc1\n\tbuffer_load_dwordx4 %5, %9, %10, %13 offen offset:1024 sc1\n\tbuffer_load_dwordx4 %8, %16, %10, %14 offen offset:2048 sc1\n3:\n\t"
                 "s_waitcnt vmcnt(0)"
                 : "+v"(rp[0]), "+v"(rp[1]), "+v"(rp[2]), "+v"(rp[3]), "+v"(rp[4]), "+v"(rp[5]), "+v"(rp[6]), "+v"(rp[7]), "+v"(rp[8])
                 : "v"(vL), "s"(srS), "s"(sa[12]), "s"(sa[12] + 8192), "s"(sa[12] + 16384), "s"(sb[1]), "s"(miss), "v"(vS) : "scc", "memory");
  }
  const u32 qq = vL >> 8, vt = ((vL >> 4) & 15) * 2304;
  for (int tb = 0; tb < 2; ++tb) {
    const float mean = __uint_as_float(mn[tb]);
    u32 o[4];
    for (int r = 0; r < 4; ++r) {
      const int a = (r >> 1) * 8 + tb * 4 + (r & 1) * 2;
      o[r] = pk2(__uint_as_float(own[a]) + ((bf_lo(rp[tb][r]) + bf_lo(rp[2 + tb][r])) + (bf_lo(rp[4 + tb][r]) + (bf_lo(hp[tb * 4 + r]) + mean))),
                 __uint_as_float(own[a + 1]) + ((bf_hi(rp[tb][r]) + bf_hi(rp[2 + tb][r])) + (bf_hi(rp[4 + tb][r]) + (bf_hi(hp[tb * 4 + r]) + mean))));
    }
    auto s0 = __builtin_amdgcn_permlane16_swap(o[0], o[2], false, false), s1 = __builtin_amdgcn_permlane16_swap(o[1], o[3], false, false);
    const u32x4 ov = {s0[0], s1[0], s0[1], s1[1]};
    asm volatile("buffer_store_dwordx4 %0, %1, %2, %3 offen" ::"v"(ov), "v"(vt + tb * 1152 + (qq & 1) * 32 + (qq >> 1) * 16), "s"(srO), "s"(sb[6]) : "memory");
    u32x2 os;
    for (int r = 0; r < 2; ++r)
      os[r] = pk2(__uint_as_float(sg[tb * 4 + r * 2]) + ((bf_lo(rp[6][tb * 2 + r]) + bf_lo(rp[7][tb * 2 + r])) + (bf_lo(rp[8][tb * 2 + r]) + (bf_lo(rs[12 + tb * 2 + r]) + mean))),
                  __uint_as_float(sg[tb * 4 + r * 2 + 1]) + ((bf_hi(rp[6][tb * 2 + r]) + bf_hi(rp[7][tb * 2 + r])) + (bf_hi(rp[8][tb * 2 + r]) + (bf_hi(rs[12 + tb * 2 + r]) + mean))));
    if (sown) asm volatile("buffer_store_dwordx2 %0, %1, %2, %3 offen" ::"v"(os), "v"(vt + tb * 1152 + qq * 8), "s"(srO), "s"(sb[7]) : "memory");
  }
  }
  }
  asm volatile("LKEND:");
}
}
extern "C" void kernel_launch(void* const* d_in, const int* in_sizes, int n_in, void* d_out, int out_size, void* d_ws, size_t ws_size, hipStream_t stream) {
  (void)hipFuncSetAttribute((const void*)kf::k_kf, hipFuncAttributeMaxDynamicSharedMemorySize, kf::LDS_BYTES);
  kf::k_kf<<<256, 256, kf::LDS_BYTES, stream>>>((const u16*)d_in[0], (const u16*)d_in[1], (const u16*)d_in[2], (const u16*)d_in[4], (const u16*)d_in[5], (const u16*)d_in[6], (char*)d_ws, (u16*)d_out);
}
```
